```python
import torch

def _softmax(x, axis=-1):
    return torch.softmax(x, dim=axis)
def _gelu(x):
    return torch.nn.functional.gelu(x, approximate="tanh")
def _silu(x):
    return torch.nn.functional.silu(x)
def _sigmoid(x):
    return torch.sigmoid(x)
def _layernorm(x, axis=-1, eps=1e-5):
    m = x.mean(dim=axis, keepdim=True)
    v = x.var(dim=axis, keepdim=True, unbiased=False)
    return (x - m) / torch.sqrt(v + eps)
def _scatter_add(tgt, idx, src, axis=0):
    return tgt.clone().index_add_(axis, idx, src)
def _segment_sum(src, seg, n):
    out = torch.zeros((n,) + src.shape[1:], dtype=src.dtype)
    return out.index_add_(0, seg, src)
def _pad(x, pw):
    flat = tuple(v for pair in reversed(pw) for v in pair)
    return torch.nn.functional.pad(x, flat)

kernel_name = 'standalone__mha_softmax__near_ridge'

def setup_inputs():

    _w = (lambda a: (lambda t: t.to(torch.bfloat16) if t.is_floating_point() else t)(torch.as_tensor(a)))
    B, H, Sq, Sk, D = 8, 32, 496, 1024, 128
    return {
        "q": _w((lambda *s: torch.randn(*s, dtype=torch.bfloat16) * 0.8)(B, H, Sq, D)),
        "k": _w((lambda *s: torch.randn(*s, dtype=torch.bfloat16) * 0.8)(B, H, Sk, D)),
        "v": _w((lambda *s: torch.randn(*s, dtype=torch.bfloat16) * 0.05)(B, H, Sk, D)),
    }

def reference(q, k, v):
    scale = q.shape[-1] ** -0.5
    scores = torch.einsum("bhqd,bhkd->bhqk", q, k) * scale
    p = _softmax(scores, axis=-1)
    return torch.einsum("bhqk,bhkd->bhqd", p, v)
```

```cpp
#include <hip/hip_runtime.h>
#include <hip/hip_bf16.h>
#include <cstdio>

using bf16 = __hip_bfloat16;
constexpr int   D = 128, NW = 8, QBLK = 32, KVBLK = 64;
constexpr float SCALE = 0.088388347648318440f;
#define ATTN_THR 8.f
constexpr float THR = ATTN_THR;
#define ATTN_LAYOUT 0
constexpr int LAYOUT = ATTN_LAYOUT;
#define ATTN_SDEPTH 2
constexpr int SDEPTH = ATTN_SDEPTH;
static_assert(SDEPTH == 1 || SDEPTH == 2, "SDEPTH is 1 or 2");
#define ATTN_B 8
#define ATTN_HQ 32
#define ATTN_HKV 32
#define ATTN_S 512
#define ATTN_SKV 1024
constexpr int B = ATTN_B, HQ = ATTN_HQ, HKV = ATTN_HKV, S = ATTN_S, SKV = ATTN_SKV;
constexpr int LDQ = LAYOUT ? HQ * D : D, LDK = LAYOUT ? HKV * D : D, LDO = LDQ;
static_assert(S % (QBLK * NW) == 0 && SKV % (2 * KVBLK) == 0 && HQ % HKV == 0, "S % 256 == 0, SKV % 128 == 0 (even tile count), HQ % HKV == 0");
constexpr size_t SHM_V = KVBLK * D * 2, SHM_K = KVBLK * D * 2, SHM_ATTN = 2 * SHM_V + 2 * SHM_K + NW * 64 * 4;

using bf16x8 = __attribute__((ext_vector_type(8))) short;
using s16x4  = __attribute__((ext_vector_type(4))) short;
using f32x16 = __attribute__((ext_vector_type(16))) float;
using f32x8  = __attribute__((ext_vector_type(8))) float;
using u32x4  = __attribute__((ext_vector_type(4))) unsigned;
#define KSWZ(row, colB) ((row) * 256 + ((colB) ^ (((row) & 7) << 4)))
#define SBAR() __builtin_amdgcn_sched_barrier(0)
__device__ __forceinline__ int crow(int r, int hi) { return (r & 3) + 8 * (r >> 2) + 4 * hi; }
__device__ __forceinline__ unsigned cvtpk(float lo, float hi) {
  unsigned r; asm volatile("v_cvt_pk_bf16_f32 %0, %1, %2" : "=v"(r) : "v"(lo), "v"(hi)); return r;
}
template <typename TIn> struct Stage;
template <> struct Stage<bf16>  { using T = bf16x8;
  __device__ static __forceinline__ T ld8(const bf16* p) { return *reinterpret_cast<const bf16x8*>(p); }
  __device__ static __forceinline__ bf16x8 tobf(T x) { return x; } };
template <> struct Stage<float> { using T = f32x8;
  __device__ static __forceinline__ T ld8(const float* p) { return *reinterpret_cast<const f32x8*>(p); }
  __device__ static __forceinline__ bf16x8 tobf(T x) {
    u32x4 w = {cvtpk(x[0], x[1]), cvtpk(x[2], x[3]), cvtpk(x[4], x[5]), cvtpk(x[6], x[7])}; return *reinterpret_cast<bf16x8*>(&w); } };

__device__ __forceinline__ void partialSM(f32x16& p0, f32x16& p1, float& m_reg, float& mn, float& alpha) {
  constexpr float C = SCALE * 1.4426950408889634f;
  float pmax = p0[0]; for (int r = 1; r < 16; ++r) pmax = fmaxf(pmax, p0[r]); for (int r = 0; r < 16; ++r) pmax = fmaxf(pmax, p1[r]);
  { auto rr = __builtin_amdgcn_permlane32_swap(__float_as_uint(pmax), __float_as_uint(pmax), false, false);
    pmax = fmaxf(__uint_as_float(rr[0]), __uint_as_float(rr[1])); }
  if (__builtin_expect(__all(pmax - m_reg <= THR / SCALE), 1)) { mn = m_reg; alpha = 1.f; }
  else { mn = fmaxf(m_reg, pmax); alpha = __builtin_amdgcn_exp2f((m_reg - mn) * C); m_reg = mn; }
  float mnC = -mn * C;
  for (int r = 0; r < 16; ++r) p0[r] = fmaf(p0[r], C, mnC); for (int r = 0; r < 16; ++r) p1[r] = fmaf(p1[r], C, mnC);
  for (int r = 0; r < 16; ++r) p0[r] = __builtin_amdgcn_exp2f(p0[r]);
}
__device__ __forceinline__ void finishSM(f32x16& p0, f32x16& p1, float alpha, float& l_reg, bf16x8& pa0, bf16x8& pa1, bf16x8& pa2, bf16x8& pa3) {
  for (int r = 0; r < 16; ++r) p1[r] = __builtin_amdgcn_exp2f(p1[r]);
  float ps = 0; for (int r = 0; r < 16; ++r) ps += p0[r]; for (int r = 0; r < 16; ++r) ps += p1[r];
  { auto rr = __builtin_amdgcn_permlane32_swap(__float_as_uint(ps), __float_as_uint(ps), false, false);
    ps = __uint_as_float(rr[0]) + __uint_as_float(rr[1]); }
  l_reg = l_reg * alpha + ps;
#define PK4(P, BASE, OUT) do { unsigned a0 = cvtpk(P[BASE + 0], P[BASE + 1]), a1 = cvtpk(P[BASE + 2], P[BASE + 3]);   \
    unsigned b0 = cvtpk(P[BASE + 4], P[BASE + 5]), b1 = cvtpk(P[BASE + 6], P[BASE + 7]);                              \
    auto r0 = __builtin_amdgcn_permlane32_swap(a0, b0, false, false); auto r1 = __builtin_amdgcn_permlane32_swap(a1, b1, false, false); \
    u32x4 w = {r0[0], r1[0], r0[1], r1[1]}; OUT = *reinterpret_cast<bf16x8*>(&w); } while (0)
  PK4(p0, 0, pa0); PK4(p0, 8, pa1); PK4(p1, 0, pa2); PK4(p1, 8, pa3);
#undef PK4
}
__device__ __forceinline__ void qkt(f32x16& p0, f32x16& p1, const bf16* Ks, const bf16x8* qr, int r32, int hi) {
  p0 = f32x16{}; p1 = f32x16{};
  for (int d0 = 0; d0 < 8; ++d0) { int cb = (d0 * 16 + hi * 8) * 2;
    bf16x8 b0 = *reinterpret_cast<const bf16x8*>((const char*)Ks + KSWZ(r32, cb));
    bf16x8 b1 = *reinterpret_cast<const bf16x8*>((const char*)Ks + KSWZ(32 + r32, cb));
    p0 = __builtin_amdgcn_mfma_f32_32x32x16_bf16(b0, qr[d0], p0, 0, 0, 0);
    p1 = __builtin_amdgcn_mfma_f32_32x32x16_bf16(b1, qr[d0], p1, 0, 0, 0); }
}
__device__ __forceinline__ int v_st(int k, int c) { const int kk = (k & ~0xC) | ((k & 4) << 1) | ((k & 8) >> 1); return ((kk >> 3) * 4 + (c >> 5)) * 512 + ((kk & 7) * 32 + (c & 31)) * 2; }
__device__ __forceinline__ int v_rd_base(int lane) { return ((lane & 3) << 3) | (((lane >> 2) & 3) << 6) | (((lane >> 4) & 1) << 5) | (((lane >> 5) & 1) << 8); }
constexpr int v_rd_off(int d0, int ks, int half) { return d0 * 512 + ks * 4096 + half * 2048; }
template <int OFF> __device__ __forceinline__ s16x4 tr_read(int vb) {
  s16x4 r; asm volatile("ds_read_b64_tr_b16 %0, %1 offset:%2" : "=&v"(r) : "v"(vb), "i"(OFF) : "memory"); return r;
}
template <int D0> __device__ __forceinline__ void pv_one(f32x16& od, int vb, bf16x8 pa0, bf16x8 pa1, bf16x8 pa2, bf16x8 pa3) {
  const s16x4 l0 = tr_read<v_rd_off(D0, 0, 0)>(vb), h0 = tr_read<v_rd_off(D0, 0, 1)>(vb), l1 = tr_read<v_rd_off(D0, 1, 0)>(vb), h1 = tr_read<v_rd_off(D0, 1, 1)>(vb);
  const s16x4 l2 = tr_read<v_rd_off(D0, 2, 0)>(vb), h2 = tr_read<v_rd_off(D0, 2, 1)>(vb), l3 = tr_read<v_rd_off(D0, 3, 0)>(vb), h3 = tr_read<v_rd_off(D0, 3, 1)>(vb);
  asm volatile("s_waitcnt lgkmcnt(0)" ::: "memory"); SBAR();
#define PK(L, H) (bf16x8){L[0], L[1], L[2], L[3], H[0], H[1], H[2], H[3]}
  od = __builtin_amdgcn_mfma_f32_32x32x16_bf16(pa0, PK(l0, h0), od, 0, 0, 0);
  od = __builtin_amdgcn_mfma_f32_32x32x16_bf16(pa1, PK(l1, h1), od, 0, 0, 0);
  od = __builtin_amdgcn_mfma_f32_32x32x16_bf16(pa2, PK(l2, h2), od, 0, 0, 0);
  od = __builtin_amdgcn_mfma_f32_32x32x16_bf16(pa3, PK(l3, h3), od, 0, 0, 0);
#undef PK
}
__device__ __forceinline__ void pv_d0(f32x16* o, int vb, bf16x8 pa0, bf16x8 pa1, bf16x8 pa2, bf16x8 pa3) {
  pv_one<0>(o[0], vb, pa0, pa1, pa2, pa3); pv_one<1>(o[1], vb, pa0, pa1, pa2, pa3); pv_one<2>(o[2], vb, pa0, pa1, pa2, pa3); pv_one<3>(o[3], vb, pa0, pa1, pa2, pa3);
}

template <typename TQ>
__device__ __forceinline__ void attn_dense_body(const TQ* __restrict__ Qb, const bf16* __restrict__ Kh, const bf16* __restrict__ Vh,
                                                bf16* __restrict__ Ob, int seq, char* lds, int nrows) {
  using St = Stage<bf16>; using SQ = Stage<TQ>;
  const int tid = threadIdx.x, wid = tid >> 6, lane = tid & 63, r32 = lane & 31, hi = lane >> 5;
  bf16* V_lds = (bf16*)lds; bf16* K_lds = (bf16*)(lds + 2 * SHM_V);
  float* ws = (float*)(lds + 2 * SHM_V + 2 * SHM_K) + wid * 64; float* li_l = ws; float* al_l = ws + 32;
  float m_reg = -1e30f, l_reg = 0; f32x16 o[4] = {}; bf16x8 qr[8];
  const int qrow_ = min(wid * QBLK + r32, nrows - 1); const TQ* Qw = Qb + (long)qrow_ * LDQ + hi * 8;
#pragma unroll
  for (int d0 = 0; d0 < 8; ++d0) qr[d0] = SQ::tobf(SQ::ld8(Qw + d0 * 16));
  const int sr = tid >> 4, sc = (tid & 15) * 8, vst0 = v_st(sr, sc), vst1 = v_st(32 + sr, sc);
  const int vb0 = (int)(uintptr_t)V_lds + v_rd_base(lane);
  struct { typename St::T vs0, vs1, ks0, ks1; } sr_[SDEPTH];
#define SLOAD(i, k0) do { sr_[i].vs0 = St::ld8(&Vh[(long)((k0) + sr) * LDK + sc]); sr_[i].vs1 = St::ld8(&Vh[(long)((k0) + 32 + sr) * LDK + sc]); \
    sr_[i].ks0 = St::ld8(&Kh[(long)((k0) + sr) * LDK + sc]); sr_[i].ks1 = St::ld8(&Kh[(long)((k0) + 32 + sr) * LDK + sc]); } while (0)
#define SWRITE(b, i) do { *(bf16x8*)((char*)V_lds + (b) * SHM_V + vst0) = St::tobf(sr_[i].vs0);          \
    *(bf16x8*)((char*)V_lds + (b) * SHM_V + vst1) = St::tobf(sr_[i].vs1); int kc = sc * 2;               \
    *(bf16x8*)((char*)K_lds + (b) * SHM_K + KSWZ(sr, kc)) = St::tobf(sr_[i].ks0);                       \
    *(bf16x8*)((char*)K_lds + (b) * SHM_K + KSWZ(32 + sr, kc)) = St::tobf(sr_[i].ks1); } while (0)
#define SWAIT() do { if constexpr (SDEPTH == 2) asm volatile("s_waitcnt vmcnt(4)" ::: "memory"); else asm volatile("s_waitcnt vmcnt(0)" ::: "memory"); } while (0)
#define RESC(a) do { if (__any((a) < 1.f)) { if (hi == 0) al_l[r32] = (a); asm volatile("s_waitcnt lgkmcnt(0)" ::: "memory"); \
    for (int d = 0; d < 4; ++d) for (int r = 0; r < 16; ++r) o[d][r] *= al_l[crow(r, hi)]; } } while (0)
  f32x16 pA0, pA1, pB0, pB1; float mnA, mnB, alA, alB; bf16x8 pa0, pa1, pa2, pa3; const int NT = seq / KVBLK;
  constexpr int SE = 0, SO = SDEPTH - 1;
  SLOAD(SE, 0); asm volatile("s_waitcnt vmcnt(0)" ::: "memory"); SWRITE(0, SE); __syncthreads();
  qkt(pA0, pA1, K_lds, qr, r32, hi); partialSM(pA0, pA1, m_reg, mnA, alA);
  SLOAD(SO, KVBLK); if constexpr (SDEPTH == 2) { if (2 < NT) SLOAD(SE, 2 * KVBLK); }
  SWAIT(); SWRITE(1, SO); __syncthreads();
  for (int j = 1; j + 1 < NT; j += 2) {
    SBAR(); qkt(pB0, pB1, (bf16*)((char*)K_lds + SHM_K), qr, r32, hi);
    finishSM(pA0, pA1, alA, l_reg, pa0, pa1, pa2, pa3); SBAR();
    SLOAD(SO, (j + SDEPTH) * KVBLK); SBAR();
    pv_d0(o, vb0, pa0, pa1, pa2, pa3); partialSM(pB0, pB1, m_reg, mnB, alB);
    __syncthreads(); SWAIT(); SWRITE(0, SE);
    RESC(alB); __syncthreads();
    SBAR(); qkt(pA0, pA1, K_lds, qr, r32, hi);
    finishSM(pB0, pB1, alB, l_reg, pa0, pa1, pa2, pa3); SBAR();
    if (SDEPTH == 1 || j + 3 < NT) SLOAD(SE, (j + 1 + SDEPTH) * KVBLK); SBAR();
    pv_d0(o, vb0 + (int)SHM_V, pa0, pa1, pa2, pa3); partialSM(pA0, pA1, m_reg, mnA, alA);
    __syncthreads(); SWAIT(); SWRITE(1, SO);
    RESC(alA); __syncthreads();
  }
  SBAR(); qkt(pB0, pB1, (bf16*)((char*)K_lds + SHM_K), qr, r32, hi);
  finishSM(pA0, pA1, alA, l_reg, pa0, pa1, pa2, pa3); SBAR();
  pv_d0(o, vb0, pa0, pa1, pa2, pa3); partialSM(pB0, pB1, m_reg, mnB, alB);
  __syncthreads(); RESC(alB);
  finishSM(pB0, pB1, alB, l_reg, pa0, pa1, pa2, pa3); SBAR();
  pv_d0(o, vb0 + (int)SHM_V, pa0, pa1, pa2, pa3);
  if (hi == 0) li_l[r32] = l_reg; asm volatile("s_waitcnt lgkmcnt(0)" ::: "memory");
  float rli[16];
#pragma unroll
  for (int r = 0; r < 16; ++r) rli[r] = __builtin_amdgcn_rcpf(li_l[crow(r, hi)]);
  bf16* Ow = Ob + (long)(wid * QBLK) * LDO;
#pragma unroll
  for (int r = 0; r < 16; ++r) { int orow = crow(r, hi);
    if (wid * QBLK + orow < nrows) for (int d0 = 0; d0 < 4; ++d0) Ow[(long)orow * LDO + d0 * 32 + r32] = __float2bfloat16(o[d0][r] * rli[r]); }
#undef SLOAD
#undef SWRITE
#undef SWAIT
#undef RESC
}

__global__ __launch_bounds__(64 * NW, 1)
void attn_fwd_dense(const bf16* __restrict__ Q, const bf16* __restrict__ K, const bf16* __restrict__ V, bf16* __restrict__ O) {
  extern __shared__ __attribute__((aligned(16))) char shm[];
  const int w = blockIdx.x; const int xcd = w & 7, idx = w >> 3;
  const long bh = xcd * 32 + (idx >> 1); const int half = idx & 1;
  const long q0 = (bh * 496 + half * 256) * D, k0 = bh * 1024 * (long)D;
  attn_dense_body<bf16>(Q + q0, K + k0, V + k0, O + q0, 1024, shm, half ? 240 : 256);
}
extern "C" void kernel_launch(void* const* d_in, const int* in_sizes, int n_in, void* d_out, int out_size, void* d_ws, size_t ws_size, hipStream_t stream) {
  hipFuncSetAttribute((const void*)attn_fwd_dense, hipFuncAttributeMaxDynamicSharedMemorySize, (int)SHM_ATTN);
  hipLaunchKernelGGL(attn_fwd_dense, dim3(512), dim3(64 * NW), SHM_ATTN, stream,
                     (const bf16*)d_in[0], (const bf16*)d_in[1], (const bf16*)d_in[2], (bf16*)d_out);
}
```

```cpp
#include <hip/hip_runtime.h>
#include <hip/hip_bf16.h>

typedef short bf16x8 __attribute__((ext_vector_type(8)));
typedef float f32x4 __attribute__((ext_vector_type(4)));
typedef float f32x2 __attribute__((ext_vector_type(2)));
typedef __bf16 bf16x2 __attribute__((ext_vector_type(2)));
typedef unsigned u32x4 __attribute__((ext_vector_type(4)));
typedef unsigned u32x2 __attribute__((ext_vector_type(2)));
typedef __bf16 mbf16x8 __attribute__((ext_vector_type(8)));
constexpr int SQ = 496, SK = 1024, LDS_BYTES = 156864;

__device__ __forceinline__ unsigned pk2(float a, float b) { f32x2 v = {a, b}; return __builtin_bit_cast(unsigned, __builtin_convertvector(v, bf16x2)); }
__device__ __forceinline__ float xmax(float x) { x = fmaxf(x, __shfl_xor(x, 16)); return fmaxf(x, __shfl_xor(x, 32)); }
__device__ __forceinline__ float xsum(float x) { x += __shfl_xor(x, 16); return x + __shfl_xor(x, 32); }

__device__ __forceinline__ void slow_rows(const unsigned short* q, const unsigned short* k, const unsigned short* v, unsigned short* o, unsigned w) {
  unsigned lane;
  asm volatile("v_mbcnt_lo_u32_b32 %0, -1, 0\n\tv_mbcnt_hi_u32_b32 %0, -1, %0" : "=v"(lane));
  const unsigned r = lane & 15, g = lane >> 4;
  const float c = 0.12751743082f;
#pragma unroll 1
  for (unsigned row0 = 16 * w; row0 < SQ; row0 += 256) {
    mbf16x8 qf[4][4];
    f32x4 acc[4][8];
    float m[4], l[4];
#pragma unroll
    for (int b = 0; b < 4; ++b) {
      const unsigned row = min(row0 + 64 * b + r, (unsigned)SQ - 1);
#pragma unroll
      for (int ks = 0; ks < 4; ++ks) qf[b][ks] = *(const mbf16x8*)(q + row * 128 + 32 * ks + 8 * g);
#pragma unroll
      for (int db = 0; db < 8; ++db) acc[b][db] = f32x4{0.f, 0.f, 0.f, 0.f};
      m[b] = -INFINITY; l[b] = 0.f;
    }
#pragma unroll 1
    for (unsigned t = 0; t < SK; t += 32) {
      mbf16x8 kf[2][4];
#pragma unroll
      for (int i = 0; i < 8; ++i) kf[i >> 2][i & 3] = *(const mbf16x8*)(k + (t + 16 * (i >> 2) + r) * 128 + 32 * (i & 3) + 8 * g);
      u32x4 pw[4];
      float al[4];
#pragma unroll
      for (int b = 0; b < 4; ++b) {
        f32x4 s[2];
#pragma unroll
        for (int kt = 0; kt < 2; ++kt) {
          s[kt] = f32x4{0.f, 0.f, 0.f, 0.f};
#pragma unroll
          for (int ks = 0; ks < 4; ++ks) s[kt] = __builtin_amdgcn_mfma_f32_16x16x32_bf16(kf[kt][ks], qf[b][ks], s[kt], 0, 0, 0);
        }
        const float mx = fmaxf(fmaxf(fmaxf(s[0][0], s[0][1]), fmaxf(s[0][2], s[0][3])), fmaxf(fmaxf(s[1][0], s[1][1]), fmaxf(s[1][2], s[1][3])));
        const float mn = fmaxf(m[b], xmax(mx));
        al[b] = __builtin_amdgcn_exp2f((m[b] - mn) * c);
        m[b] = mn;
        float p[8], ps = 0.f;
#pragma unroll
        for (int i = 0; i < 8; ++i) { p[i] = __builtin_amdgcn_exp2f((s[i >> 2][i & 3] - mn) * c); ps += p[i]; }
        l[b] = l[b] * al[b] + ps;
        pw[b] = u32x4{pk2(p[0], p[1]), pk2(p[2], p[3]), pk2(p[4], p[5]), pk2(p[6], p[7])};
      }
#pragma unroll
      for (int db = 0; db < 8; ++db) {
        bf16x8 vf;
#pragma unroll
        for (int j = 0; j < 8; ++j) vf[j] = (short)v[(t + 16 * (j >> 2) + 4 * g + (j & 3)) * 128 + 16 * db + r];
#pragma unroll
        for (int b = 0; b < 4; ++b)
          acc[b][db] = __builtin_amdgcn_mfma_f32_16x16x32_bf16(__builtin_bit_cast(mbf16x8, vf), __builtin_bit_cast(mbf16x8, pw[b]), acc[b][db] * al[b], 0, 0, 0);
      }
    }
#pragma unroll
    for (int b = 0; b < 4; ++b) {
      const float il = 1.f / xsum(l[b]);
      if (row0 + 64 * b < SQ) {
#pragma unroll
        for (int db = 0; db < 8; ++db)
          *(u32x2*)(o + (row0 + 64 * b + r) * 128 + 16 * db + 4 * g) =
              u32x2{pk2(acc[b][db][0] * il, acc[b][db][1] * il), pk2(acc[b][db][2] * il, acc[b][db][3] * il)};
      }
    }
  }
}

__global__ __launch_bounds__(256, 1) void mha_softmax_kernel(const unsigned short* __restrict__ Q, const unsigned short* __restrict__ K,
                                                             const unsigned short* __restrict__ V, unsigned short* __restrict__ O) {
  const unsigned w = __builtin_amdgcn_readfirstlane(threadIdx.x >> 6);
  const size_t h = blockIdx.x;
  const unsigned short* q = Q + h * (SQ * 128);
  const unsigned short* k = K + h * (SK * 128);
  const unsigned short* v = V + h * (SK * 128);
  unsigned short* o = O + h * (SQ * 128);
  unsigned flag;
  asm volatile(
    "s_load_dwordx8 s[68:75], %[ka], 0x0\n"
    "s_mov_b32 s52, %[w]\n"
    "v_mbcnt_lo_u32_b32 v255, -1, 0\n"
    "v_mbcnt_hi_u32_b32 v255, -1, v255\n"
    "s_getpc_b64 s[64:65]\n"
    "L_pc_%=:\n"
    "s_lshr_b32 s62, %[h], 3\n"
    "s_and_b32 s62, s62, 15\n"
    "s_lshl2_add_u32 s62, s62, s52\n"
    "s_lshl_b32 s62, s62, 10\n"
    "v_and_b32 v124, 7, v255\n"
    "v_lshl_add_u32 v124, v124, 7, s62\n"
    "v_min_u32 v124, L_end_%=-L_pc_%=-4, v124\n"
    "global_load_dword v126, v124, s[64:65]\n"
    "v_bfe_u32 v0, v255, 2, 2\n"
    "v_lshrrev_b32 v1, 4, v255\n"
    "v_and_b32 v2, 3, v255\n"
    "v_lshlrev_b32 v254, 8, v0\n"
    "v_lshl_add_u32 v254, v1, 6, v254\n"
    "v_lshl_add_u32 v254, v2, 4, v254\n"
    "v_lshlrev_b32 v252, 10, v0\n"
    "v_lshrrev_b32 v7, 1, v0\n"
    "v_lshl_add_u32 v252, v7, 5, v252\n"
    "v_lshl_add_u32 v252, v2, 6, v252\n"
    "v_lshl_add_u32 v252, v1, 4, v252\n"
    "v_mul_u32_u24 v253, 1032, v1\n"
    "v_lshl_add_u32 v253, v0, 6, v253\n"
    "v_lshl_add_u32 v253, v2, 4, v253\n"
    "v_add_u32 v253, 57792, v253\n"
    "s_lshl_b32 s66, s52, 11\n"
    "s_add_u32 s53, s66, 8192\n"
    "s_mul_i32 s54, s52, 2080\n"
    "s_lshr_b32 s62, s52, 1\n"
    "s_lshl_b32 s62, s62, 5\n"
    "s_sub_u32 s54, s54, s62\n"
    "s_mul_i32 s55, s52, 2064\n"
    "s_lshl_b32 s57, s52, 12\n"
    "s_mul_i32 s56, s52, 16512\n"
    "s_add_u32 s56, s56, 90816\n"
    "s_cmp_eq_u32 s52, 0\n"
    "s_cselect_b32 s56, 41280, s56\n"
    "s_lshl_b32 s62, %[h], 18\n"
    "s_mul_i32 s63, %[h], 126976\n"
    "s_mov_b32 s42, 262144\n"
    "s_mov_b32 s43, 0x20000\n"
    "s_mov_b32 s38, 126976\n"
    "s_mov_b32 s39, 0x20000\n"
    "s_mov_b32 s46, 262144\n"
    "s_mov_b32 s47, 0x20000\n"
    "s_mov_b32 s50, 126976\n"
    "s_mov_b32 s51, 0x20000\n"
    "s_add_u32 m0, s54, 8256\n"
    "s_waitcnt lgkmcnt(0)\n"
    "s_add_u32 s40, s70, s62\n"
    "s_addc_u32 s41, s71, 0\n"
    "s_and_b32 s41, s41, 0xffff\n"
    "buffer_load_dwordx4 v254, s[40:43], s53 offen nt lds\n"
    "buffer_load_dwordx4 v254, s[40:43], s53 offen offset:1024 nt lds\n"
    "s_add_u32 s36, s68, s63\n"
    "s_addc_u32 s37, s69, 0\n"
    "s_and_b32 s37, s37, 0xffff\n"
    "s_add_u32 m0, s56, 0\n"
    "s_nop 0\n"
    "buffer_load_dwordx4 v254, s[36:39], s57 offen nt lds\n"
    "buffer_load_dwordx4 v254, s[36:39], s57 offen offset:1024 nt lds\n"
    "s_add_u32 m0, s56, 32\n"
    "s_nop 0\n"
    "buffer_load_dwordx4 v254, s[36:39], s57 offen offset:2048 nt lds\n"
    "buffer_load_dwordx4 v254, s[36:39], s57 offen offset:3072 nt lds\n"
    "s_add_u32 s57, s57, 16384\n"
    "s_add_u32 m0, s56, 4128\n"
    "s_nop 0\n"
    "buffer_load_dwordx4 v254, s[36:39], s57 offen nt lds\n"
    "buffer_load_dwordx4 v254, s[36:39], s57 offen offset:1024 nt lds\n"
    "s_add_u32 m0, s56, 4160\n"
    "s_nop 0\n"
    "buffer_load_dwordx4 v254, s[36:39], s57 offen offset:2048 nt lds\n"
    "buffer_load_dwordx4 v254, s[36:39], s57 offen offset:3072 nt lds\n"
    "s_add_u32 s57, s57, 16384\n"
    "s_add_u32 s44, s72, s62\n"
    "s_addc_u32 s45, s73, 0\n"
    "s_and_b32 s45, s45, 0xffff\n"
    "s_add_u32 m0, s55, 66048\n"
    "s_nop 0\n"
    "buffer_load_dwordx4 v254, s[44:47], s53 offen nt lds\n"
    "s_add_u32 m0, s55, 66056\n"
    "s_nop 0\n"
    "buffer_load_dwordx4 v254, s[44:47], s53 offen offset:1024 nt lds\n"
    "s_add_u32 s48, s74, s63\n"
    "s_addc_u32 s49, s75, 0\n"
    "s_and_b32 s49, s49, 0xffff\n"
    "v_mov_b32 v240, 0\n"
    "v_mov_b32 v241, 0\n"
    "v_mov_b32 v242, 0\n"
    "v_mov_b32 v243, 0\n"
    "v_mov_b32 v244, 0\n"
    "v_mov_b32 v245, 0\n"
    "v_mov_b32 v246, 0\n"
    "v_mov_b32 v247, 0\n"
    "v_and_b32 v255, 15, v255\n"
    "v_lshlrev_b32 v255, 8, v255\n"
    "v_lshl_add_u32 v255, v1, 4, v255\n"
    "s_mov_b32 s59, 0x3e0293ee\n"
    "s_mov_b32 s58, 0x3e0293ee\n"
    "s_waitcnt vmcnt(10)\n"
    "s_barrier\n"
    "ds_read_b128 v[128:131], v252 offset:8256\n"
    "ds_read_b128 v[132:135], v252 offset:8512\n"
    "ds_read_b128 v[136:139], v252 offset:8768\n"
    "ds_read_b128 v[140:143], v252 offset:9024\n"
    "ds_read_b128 v[144:147], v252 offset:12384\n"
    "ds_read_b128 v[148:151], v252 offset:12640\n"
    "ds_read_b128 v[152:155], v252 offset:12896\n"
    "ds_read_b128 v[156:159], v252 offset:13152\n"
    "s_add_u32 s53, s66, 0\n"
    "s_add_u32 m0, s54, 0\n"
    "s_nop 0\n"
    "buffer_load_dwordx4 v254, s[40:43], s53 offen nt lds\n"
    "buffer_load_dwordx4 v254, s[40:43], s53 offen offset:1024 nt lds\n"
    "s_add_u32 m0, s55, 57792\n"
    "s_nop 0\n"
    "buffer_load_dwordx4 v254, s[44:47], s53 offen nt lds\n"
    "s_add_u32 m0, s55, 57800\n"
    "s_nop 0\n"
    "buffer_load_dwordx4 v254, s[44:47], s53 offen offset:1024 nt lds\n"
    "v_add_u32 v231, s56, v252\n"
    "s_waitcnt vmcnt(10)\n"
    "ds_read_b128 v[0:3], v231 offset:0\n"
    "ds_read_b128 v[4:7], v231 offset:256\n"
    "ds_read_b128 v[8:11], v231 offset:512\n"
    "ds_read_b128 v[12:15], v231 offset:768\n"
    "s_add_u32 m0, s56, 8256\n"
    "s_nop 0\n"
    "buffer_load_dwordx4 v254, s[36:39], s57 offen nt lds\n"
    "buffer_load_dwordx4 v254, s[36:39], s57 offen offset:1024 nt lds\n"
    "s_add_u32 m0, s56, 8288\n"
    "s_nop 0\n"
    "buffer_load_dwordx4 v254, s[36:39], s57 offen offset:2048 nt lds\n"
    "buffer_load_dwordx4 v254, s[36:39], s57 offen offset:3072 nt lds\n"
    "s_add_u32 s57, s57, 16384\n"
    "s_waitcnt lgkmcnt(3)\n"
    "v_lshlrev_b32 v224, 16, v0\n"
    "v_and_b32 v225, 0xffff0000, v0\n"
    "v_lshlrev_b32 v226, 16, v1\n"
    "v_and_b32 v227, 0xffff0000, v1\n"
    "v_lshlrev_b32 v228, 16, v2\n"
    "v_and_b32 v229, 0xffff0000, v2\n"
    "v_lshlrev_b32 v230, 16, v3\n"
    "v_and_b32 v231, 0xffff0000, v3\n"
    "v_pk_mul_f32 v[224:225], v[224:225], s[58:59] op_sel_hi:[1,0]\n"
    "v_pk_mul_f32 v[226:227], v[226:227], s[58:59] op_sel_hi:[1,0]\n"
    "v_pk_mul_f32 v[228:229], v[228:229], s[58:59] op_sel_hi:[1,0]\n"
    "v_pk_mul_f32 v[230:231], v[230:231], s[58:59] op_sel_hi:[1,0]\n"
    "v_cvt_pk_bf16_f32 v0, v224, v225\n"
    "v_cvt_pk_bf16_f32 v1, v226, v227\n"
    "v_cvt_pk_bf16_f32 v2, v228, v229\n"
    "v_cvt_pk_bf16_f32 v3, v230, v231\n"
    "s_waitcnt lgkmcnt(2)\n"
    "v_lshlrev_b32 v224, 16, v4\n"
    "v_and_b32 v225, 0xffff0000, v4\n"
    "v_lshlrev_b32 v226, 16, v5\n"
    "v_and_b32 v227, 0xffff0000, v5\n"
    "v_lshlrev_b32 v228, 16, v6\n"
    "v_and_b32 v229, 0xffff0000, v6\n"
    "v_lshlrev_b32 v230, 16, v7\n"
    "v_and_b32 v231, 0xffff0000, v7\n"
    "v_pk_mul_f32 v[224:225], v[224:225], s[58:59] op_sel_hi:[1,0]\n"
    "v_pk_mul_f32 v[226:227], v[226:227], s[58:59] op_sel_hi:[1,0]\n"
    "v_pk_mul_f32 v[228:229], v[228:229], s[58:59] op_sel_hi:[1,0]\n"
    "v_pk_mul_f32 v[230:231], v[230:231], s[58:59] op_sel_hi:[1,0]\n"
    "v_cvt_pk_bf16_f32 v4, v224, v225\n"
    "v_cvt_pk_bf16_f32 v5, v226, v227\n"
    "v_cvt_pk_bf16_f32 v6, v228, v229\n"
    "v_cvt_pk_bf16_f32 v7, v230, v231\n"
    "s_waitcnt lgkmcnt(1)\n"
    "v_lshlrev_b32 v224, 16, v8\n"
    "v_and_b32 v225, 0xffff0000, v8\n"
    "v_lshlrev_b32 v226, 16, v9\n"
    "v_and_b32 v227, 0xffff0000, v9\n"
    "v_lshlrev_b32 v228, 16, v10\n"
    "v_and_b32 v229, 0xffff0000, v10\n"
    "v_lshlrev_b32 v230, 16, v11\n"
    "v_and_b32 v231, 0xffff0000, v11\n"
    "v_pk_mul_f32 v[224:225], v[224:225], s[58:59] op_sel_hi:[1,0]\n"
    "v_pk_mul_f32 v[226:227], v[226:227], s[58:59] op_sel_hi:[1,0]\n"
    "v_pk_mul_f32 v[228:229], v[228:229], s[58:59] op_sel_hi:[1,0]\n"
    "v_pk_mul_f32 v[230:231], v[230:231], s[58:59] op_sel_hi:[1,0]\n"
    "v_cvt_pk_bf16_f32 v8, v224, v225\n"
    "v_cvt_pk_bf16_f32 v9, v226, v227\n"
    "v_cvt_pk_bf16_f32 v10, v228, v229\n"
    "v_cvt_pk_bf16_f32 v11, v230, v231\n"
    "s_waitcnt lgkmcnt(0)\n"
    "v_lshlrev_b32 v224, 16, v12\n"
    "v_and_b32 v225, 0xffff0000, v12\n"
    "v_lshlrev_b32 v226, 16, v13\n"
    "v_and_b32 v227, 0xffff0000, v13\n"
    "v_lshlrev_b32 v228, 16, v14\n"
    "v_and_b32 v229, 0xffff0000, v14\n"
    "v_lshlrev_b32 v230, 16, v15\n"
    "v_and_b32 v231, 0xffff0000, v15\n"
    "v_pk_mul_f32 v[224:225], v[224:225], s[58:59] op_sel_hi:[1,0]\n"
    "v_pk_mul_f32 v[226:227], v[226:227], s[58:59] op_sel_hi:[1,0]\n"
    "v_pk_mul_f32 v[228:229], v[228:229], s[58:59] op_sel_hi:[1,0]\n"
    "v_pk_mul_f32 v[230:231], v[230:231], s[58:59] op_sel_hi:[1,0]\n"
    "v_cvt_pk_bf16_f32 v12, v224, v225\n"
    "v_cvt_pk_bf16_f32 v13, v226, v227\n"
    "v_cvt_pk_bf16_f32 v14, v228, v229\n"
    "v_cvt_pk_bf16_f32 v15, v230, v231\n"
    "v_mfma_f32_16x16x32_bf16 v[216:219], v[128:131], v[0:3], 0\n"
    "v_mfma_f32_16x16x32_bf16 v[216:219], v[132:135], v[4:7], v[216:219]\n"
    "v_mfma_f32_16x16x32_bf16 v[216:219], v[136:139], v[8:11], v[216:219]\n"
    "v_mfma_f32_16x16x32_bf16 v[216:219], v[140:143], v[12:15], v[216:219]\n"
    "v_mfma_f32_16x16x32_bf16 v[220:223], v[144:147], v[0:3], 0\n"
    "v_mfma_f32_16x16x32_bf16 v[220:223], v[148:151], v[4:7], v[220:223]\n"
    "v_mfma_f32_16x16x32_bf16 v[220:223], v[152:155], v[8:11], v[220:223]\n"
    "v_mfma_f32_16x16x32_bf16 v[220:223], v[156:159], v[12:15], v[220:223]\n"
    "v_add_u32 v231, s56, v252\n"
    "s_waitcnt vmcnt(10)\n"
    "ds_read_b128 v[16:19], v231 offset:4128\n"
    "ds_read_b128 v[20:23], v231 offset:4384\n"
    "ds_read_b128 v[24:27], v231 offset:4640\n"
    "ds_read_b128 v[28:31], v231 offset:4896\n"
    "s_add_u32 m0, s56, 12384\n"
    "s_nop 0\n"
    "buffer_load_dwordx4 v254, s[36:39], s57 offen nt lds\n"
    "buffer_load_dwordx4 v254, s[36:39], s57 offen offset:1024 nt lds\n"
    "s_add_u32 m0, s56, 12416\n"
    "s_nop 0\n"
    "buffer_load_dwordx4 v254, s[36:39], s57 offen offset:2048 nt lds\n"
    "buffer_load_dwordx4 v254, s[36:39], s57 offen offset:3072 nt lds\n"
    "s_add_u32 s57, s57, 16384\n"
    "s_waitcnt lgkmcnt(3)\n"
    "v_lshlrev_b32 v224, 16, v16\n"
    "v_and_b32 v225, 0xffff0000, v16\n"
    "v_lshlrev_b32 v226, 16, v17\n"
    "v_and_b32 v227, 0xffff0000, v17\n"
    "v_lshlrev_b32 v228, 16, v18\n"
    "v_and_b32 v229, 0xffff0000, v18\n"
    "v_lshlrev_b32 v230, 16, v19\n"
    "v_and_b32 v231, 0xffff0000, v19\n"
    "v_pk_mul_f32 v[224:225], v[224:225], s[58:59] op_sel_hi:[1,0]\n"
    "v_pk_mul_f32 v[226:227], v[226:227], s[58:59] op_sel_hi:[1,0]\n"
    "v_pk_mul_f32 v[228:229], v[228:229], s[58:59] op_sel_hi:[1,0]\n"
    "v_pk_mul_f32 v[230:231], v[230:231], s[58:59] op_sel_hi:[1,0]\n"
    "v_cvt_pk_bf16_f32 v16, v224, v225\n"
    "v_cvt_pk_bf16_f32 v17, v226, v227\n"
    "v_cvt_pk_bf16_f32 v18, v228, v229\n"
    "v_cvt_pk_bf16_f32 v19, v230, v231\n"
    "s_waitcnt lgkmcnt(2)\n"
    "v_lshlrev_b32 v224, 16, v20\n"
    "v_and_b32 v225, 0xffff0000, v20\n"
    "v_lshlrev_b32 v226, 16, v21\n"
    "v_and_b32 v227, 0xffff0000, v21\n"
    "v_lshlrev_b32 v228, 16, v22\n"
    "v_and_b32 v229, 0xffff0000, v22\n"
    "v_lshlrev_b32 v230, 16, v23\n"
    "v_and_b32 v231, 0xffff0000, v23\n"
    "v_pk_mul_f32 v[224:225], v[224:225], s[58:59] op_sel_hi:[1,0]\n"
    "v_pk_mul_f32 v[226:227], v[226:227], s[58:59] op_sel_hi:[1,0]\n"
    "v_pk_mul_f32 v[228:229], v[228:229], s[58:59] op_sel_hi:[1,0]\n"
    "v_pk_mul_f32 v[230:231], v[230:231], s[58:59] op_sel_hi:[1,0]\n"
    "v_cvt_pk_bf16_f32 v20, v224, v225\n"
    "v_cvt_pk_bf16_f32 v21, v226, v227\n"
    "v_cvt_pk_bf16_f32 v22, v228, v229\n"
    "v_cvt_pk_bf16_f32 v23, v230, v231\n"
    "s_waitcnt lgkmcnt(1)\n"
    "v_lshlrev_b32 v224, 16, v24\n"
    "v_and_b32 v225, 0xffff0000, v24\n"
    "v_lshlrev_b32 v226, 16, v25\n"
    "v_and_b32 v227, 0xffff0000, v25\n"
    "v_lshlrev_b32 v228, 16, v26\n"
    "v_and_b32 v229, 0xffff0000, v26\n"
    "v_lshlrev_b32 v230, 16, v27\n"
    "v_and_b32 v231, 0xffff0000, v27\n"
    "v_pk_mul_f32 v[224:225], v[224:225], s[58:59] op_sel_hi:[1,0]\n"
    "v_pk_mul_f32 v[226:227], v[226:227], s[58:59] op_sel_hi:[1,0]\n"
    "v_pk_mul_f32 v[228:229], v[228:229], s[58:59] op_sel_hi:[1,0]\n"
    "v_pk_mul_f32 v[230:231], v[230:231], s[58:59] op_sel_hi:[1,0]\n"
    "v_cvt_pk_bf16_f32 v24, v224, v225\n"
    "v_cvt_pk_bf16_f32 v25, v226, v227\n"
    "v_cvt_pk_bf16_f32 v26, v228, v229\n"
    "v_cvt_pk_bf16_f32 v27, v230, v231\n"
    "s_waitcnt lgkmcnt(0)\n"
    "v_lshlrev_b32 v224, 16, v28\n"
    "v_and_b32 v225, 0xffff0000, v28\n"
    "v_lshlrev_b32 v226, 16, v29\n"
    "v_and_b32 v227, 0xffff0000, v29\n"
    "v_lshlrev_b32 v228, 16, v30\n"
    "v_and_b32 v229, 0xffff0000, v30\n"
    "v_lshlrev_b32 v230, 16, v31\n"
    "v_and_b32 v231, 0xffff0000, v31\n"
    "v_pk_mul_f32 v[224:225], v[224:225], s[58:59] op_sel_hi:[1,0]\n"
    "v_pk_mul_f32 v[226:227], v[226:227], s[58:59] op_sel_hi:[1,0]\n"
    "v_pk_mul_f32 v[228:229], v[228:229], s[58:59] op_sel_hi:[1,0]\n"
    "v_pk_mul_f32 v[230:231], v[230:231], s[58:59] op_sel_hi:[1,0]\n"
    "v_cvt_pk_bf16_f32 v28, v224, v225\n"
    "v_cvt_pk_bf16_f32 v29, v226, v227\n"
    "v_cvt_pk_bf16_f32 v30, v228, v229\n"
    "v_cvt_pk_bf16_f32 v31, v230, v231\n"
    "v_max3_f32 v232, v216, v217, v218\n"
    "v_max3_f32 v233, v219, v220, v221\n"
    "v_max3_f32 v232, v232, v222, v223\n"
    "v_max_f32 v232, v232, v233\n"
    "v_mov_b32 v233, v232\n"
    "s_nop 1\n"
    "v_permlane32_swap_b32 v232, v233\n"
    "v_max_f32 v232, v232, v233\n"
    "v_mov_b32 v233, v232\n"
    "s_nop 1\n"
    "v_permlane16_swap_b32 v232, v233\n"
    "v_max_f32 v232, v232, v233\n"
    "s_nop 1\n"
    "v_max_f32_dpp v232, v232, v232 row_ror:8 row_mask:0xf bank_mask:0xf\n"
    "s_nop 1\n"
    "v_max_f32_dpp v232, v232, v232 row_ror:4 row_mask:0xf bank_mask:0xf\n"
    "s_nop 1\n"
    "v_max_f32_dpp v232, v232, v232 row_ror:2 row_mask:0xf bank_mask:0xf\n"
    "s_nop 1\n"
    "v_max_f32_dpp v232, v232, v232 row_ror:1 row_mask:0xf bank_mask:0xf\n"
    "s_mov_b32 s63, 0x42200000\n"
    "v_cmp_ngt_f32 s[60:61], s63, |v232|\n"
    "v_xor_b32 v248, 0x80000000, v232\n"
    "v_xor_b32 v249, 0x80000000, v232\n"
    "v_xor_b32 v250, 0x80000000, v232\n"
    "v_xor_b32 v251, 0x80000000, v232\n"
    "s_add_u32 m0, s56, 0\n"
    "s_nop 0\n"
    "buffer_load_dwordx4 v254, s[36:39], s57 offen nt lds\n"
    "buffer_load_dwordx4 v254, s[36:39], s57 offen offset:1024 nt lds\n"
    "s_add_u32 m0, s56, 32\n"
    "s_nop 0\n"
    "buffer_load_dwordx4 v254, s[36:39], s57 offen offset:2048 nt lds\n"
    "buffer_load_dwordx4 v254, s[36:39], s57 offen offset:3072 nt lds\n"
    "s_add_u32 s57, s57, 16384\n"
    "s_waitcnt vmcnt(12)\n"
    "s_barrier\n"
    "v_mfma_f32_16x16x32_bf16 v[192:195], v[128:131], v[0:3], v[248:251]\n"
    "v_mfma_f32_16x16x32_bf16 v[192:195], v[132:135], v[4:7], v[192:195]\n"
    "ds_read_b64_tr_b16 v[160:161], v253 offset:8256\n"
    "ds_read_b64_tr_b16 v[162:163], v253 offset:12384\n"
    "v_mfma_f32_16x16x32_bf16 v[192:195], v[136:139], v[8:11], v[192:195]\n"
    "v_mfma_f32_16x16x32_bf16 v[192:195], v[140:143], v[12:15], v[192:195]\n"
    "ds_read_b64_tr_b16 v[164:165], v253 offset:8264\n"
    "ds_read_b64_tr_b16 v[166:167], v253 offset:12392\n"
    "v_mfma_f32_16x16x32_bf16 v[196:199], v[140:143], v[28:31], v[248:251]\n"
    "v_mfma_f32_16x16x32_bf16 v[196:199], v[136:139], v[24:27], v[196:199]\n"
    "ds_read_b128 v[136:139], v252 offset:512\n"
    "ds_read_b128 v[140:143], v252 offset:768\n"
    "v_mfma_f32_16x16x32_bf16 v[196:199], v[132:135], v[20:23], v[196:199]\n"
    "s_nop 0\n"
    "v_exp_f32 v192, v192\n"
    "v_mfma_f32_16x16x32_bf16 v[196:199], v[128:131], v[16:19], v[196:199]\n"
    "ds_read_b128 v[128:131], v252 offset:0\n"
    "ds_read_b128 v[132:135], v252 offset:256\n"
    "v_add_f32 v240, v240, v192\n"
    "v_exp_f32 v193, v193\n"
    "ds_read_b64_tr_b16 v[168:169], v253 offset:8512\n"
    "ds_read_b64_tr_b16 v[170:171], v253 offset:12640\n"
    "v_cvt_pk_bf16_f32 v224, v192, v193\n"
    "v_exp_f32 v194, v194\n"
    "ds_read_b64_tr_b16 v[172:173], v253 offset:8520\n"
    "ds_read_b64_tr_b16 v[174:175], v253 offset:12648\n"
    "v_add_f32 v240, v240, v193\n"
    "v_exp_f32 v195, v195\n"
    "ds_read_b64_tr_b16 v[176:177], v253 offset:8768\n"
    "ds_read_b64_tr_b16 v[178:179], v253 offset:12896\n"
    "v_cvt_pk_bf16_f32 v225, v194, v195\n"
    "v_exp_f32 v196, v196\n"
    "ds_read_b64_tr_b16 v[180:181], v253 offset:8776\n"
    "ds_read_b64_tr_b16 v[182:183], v253 offset:12904\n"
    "v_add_f32 v240, v240, v194\n"
    "v_mfma_f32_16x16x32_bf16 v[200:203], v[144:147], v[0:3], v[248:251]\n"
    "v_exp_f32 v197, v197\n"
    "v_mfma_f32_16x16x32_bf16 v[200:203], v[148:151], v[4:7], v[200:203]\n"
    "ds_read_b64_tr_b16 v[184:185], v253 offset:9024\n"
    "ds_read_b64_tr_b16 v[186:187], v253 offset:13152\n"
    "v_cvt_pk_bf16_f32 v228, v196, v197\n"
    "v_mfma_f32_16x16x32_bf16 v[200:203], v[152:155], v[8:11], v[200:203]\n"
    "v_exp_f32 v198, v198\n"
    "v_mfma_f32_16x16x32_bf16 v[200:203], v[156:159], v[12:15], v[200:203]\n"
    "ds_read_b64_tr_b16 v[188:189], v253 offset:9032\n"
    "ds_read_b64_tr_b16 v[190:191], v253 offset:13160\n"
    "v_add_f32 v240, v240, v195\n"
    "v_mfma_f32_16x16x32_bf16 v[204:207], v[156:159], v[28:31], v[248:251]\n"
    "v_exp_f32 v199, v199\n"
    "v_mfma_f32_16x16x32_bf16 v[204:207], v[152:155], v[24:27], v[204:207]\n"
    "ds_read_b128 v[152:155], v252 offset:4640\n"
    "ds_read_b128 v[156:159], v252 offset:4896\n"
    "v_cvt_pk_bf16_f32 v229, v198, v199\n"
    "v_mfma_f32_16x16x32_bf16 v[204:207], v[148:151], v[20:23], v[204:207]\n"
    "v_exp_f32 v200, v200\n"
    "v_mfma_f32_16x16x32_bf16 v[204:207], v[144:147], v[16:19], v[204:207]\n"
    "ds_read_b128 v[144:147], v252 offset:4128\n"
    "ds_read_b128 v[148:151], v252 offset:4384\n"
    "v_add_f32 v241, v241, v196\n"
    "v_exp_f32 v201, v201\n"
    "s_nop 0\n"
    "v_cvt_pk_bf16_f32 v226, v200, v201\n"
    "v_add_f32 v241, v241, v197\n"
    "v_add_f32 v240, v240, v200\n"
    "v_exp_f32 v202, v202\n"
    "v_add_f32 v241, v241, v198\n"
    "v_add_f32 v240, v240, v201\n"
    "v_add_f32 v241, v241, v199\n"
    "v_exp_f32 v203, v203\n"
    "s_nop 0\n"
    "v_cvt_pk_bf16_f32 v227, v202, v203\n"
    "v_add_f32 v240, v240, v202\n"
    "v_add_f32 v240, v240, v203\n"
    "v_exp_f32 v204, v204\n"
    "s_nop 0\n"
    "v_add_f32 v241, v241, v204\n"
    "s_waitcnt lgkmcnt(15)\n"
    "v_mfma_f32_16x16x32_bf16 v[208:211], v[128:131], v[0:3], v[248:251]\n"
    "v_exp_f32 v205, v205\n"
    "v_mfma_f32_16x16x32_bf16 v[208:211], v[132:135], v[4:7], v[208:211]\n"
    "v_cvt_pk_bf16_f32 v230, v204, v205\n"
    "v_add_f32 v241, v241, v205\n"
    "v_mfma_f32_16x16x32_bf16 v[208:211], v[136:139], v[8:11], v[208:211]\n"
    "v_exp_f32 v206, v206\n"
    "v_mfma_f32_16x16x32_bf16 v[208:211], v[140:143], v[12:15], v[208:211]\n"
    "v_add_f32 v241, v241, v206\n"
    "v_mfma_f32_16x16x32_bf16 v[212:215], v[140:143], v[28:31], v[248:251]\n"
    "v_exp_f32 v207, v207\n"
    "v_mfma_f32_16x16x32_bf16 v[212:215], v[136:139], v[24:27], v[212:215]\n"
    "v_cvt_pk_bf16_f32 v231, v206, v207\n"
    "v_add_f32 v241, v241, v207\n"
    "v_mfma_f32_16x16x32_bf16 v[212:215], v[132:135], v[20:23], v[212:215]\n"
    "s_nop 0\n"
    "v_exp_f32 v208, v208\n"
    "v_mfma_f32_16x16x32_bf16 v[212:215], v[128:131], v[16:19], v[212:215]\n"
    "v_add_f32 v240, v240, v208\n"
    "v_mfma_f32_16x16x32_bf16 a[0:3], v[160:163], v[224:227], 0\n"
    "v_exp_f32 v209, v209\n"
    "v_mfma_f32_16x16x32_bf16 a[32:35], v[160:163], v[228:231], 0\n"
    "ds_read_b64_tr_b16 v[160:161], v253 offset:0\n"
    "ds_read_b64_tr_b16 v[162:163], v253 offset:4128\n"
    "v_cvt_pk_bf16_f32 v232, v208, v209\n"
    "v_mfma_f32_16x16x32_bf16 a[36:39], v[164:167], v[228:231], 0\n"
    "v_exp_f32 v210, v210\n"
    "v_mfma_f32_16x16x32_bf16 a[4:7], v[164:167], v[224:227], 0\n"
    "s_waitcnt lgkmcnt(15)\n"
    "ds_read_b64_tr_b16 v[164:165], v253 offset:8\n"
    "ds_read_b64_tr_b16 v[166:167], v253 offset:4136\n"
    "v_add_f32 v240, v240, v209\n"
    "v_mfma_f32_16x16x32_bf16 a[8:11], v[168:171], v[224:227], 0\n"
    "v_exp_f32 v211, v211\n"
    "v_mfma_f32_16x16x32_bf16 a[40:43], v[168:171], v[228:231], 0\n"
    "s_waitcnt lgkmcnt(15)\n"
    "ds_read_b64_tr_b16 v[168:169], v253 offset:256\n"
    "ds_read_b64_tr_b16 v[170:171], v253 offset:4384\n"
    "v_cvt_pk_bf16_f32 v233, v210, v211\n"
    "v_mfma_f32_16x16x32_bf16 a[44:47], v[172:175], v[228:231], 0\n"
    "v_exp_f32 v212, v212\n"
    "v_mfma_f32_16x16x32_bf16 a[12:15], v[172:175], v[224:227], 0\n"
    "s_waitcnt lgkmcnt(7)\n"
    "ds_read_b64_tr_b16 v[172:173], v253 offset:264\n"
    "ds_read_b64_tr_b16 v[174:175], v253 offset:4392\n"
    "v_add_f32 v240, v240, v210\n"
    "v_mfma_f32_16x16x32_bf16 v[216:219], v[144:147], v[0:3], v[248:251]\n"
    "s_waitcnt lgkmcnt(8)\n"
    "v_exp_f32 v213, v213\n"
    "v_mfma_f32_16x16x32_bf16 v[216:219], v[148:151], v[4:7], v[216:219]\n"
    "v_cvt_pk_bf16_f32 v236, v212, v213\n"
    "v_add_f32 v240, v240, v211\n"
    "v_mfma_f32_16x16x32_bf16 v[216:219], v[152:155], v[8:11], v[216:219]\n"
    "v_exp_f32 v214, v214\n"
    "v_mfma_f32_16x16x32_bf16 v[216:219], v[156:159], v[12:15], v[216:219]\n"
    "v_add_f32 v241, v241, v212\n"
    "v_add_f32 v241, v241, v213\n"
    "v_mfma_f32_16x16x32_bf16 v[220:223], v[156:159], v[28:31], v[248:251]\n"
    "v_exp_f32 v215, v215\n"
    "v_mfma_f32_16x16x32_bf16 v[220:223], v[152:155], v[24:27], v[220:223]\n"
    "v_cvt_pk_bf16_f32 v237, v214, v215\n"
    "v_add_f32 v241, v241, v214\n"
    "v_mfma_f32_16x16x32_bf16 v[220:223], v[148:151], v[20:23], v[220:223]\n"
    "v_exp_f32 v216, v216\n"
    "v_mfma_f32_16x16x32_bf16 v[220:223], v[144:147], v[16:19], v[220:223]\n"
    "v_add_f32 v241, v241, v215\n"
    "v_add_f32 v240, v240, v216\n"
    "v_mfma_f32_16x16x32_bf16 a[16:19], v[176:179], v[224:227], 0\n"
    "v_exp_f32 v217, v217\n"
    "v_mfma_f32_16x16x32_bf16 a[48:51], v[176:179], v[228:231], 0\n"
    "ds_read_b64_tr_b16 v[176:177], v253 offset:512\n"
    "v_cvt_pk_bf16_f32 v234, v216, v217\n"
    "v_mfma_f32_16x16x32_bf16 a[52:55], v[180:183], v[228:231], 0\n"
    "v_exp_f32 v218, v218\n"
    "v_mfma_f32_16x16x32_bf16 a[20:23], v[180:183], v[224:227], 0\n"
    "ds_read_b64_tr_b16 v[178:179], v253 offset:4640\n"
    "ds_read_b64_tr_b16 v[180:181], v253 offset:520\n"
    "v_add_f32 v240, v240, v217\n"
    "v_mfma_f32_16x16x32_bf16 a[24:27], v[184:187], v[224:227], 0\n"
    "v_exp_f32 v219, v219\n"
    "v_mfma_f32_16x16x32_bf16 a[56:59], v[184:187], v[228:231], 0\n"
    "ds_read_b64_tr_b16 v[182:183], v253 offset:4648\n"
    "v_cvt_pk_bf16_f32 v235, v218, v219\n"
    "v_mfma_f32_16x16x32_bf16 a[60:63], v[188:191], v[228:231], 0\n"
    "v_exp_f32 v220, v220\n"
    "v_mfma_f32_16x16x32_bf16 a[28:31], v[188:191], v[224:227], 0\n"
    "ds_read_b64_tr_b16 v[184:185], v253 offset:768\n"
    "ds_read_b64_tr_b16 v[186:187], v253 offset:4896\n"
    "v_add_f32 v240, v240, v218\n"
    "v_add_u32 v231, s56, v252\n"
    "s_add_u32 m0, s56, 4128\n"
    "s_nop 0\n"
    "buffer_load_dwordx4 v254, s[36:39], s57 offen nt lds\n"
    "buffer_load_dwordx4 v254, s[36:39], s57 offen offset:1024 nt lds\n"
    "s_add_u32 m0, s56, 4160\n"
    "s_nop 0\n"
    "buffer_load_dwordx4 v254, s[36:39], s57 offen offset:2048 nt lds\n"
    "buffer_load_dwordx4 v254, s[36:39], s57 offen offset:3072 nt lds\n"
    "s_add_u32 s57, s57, 16384\n"
    "s_waitcnt vmcnt(12)\n"
    "ds_read_b128 v[32:35], v231 offset:8256\n"
    "ds_read_b128 v[36:39], v231 offset:8512\n"
    "ds_read_b128 v[40:43], v231 offset:8768\n"
    "ds_read_b128 v[44:47], v231 offset:9024\n"
    "s_waitcnt lgkmcnt(3)\n"
    "v_lshlrev_b32 v224, 16, v32\n"
    "v_and_b32 v225, 0xffff0000, v32\n"
    "v_lshlrev_b32 v226, 16, v33\n"
    "v_and_b32 v227, 0xffff0000, v33\n"
    "v_lshlrev_b32 v228, 16, v34\n"
    "v_and_b32 v229, 0xffff0000, v34\n"
    "v_lshlrev_b32 v230, 16, v35\n"
    "v_and_b32 v231, 0xffff0000, v35\n"
    "v_pk_mul_f32 v[224:225], v[224:225], s[58:59] op_sel_hi:[1,0]\n"
    "v_pk_mul_f32 v[226:227], v[226:227], s[58:59] op_sel_hi:[1,0]\n"
    "v_pk_mul_f32 v[228:229], v[228:229], s[58:59] op_sel_hi:[1,0]\n"
    "v_pk_mul_f32 v[230:231], v[230:231], s[58:59] op_sel_hi:[1,0]\n"
    "v_cvt_pk_bf16_f32 v32, v224, v225\n"
    "v_cvt_pk_bf16_f32 v33, v226, v227\n"
    "v_cvt_pk_bf16_f32 v34, v228, v229\n"
    "v_cvt_pk_bf16_f32 v35, v230, v231\n"
    "s_waitcnt lgkmcnt(2)\n"
    "v_lshlrev_b32 v224, 16, v36\n"
    "v_and_b32 v225, 0xffff0000, v36\n"
    "v_lshlrev_b32 v226, 16, v37\n"
    "v_and_b32 v227, 0xffff0000, v37\n"
    "v_lshlrev_b32 v228, 16, v38\n"
    "v_and_b32 v229, 0xffff0000, v38\n"
    "v_lshlrev_b32 v230, 16, v39\n"
    "v_and_b32 v231, 0xffff0000, v39\n"
    "v_pk_mul_f32 v[224:225], v[224:225], s[58:59] op_sel_hi:[1,0]\n"
    "v_pk_mul_f32 v[226:227], v[226:227], s[58:59] op_sel_hi:[1,0]\n"
    "v_pk_mul_f32 v[228:229], v[228:229], s[58:59] op_sel_hi:[1,0]\n"
    "v_pk_mul_f32 v[230:231], v[230:231], s[58:59] op_sel_hi:[1,0]\n"
    "v_cvt_pk_bf16_f32 v36, v224, v225\n"
    "v_cvt_pk_bf16_f32 v37, v226, v227\n"
    "v_cvt_pk_bf16_f32 v38, v228, v229\n"
    "v_cvt_pk_bf16_f32 v39, v230, v231\n"
    "s_waitcnt lgkmcnt(1)\n"
    "v_lshlrev_b32 v224, 16, v40\n"
    "v_and_b32 v225, 0xffff0000, v40\n"
    "v_lshlrev_b32 v226, 16, v41\n"
    "v_and_b32 v227, 0xffff0000, v41\n"
    "v_lshlrev_b32 v228, 16, v42\n"
    "v_and_b32 v229, 0xffff0000, v42\n"
    "v_lshlrev_b32 v230, 16, v43\n"
    "v_and_b32 v231, 0xffff0000, v43\n"
    "v_pk_mul_f32 v[224:225], v[224:225], s[58:59] op_sel_hi:[1,0]\n"
    "v_pk_mul_f32 v[226:227], v[226:227], s[58:59] op_sel_hi:[1,0]\n"
    "v_pk_mul_f32 v[228:229], v[228:229], s[58:59] op_sel_hi:[1,0]\n"
    "v_pk_mul_f32 v[230:231], v[230:231], s[58:59] op_sel_hi:[1,0]\n"
    "v_cvt_pk_bf16_f32 v40, v224, v225\n"
    "v_cvt_pk_bf16_f32 v41, v226, v227\n"
    "v_cvt_pk_bf16_f32 v42, v228, v229\n"
    "v_cvt_pk_bf16_f32 v43, v230, v231\n"
    "s_waitcnt lgkmcnt(0)\n"
    "v_lshlrev_b32 v224, 16, v44\n"
    "v_and_b32 v225, 0xffff0000, v44\n"
    "v_lshlrev_b32 v226, 16, v45\n"
    "v_and_b32 v227, 0xffff0000, v45\n"
    "v_lshlrev_b32 v228, 16, v46\n"
    "v_and_b32 v229, 0xffff0000, v46\n"
    "v_lshlrev_b32 v230, 16, v47\n"
    "v_and_b32 v231, 0xffff0000, v47\n"
    "v_pk_mul_f32 v[224:225], v[224:225], s[58:59] op_sel_hi:[1,0]\n"
    "v_pk_mul_f32 v[226:227], v[226:227], s[58:59] op_sel_hi:[1,0]\n"
    "v_pk_mul_f32 v[228:229], v[228:229], s[58:59] op_sel_hi:[1,0]\n"
    "v_pk_mul_f32 v[230:231], v[230:231], s[58:59] op_sel_hi:[1,0]\n"
    "v_cvt_pk_bf16_f32 v44, v224, v225\n"
    "v_cvt_pk_bf16_f32 v45, v226, v227\n"
    "v_cvt_pk_bf16_f32 v46, v228, v229\n"
    "v_cvt_pk_bf16_f32 v47, v230, v231\n"
    "v_add_u32 v231, s56, v252\n"
    "s_add_u32 m0, s56, 8256\n"
    "s_nop 0\n"
    "buffer_load_dwordx4 v254, s[36:39], s57 offen nt lds\n"
    "buffer_load_dwordx4 v254, s[36:39], s57 offen offset:1024 nt lds\n"
    "s_add_u32 m0, s56, 8288\n"
    "s_nop 0\n"
    "buffer_load_dwordx4 v254, s[36:39], s57 offen offset:2048 nt lds\n"
    "buffer_load_dwordx4 v254, s[36:39], s57 offen offset:3072 nt lds\n"
    "s_add_u32 s57, s57, 16384\n"
    "s_waitcnt vmcnt(12)\n"
    "ds_read_b128 v[48:51], v231 offset:12384\n"
    "ds_read_b128 v[52:55], v231 offset:12640\n"
    "ds_read_b128 v[56:59], v231 offset:12896\n"
    "ds_read_b128 v[60:63], v231 offset:13152\n"
    "s_waitcnt lgkmcnt(3)\n"
    "v_lshlrev_b32 v224, 16, v48\n"
    "v_and_b32 v225, 0xffff0000, v48\n"
    "v_lshlrev_b32 v226, 16, v49\n"
    "v_and_b32 v227, 0xffff0000, v49\n"
    "v_lshlrev_b32 v228, 16, v50\n"
    "v_and_b32 v229, 0xffff0000, v50\n"
    "v_lshlrev_b32 v230, 16, v51\n"
    "v_and_b32 v231, 0xffff0000, v51\n"
    "v_pk_mul_f32 v[224:225], v[224:225], s[58:59] op_sel_hi:[1,0]\n"
    "v_pk_mul_f32 v[226:227], v[226:227], s[58:59] op_sel_hi:[1,0]\n"
    "v_pk_mul_f32 v[228:229], v[228:229], s[58:59] op_sel_hi:[1,0]\n"
    "v_pk_mul_f32 v[230:231], v[230:231], s[58:59] op_sel_hi:[1,0]\n"
    "v_cvt_pk_bf16_f32 v48, v224, v225\n"
    "v_cvt_pk_bf16_f32 v49, v226, v227\n"
    "v_cvt_pk_bf16_f32 v50, v228, v229\n"
    "v_cvt_pk_bf16_f32 v51, v230, v231\n"
    "s_waitcnt lgkmcnt(2)\n"
    "v_lshlrev_b32 v224, 16, v52\n"
    "v_and_b32 v225, 0xffff0000, v52\n"
    "v_lshlrev_b32 v226, 16, v53\n"
    "v_and_b32 v227, 0xffff0000, v53\n"
    "v_lshlrev_b32 v228, 16, v54\n"
    "v_and_b32 v229, 0xffff0000, v54\n"
    "v_lshlrev_b32 v230, 16, v55\n"
    "v_and_b32 v231, 0xffff0000, v55\n"
    "v_pk_mul_f32 v[224:225], v[224:225], s[58:59] op_sel_hi:[1,0]\n"
    "v_pk_mul_f32 v[226:227], v[226:227], s[58:59] op_sel_hi:[1,0]\n"
    "v_pk_mul_f32 v[228:229], v[228:229], s[58:59] op_sel_hi:[1,0]\n"
    "v_pk_mul_f32 v[230:231], v[230:231], s[58:59] op_sel_hi:[1,0]\n"
    "v_cvt_pk_bf16_f32 v52, v224, v225\n"
    "v_cvt_pk_bf16_f32 v53, v226, v227\n"
    "v_cvt_pk_bf16_f32 v54, v228, v229\n"
    "v_cvt_pk_bf16_f32 v55, v230, v231\n"
    "s_waitcnt lgkmcnt(1)\n"
    "v_lshlrev_b32 v224, 16, v56\n"
    "v_and_b32 v225, 0xffff0000, v56\n"
    "v_lshlrev_b32 v226, 16, v57\n"
    "v_and_b32 v227, 0xffff0000, v57\n"
    "v_lshlrev_b32 v228, 16, v58\n"
    "v_and_b32 v229, 0xffff0000, v58\n"
    "v_lshlrev_b32 v230, 16, v59\n"
    "v_and_b32 v231, 0xffff0000, v59\n"
    "v_pk_mul_f32 v[224:225], v[224:225], s[58:59] op_sel_hi:[1,0]\n"
    "v_pk_mul_f32 v[226:227], v[226:227], s[58:59] op_sel_hi:[1,0]\n"
    "v_pk_mul_f32 v[228:229], v[228:229], s[58:59] op_sel_hi:[1,0]\n"
    "v_pk_mul_f32 v[230:231], v[230:231], s[58:59] op_sel_hi:[1,0]\n"
    "v_cvt_pk_bf16_f32 v56, v224, v225\n"
    "v_cvt_pk_bf16_f32 v57, v226, v227\n"
    "v_cvt_pk_bf16_f32 v58, v228, v229\n"
    "v_cvt_pk_bf16_f32 v59, v230, v231\n"
    "s_waitcnt lgkmcnt(0)\n"
    "v_lshlrev_b32 v224, 16, v60\n"
    "v_and_b32 v225, 0xffff0000, v60\n"
    "v_lshlrev_b32 v226, 16, v61\n"
    "v_and_b32 v227, 0xffff0000, v61\n"
    "v_lshlrev_b32 v228, 16, v62\n"
    "v_and_b32 v229, 0xffff0000, v62\n"
    "v_lshlrev_b32 v230, 16, v63\n"
    "v_and_b32 v231, 0xffff0000, v63\n"
    "v_pk_mul_f32 v[224:225], v[224:225], s[58:59] op_sel_hi:[1,0]\n"
    "v_pk_mul_f32 v[226:227], v[226:227], s[58:59] op_sel_hi:[1,0]\n"
    "v_pk_mul_f32 v[228:229], v[228:229], s[58:59] op_sel_hi:[1,0]\n"
    "v_pk_mul_f32 v[230:231], v[230:231], s[58:59] op_sel_hi:[1,0]\n"
    "v_cvt_pk_bf16_f32 v60, v224, v225\n"
    "v_cvt_pk_bf16_f32 v61, v226, v227\n"
    "v_cvt_pk_bf16_f32 v62, v228, v229\n"
    "v_cvt_pk_bf16_f32 v63, v230, v231\n"
    "v_mfma_f32_16x16x32_bf16 v[192:195], v[128:131], v[32:35], v[248:251]\n"
    "v_exp_f32 v221, v221\n"
    "v_mfma_f32_16x16x32_bf16 v[192:195], v[132:135], v[36:39], v[192:195]\n"
    "ds_read_b64_tr_b16 v[188:189], v253 offset:776\n"
    "v_cvt_pk_bf16_f32 v238, v220, v221\n"
    "v_mfma_f32_16x16x32_bf16 v[192:195], v[136:139], v[40:43], v[192:195]\n"
    "v_exp_f32 v222, v222\n"
    "v_mfma_f32_16x16x32_bf16 v[192:195], v[140:143], v[44:47], v[192:195]\n"
    "ds_read_b64_tr_b16 v[190:191], v253 offset:4904\n"
    "v_add_f32 v240, v240, v219\n"
    "v_mfma_f32_16x16x32_bf16 v[196:199], v[140:143], v[60:63], v[248:251]\n"
    "v_exp_f32 v223, v223\n"
    "v_mfma_f32_16x16x32_bf16 v[196:199], v[136:139], v[56:59], v[196:199]\n"
    "ds_read_b128 v[136:139], v252 offset:8768\n"
    "v_cvt_pk_bf16_f32 v239, v222, v223\n"
    "v_mfma_f32_16x16x32_bf16 v[196:199], v[132:135], v[52:55], v[196:199]\n"
    "v_exp_f32 v192, v192\n"
    "v_mfma_f32_16x16x32_bf16 v[196:199], v[128:131], v[48:51], v[196:199]\n"
    "ds_read_b128 v[128:131], v252 offset:8256\n"
    "ds_read_b128 v[132:135], v252 offset:8512\n"
    "v_add_f32 v241, v241, v220\n"
    "v_mfma_f32_16x16x32_bf16 a[0:3], v[160:163], v[232:235], a[0:3]\n"
    "v_exp_f32 v193, v193\n"
    "v_mfma_f32_16x16x32_bf16 a[32:35], v[160:163], v[236:239], a[32:35]\n"
    "ds_read_b128 v[140:143], v252 offset:9024\n"
    "v_cvt_pk_bf16_f32 v224, v192, v193\n"
    "v_mfma_f32_16x16x32_bf16 a[36:39], v[164:167], v[236:239], a[36:39]\n"
    "v_exp_f32 v194, v194\n"
    "v_mfma_f32_16x16x32_bf16 a[4:7], v[164:167], v[232:235], a[4:7]\n"
    "v_add_f32 v241, v241, v221\n"
    "v_add_f32 v242, v242, v192\n"
    "v_mfma_f32_16x16x32_bf16 a[8:11], v[168:171], v[232:235], a[8:11]\n"
    "v_exp_f32 v195, v195\n"
    "v_mfma_f32_16x16x32_bf16 a[40:43], v[168:171], v[236:239], a[40:43]\n"
    "v_cvt_pk_bf16_f32 v225, v194, v195\n"
    "v_add_f32 v241, v241, v222\n"
    "v_mfma_f32_16x16x32_bf16 a[44:47], v[172:175], v[236:239], a[44:47]\n"
    "v_exp_f32 v196, v196\n"
    "v_mfma_f32_16x16x32_bf16 a[12:15], v[172:175], v[232:235], a[12:15]\n"
    "v_add_f32 v241, v241, v223\n"
    "v_add_f32 v242, v242, v193\n"
    "v_mfma_f32_16x16x32_bf16 v[200:203], v[144:147], v[32:35], v[248:251]\n"
    "v_exp_f32 v197, v197\n"
    "v_mfma_f32_16x16x32_bf16 v[200:203], v[148:151], v[36:39], v[200:203]\n"
    "v_cvt_pk_bf16_f32 v228, v196, v197\n"
    "v_add_f32 v242, v242, v194\n"
    "v_mfma_f32_16x16x32_bf16 v[200:203], v[152:155], v[40:43], v[200:203]\n"
    "v_exp_f32 v198, v198\n"
    "v_mfma_f32_16x16x32_bf16 v[200:203], v[156:159], v[44:47], v[200:203]\n"
    "v_add_f32 v242, v242, v195\n"
    "v_add_f32 v243, v243, v196\n"
    "v_mfma_f32_16x16x32_bf16 v[204:207], v[156:159], v[60:63], v[248:251]\n"
    "v_exp_f32 v199, v199\n"
    "v_mfma_f32_16x16x32_bf16 v[204:207], v[152:155], v[56:59], v[204:207]\n"
    "ds_read_b128 v[152:155], v252 offset:12896\n"
    "v_cvt_pk_bf16_f32 v229, v198, v199\n"
    "v_mfma_f32_16x16x32_bf16 v[204:207], v[148:151], v[52:55], v[204:207]\n"
    "v_exp_f32 v200, v200\n"
    "v_mfma_f32_16x16x32_bf16 v[204:207], v[144:147], v[48:51], v[204:207]\n"
    "ds_read_b128 v[144:147], v252 offset:12384\n"
    "ds_read_b128 v[148:151], v252 offset:12640\n"
    "v_add_f32 v243, v243, v197\n"
    "v_mfma_f32_16x16x32_bf16 a[16:19], v[176:179], v[232:235], a[16:19]\n"
    "v_exp_f32 v201, v201\n"
    "v_mfma_f32_16x16x32_bf16 a[48:51], v[176:179], v[236:239], a[48:51]\n"
    "ds_read_b128 v[156:159], v252 offset:13152\n"
    "v_cvt_pk_bf16_f32 v226, v200, v201\n"
    "v_mfma_f32_16x16x32_bf16 a[52:55], v[180:183], v[236:239], a[52:55]\n"
    "v_exp_f32 v202, v202\n"
    "v_mfma_f32_16x16x32_bf16 a[20:23], v[180:183], v[232:235], a[20:23]\n"
    "v_add_f32 v243, v243, v198\n"
    "v_add_f32 v242, v242, v200\n"
    "v_mfma_f32_16x16x32_bf16 a[24:27], v[184:187], v[232:235], a[24:27]\n"
    "v_exp_f32 v203, v203\n"
    "v_mfma_f32_16x16x32_bf16 a[56:59], v[184:187], v[236:239], a[56:59]\n"
    "s_waitcnt lgkmcnt(8)\n"
    "v_cvt_pk_bf16_f32 v227, v202, v203\n"
    "v_add_f32 v243, v243, v199\n"
    "v_mfma_f32_16x16x32_bf16 a[60:63], v[188:191], v[236:239], a[60:63]\n"
    "v_exp_f32 v204, v204\n"
    "v_mfma_f32_16x16x32_bf16 a[28:31], v[188:191], v[232:235], a[28:31]\n"
    "s_waitcnt lgkmcnt(6)\n"
    "v_add_f32 v242, v242, v201\n"
    "v_add_f32 v243, v243, v204\n"
    "v_mfma_f32_16x16x32_bf16 v[208:211], v[128:131], v[32:35], v[248:251]\n"
    "s_waitcnt lgkmcnt(5)\n"
    "v_exp_f32 v205, v205\n"
    "v_mfma_f32_16x16x32_bf16 v[208:211], v[132:135], v[36:39], v[208:211]\n"
    "v_cvt_pk_bf16_f32 v230, v204, v205\n"
    "v_add_f32 v242, v242, v202\n"
    "v_mfma_f32_16x16x32_bf16 v[208:211], v[136:139], v[40:43], v[208:211]\n"
    "s_waitcnt lgkmcnt(4)\n"
    "v_exp_f32 v206, v206\n"
    "v_mfma_f32_16x16x32_bf16 v[208:211], v[140:143], v[44:47], v[208:211]\n"
    "v_add_f32 v242, v242, v203\n"
    "v_add_f32 v243, v243, v205\n"
    "v_mfma_f32_16x16x32_bf16 v[212:215], v[140:143], v[60:63], v[248:251]\n"
    "v_exp_f32 v207, v207\n"
    "v_mfma_f32_16x16x32_bf16 v[212:215], v[136:139], v[56:59], v[212:215]\n"
    "v_cvt_pk_bf16_f32 v231, v206, v207\n"
    "v_add_f32 v243, v243, v206\n"
    "v_mfma_f32_16x16x32_bf16 v[212:215], v[132:135], v[52:55], v[212:215]\n"
    "v_exp_f32 v208, v208\n"
    "v_mfma_f32_16x16x32_bf16 v[212:215], v[128:131], v[48:51], v[212:215]\n"
    "v_add_f32 v243, v243, v207\n"
    "v_add_f32 v242, v242, v208\n"
    "v_mfma_f32_16x16x32_bf16 a[64:67], v[160:163], v[224:227], 0\n"
    "v_exp_f32 v209, v209\n"
    "v_mfma_f32_16x16x32_bf16 a[96:99], v[160:163], v[228:231], 0\n"
    "ds_read_b64_tr_b16 v[160:161], v253 offset:8256\n"
    "ds_read_b64_tr_b16 v[162:163], v253 offset:12384\n"
    "v_cvt_pk_bf16_f32 v232, v208, v209\n"
    "v_mfma_f32_16x16x32_bf16 a[100:103], v[164:167], v[228:231], 0\n"
    "v_exp_f32 v210, v210\n"
    "v_mfma_f32_16x16x32_bf16 a[68:71], v[164:167], v[224:227], 0\n"
    "ds_read_b64_tr_b16 v[164:165], v253 offset:8264\n"
    "ds_read_b64_tr_b16 v[166:167], v253 offset:12392\n"
    "v_add_f32 v242, v242, v209\n"
    "v_mfma_f32_16x16x32_bf16 a[72:75], v[168:171], v[224:227], 0\n"
    "v_exp_f32 v211, v211\n"
    "v_mfma_f32_16x16x32_bf16 a[104:107], v[168:171], v[228:231], 0\n"
    "ds_read_b64_tr_b16 v[168:169], v253 offset:8512\n"
    "ds_read_b64_tr_b16 v[170:171], v253 offset:12640\n"
    "v_cvt_pk_bf16_f32 v233, v210, v211\n"
    "v_mfma_f32_16x16x32_bf16 a[108:111], v[172:175], v[228:231], 0\n"
    "v_exp_f32 v212, v212\n"
    "v_mfma_f32_16x16x32_bf16 a[76:79], v[172:175], v[224:227], 0\n"
    "s_waitcnt lgkmcnt(8)\n"
    "ds_read_b64_tr_b16 v[172:173], v253 offset:8520\n"
    "ds_read_b64_tr_b16 v[174:175], v253 offset:12648\n"
    "v_add_f32 v242, v242, v210\n"
    "v_mfma_f32_16x16x32_bf16 v[216:219], v[144:147], v[32:35], v[248:251]\n"
    "s_waitcnt lgkmcnt(9)\n"
    "v_exp_f32 v213, v213\n"
    "v_mfma_f32_16x16x32_bf16 v[216:219], v[148:151], v[36:39], v[216:219]\n"
    "v_cvt_pk_bf16_f32 v236, v212, v213\n"
    "v_add_f32 v242, v242, v211\n"
    "v_mfma_f32_16x16x32_bf16 v[216:219], v[152:155], v[40:43], v[216:219]\n"
    "s_waitcnt lgkmcnt(8)\n"
    "v_exp_f32 v214, v214\n"
    "v_mfma_f32_16x16x32_bf16 v[216:219], v[156:159], v[44:47], v[216:219]\n"
    "v_add_f32 v243, v243, v212\n"
    "v_add_f32 v243, v243, v213\n"
    "v_mfma_f32_16x16x32_bf16 v[220:223], v[156:159], v[60:63], v[248:251]\n"
    "v_exp_f32 v215, v215\n"
    "v_mfma_f32_16x16x32_bf16 v[220:223], v[152:155], v[56:59], v[220:223]\n"
    "v_cvt_pk_bf16_f32 v237, v214, v215\n"
    "v_add_f32 v243, v243, v214\n"
    "v_mfma_f32_16x16x32_bf16 v[220:223], v[148:151], v[52:55], v[220:223]\n"
    "v_exp_f32 v216, v216\n"
    "v_mfma_f32_16x16x32_bf16 v[220:223], v[144:147], v[48:51], v[220:223]\n"
    "v_add_f32 v243, v243, v215\n"
    "v_add_f32 v242, v242, v216\n"
    "v_mfma_f32_16x16x32_bf16 a[80:83], v[176:179], v[224:227], 0\n"
    "v_exp_f32 v217, v217\n"
    "v_mfma_f32_16x16x32_bf16 a[112:115], v[176:179], v[228:231], 0\n"
    "ds_read_b64_tr_b16 v[176:177], v253 offset:8768\n"
    "v_cvt_pk_bf16_f32 v234, v216, v217\n"
    "v_mfma_f32_16x16x32_bf16 a[116:119], v[180:183], v[228:231], 0\n"
    "v_exp_f32 v218, v218\n"
    "v_mfma_f32_16x16x32_bf16 a[84:87], v[180:183], v[224:227], 0\n"
    "ds_read_b64_tr_b16 v[178:179], v253 offset:12896\n"
    "ds_read_b64_tr_b16 v[180:181], v253 offset:8776\n"
    "v_add_f32 v242, v242, v217\n"
    "v_mfma_f32_16x16x32_bf16 a[88:91], v[184:187], v[224:227], 0\n"
    "v_exp_f32 v219, v219\n"
    "v_mfma_f32_16x16x32_bf16 a[120:123], v[184:187], v[228:231], 0\n"
    "ds_read_b64_tr_b16 v[182:183], v253 offset:12904\n"
    "v_cvt_pk_bf16_f32 v235, v218, v219\n"
    "v_mfma_f32_16x16x32_bf16 a[124:127], v[188:191], v[228:231], 0\n"
    "v_exp_f32 v220, v220\n"
    "v_mfma_f32_16x16x32_bf16 a[92:95], v[188:191], v[224:227], 0\n"
    "ds_read_b64_tr_b16 v[184:185], v253 offset:9024\n"
    "ds_read_b64_tr_b16 v[186:187], v253 offset:13152\n"
    "v_add_f32 v242, v242, v218\n"
    "v_add_u32 v231, s56, v252\n"
    "s_add_u32 m0, s56, 12384\n"
    "s_nop 0\n"
    "buffer_load_dwordx4 v254, s[36:39], s57 offen nt lds\n"
    "buffer_load_dwordx4 v254, s[36:39], s57 offen offset:1024 nt lds\n"
    "s_add_u32 m0, s56, 12416\n"
    "s_nop 0\n"
    "buffer_load_dwordx4 v254, s[36:39], s57 offen offset:2048 nt lds\n"
    "buffer_load_dwordx4 v254, s[36:39], s57 offen offset:3072 nt lds\n"
    "s_add_u32 s57, s57, 16384\n"
    "s_waitcnt vmcnt(12)\n"
    "ds_read_b128 v[64:67], v231 offset:0\n"
    "ds_read_b128 v[68:71], v231 offset:256\n"
    "ds_read_b128 v[72:75], v231 offset:512\n"
    "ds_read_b128 v[76:79], v231 offset:768\n"
    "s_waitcnt lgkmcnt(3)\n"
    "v_lshlrev_b32 v224, 16, v64\n"
    "v_and_b32 v225, 0xffff0000, v64\n"
    "v_lshlrev_b32 v226, 16, v65\n"
    "v_and_b32 v227, 0xffff0000, v65\n"
    "v_lshlrev_b32 v228, 16, v66\n"
    "v_and_b32 v229, 0xffff0000, v66\n"
    "v_lshlrev_b32 v230, 16, v67\n"
    "v_and_b32 v231, 0xffff0000, v67\n"
    "v_pk_mul_f32 v[224:225], v[224:225], s[58:59] op_sel_hi:[1,0]\n"
    "v_pk_mul_f32 v[226:227], v[226:227], s[58:59] op_sel_hi:[1,0]\n"
    "v_pk_mul_f32 v[228:229], v[228:229], s[58:59] op_sel_hi:[1,0]\n"
    "v_pk_mul_f32 v[230:231], v[230:231], s[58:59] op_sel_hi:[1,0]\n"
    "v_cvt_pk_bf16_f32 v64, v224, v225\n"
    "v_cvt_pk_bf16_f32 v65, v226, v227\n"
    "v_cvt_pk_bf16_f32 v66, v228, v229\n"
    "v_cvt_pk_bf16_f32 v67, v230, v231\n"
    "s_waitcnt lgkmcnt(2)\n"
    "v_lshlrev_b32 v224, 16, v68\n"
    "v_and_b32 v225, 0xffff0000, v68\n"
    "v_lshlrev_b32 v226, 16, v69\n"
    "v_and_b32 v227, 0xffff0000, v69\n"
    "v_lshlrev_b32 v228, 16, v70\n"
    "v_and_b32 v229, 0xffff0000, v70\n"
    "v_lshlrev_b32 v230, 16, v71\n"
    "v_and_b32 v231, 0xffff0000, v71\n"
    "v_pk_mul_f32 v[224:225], v[224:225], s[58:59] op_sel_hi:[1,0]\n"
    "v_pk_mul_f32 v[226:227], v[226:227], s[58:59] op_sel_hi:[1,0]\n"
    "v_pk_mul_f32 v[228:229], v[228:229], s[58:59] op_sel_hi:[1,0]\n"
    "v_pk_mul_f32 v[230:231], v[230:231], s[58:59] op_sel_hi:[1,0]\n"
    "v_cvt_pk_bf16_f32 v68, v224, v225\n"
    "v_cvt_pk_bf16_f32 v69, v226, v227\n"
    "v_cvt_pk_bf16_f32 v70, v228, v229\n"
    "v_cvt_pk_bf16_f32 v71, v230, v231\n"
    "s_waitcnt lgkmcnt(1)\n"
    "v_lshlrev_b32 v224, 16, v72\n"
    "v_and_b32 v225, 0xffff0000, v72\n"
    "v_lshlrev_b32 v226, 16, v73\n"
    "v_and_b32 v227, 0xffff0000, v73\n"
    "v_lshlrev_b32 v228, 16, v74\n"
    "v_and_b32 v229, 0xffff0000, v74\n"
    "v_lshlrev_b32 v230, 16, v75\n"
    "v_and_b32 v231, 0xffff0000, v75\n"
    "v_pk_mul_f32 v[224:225], v[224:225], s[58:59] op_sel_hi:[1,0]\n"
    "v_pk_mul_f32 v[226:227], v[226:227], s[58:59] op_sel_hi:[1,0]\n"
    "v_pk_mul_f32 v[228:229], v[228:229], s[58:59] op_sel_hi:[1,0]\n"
    "v_pk_mul_f32 v[230:231], v[230:231], s[58:59] op_sel_hi:[1,0]\n"
    "v_cvt_pk_bf16_f32 v72, v224, v225\n"
    "v_cvt_pk_bf16_f32 v73, v226, v227\n"
    "v_cvt_pk_bf16_f32 v74, v228, v229\n"
    "v_cvt_pk_bf16_f32 v75, v230, v231\n"
    "s_waitcnt lgkmcnt(0)\n"
    "v_lshlrev_b32 v224, 16, v76\n"
    "v_and_b32 v225, 0xffff0000, v76\n"
    "v_lshlrev_b32 v226, 16, v77\n"
    "v_and_b32 v227, 0xffff0000, v77\n"
    "v_lshlrev_b32 v228, 16, v78\n"
    "v_and_b32 v229, 0xffff0000, v78\n"
    "v_lshlrev_b32 v230, 16, v79\n"
    "v_and_b32 v231, 0xffff0000, v79\n"
    "v_pk_mul_f32 v[224:225], v[224:225], s[58:59] op_sel_hi:[1,0]\n"
    "v_pk_mul_f32 v[226:227], v[226:227], s[58:59] op_sel_hi:[1,0]\n"
    "v_pk_mul_f32 v[228:229], v[228:229], s[58:59] op_sel_hi:[1,0]\n"
    "v_pk_mul_f32 v[230:231], v[230:231], s[58:59] op_sel_hi:[1,0]\n"
    "v_cvt_pk_bf16_f32 v76, v224, v225\n"
    "v_cvt_pk_bf16_f32 v77, v226, v227\n"
    "v_cvt_pk_bf16_f32 v78, v228, v229\n"
    "v_cvt_pk_bf16_f32 v79, v230, v231\n"
    "v_add_u32 v231, s56, v252\n"
    "s_add_u32 s53, s66, 16384\n"
    "s_add_u32 m0, s54, 16512\n"
    "s_nop 0\n"
    "buffer_load_dwordx4 v254, s[40:43], s53 offen nt lds\n"
    "buffer_load_dwordx4 v254, s[40:43], s53 offen offset:1024 nt lds\n"
    "s_add_u32 m0, s55, 74304\n"
    "s_nop 0\n"
    "buffer_load_dwordx4 v254, s[44:47], s53 offen nt lds\n"
    "s_add_u32 m0, s55, 74312\n"
    "s_nop 0\n"
    "buffer_load_dwordx4 v254, s[44:47], s53 offen offset:1024 nt lds\n"
    "s_waitcnt vmcnt(12)\n"
    "ds_read_b128 v[80:83], v231 offset:4128\n"
    "ds_read_b128 v[84:87], v231 offset:4384\n"
    "ds_read_b128 v[88:91], v231 offset:4640\n"
    "ds_read_b128 v[92:95], v231 offset:4896\n"
    "s_waitcnt lgkmcnt(3)\n"
    "v_lshlrev_b32 v224, 16, v80\n"
    "v_and_b32 v225, 0xffff0000, v80\n"
    "v_lshlrev_b32 v226, 16, v81\n"
    "v_and_b32 v227, 0xffff0000, v81\n"
    "v_lshlrev_b32 v228, 16, v82\n"
    "v_and_b32 v229, 0xffff0000, v82\n"
    "v_lshlrev_b32 v230, 16, v83\n"
    "v_and_b32 v231, 0xffff0000, v83\n"
    "v_pk_mul_f32 v[224:225], v[224:225], s[58:59] op_sel_hi:[1,0]\n"
    "v_pk_mul_f32 v[226:227], v[226:227], s[58:59] op_sel_hi:[1,0]\n"
    "v_pk_mul_f32 v[228:229], v[228:229], s[58:59] op_sel_hi:[1,0]\n"
    "v_pk_mul_f32 v[230:231], v[230:231], s[58:59] op_sel_hi:[1,0]\n"
    "v_cvt_pk_bf16_f32 v80, v224, v225\n"
    "v_cvt_pk_bf16_f32 v81, v226, v227\n"
    "v_cvt_pk_bf16_f32 v82, v228, v229\n"
    "v_cvt_pk_bf16_f32 v83, v230, v231\n"
    "s_waitcnt lgkmcnt(2)\n"
    "v_lshlrev_b32 v224, 16, v84\n"
    "v_and_b32 v225, 0xffff0000, v84\n"
    "v_lshlrev_b32 v226, 16, v85\n"
    "v_and_b32 v227, 0xffff0000, v85\n"
    "v_lshlrev_b32 v228, 16, v86\n"
    "v_and_b32 v229, 0xffff0000, v86\n"
    "v_lshlrev_b32 v230, 16, v87\n"
    "v_and_b32 v231, 0xffff0000, v87\n"
    "v_pk_mul_f32 v[224:225], v[224:225], s[58:59] op_sel_hi:[1,0]\n"
    "v_pk_mul_f32 v[226:227], v[226:227], s[58:59] op_sel_hi:[1,0]\n"
    "v_pk_mul_f32 v[228:229], v[228:229], s[58:59] op_sel_hi:[1,0]\n"
    "v_pk_mul_f32 v[230:231], v[230:231], s[58:59] op_sel_hi:[1,0]\n"
    "v_cvt_pk_bf16_f32 v84, v224, v225\n"
    "v_cvt_pk_bf16_f32 v85, v226, v227\n"
    "v_cvt_pk_bf16_f32 v86, v228, v229\n"
    "v_cvt_pk_bf16_f32 v87, v230, v231\n"
    "s_waitcnt lgkmcnt(1)\n"
    "v_lshlrev_b32 v224, 16, v88\n"
    "v_and_b32 v225, 0xffff0000, v88\n"
    "v_lshlrev_b32 v226, 16, v89\n"
    "v_and_b32 v227, 0xffff0000, v89\n"
    "v_lshlrev_b32 v228, 16, v90\n"
    "v_and_b32 v229, 0xffff0000, v90\n"
    "v_lshlrev_b32 v230, 16, v91\n"
    "v_and_b32 v231, 0xffff0000, v91\n"
    "v_pk_mul_f32 v[224:225], v[224:225], s[58:59] op_sel_hi:[1,0]\n"
    "v_pk_mul_f32 v[226:227], v[226:227], s[58:59] op_sel_hi:[1,0]\n"
    "v_pk_mul_f32 v[228:229], v[228:229], s[58:59] op_sel_hi:[1,0]\n"
    "v_pk_mul_f32 v[230:231], v[230:231], s[58:59] op_sel_hi:[1,0]\n"
    "v_cvt_pk_bf16_f32 v88, v224, v225\n"
    "v_cvt_pk_bf16_f32 v89, v226, v227\n"
    "v_cvt_pk_bf16_f32 v90, v228, v229\n"
    "v_cvt_pk_bf16_f32 v91, v230, v231\n"
    "s_waitcnt lgkmcnt(0)\n"
    "v_lshlrev_b32 v224, 16, v92\n"
    "v_and_b32 v225, 0xffff0000, v92\n"
    "v_lshlrev_b32 v226, 16, v93\n"
    "v_and_b32 v227, 0xffff0000, v93\n"
    "v_lshlrev_b32 v228, 16, v94\n"
    "v_and_b32 v229, 0xffff0000, v94\n"
    "v_lshlrev_b32 v230, 16, v95\n"
    "v_and_b32 v231, 0xffff0000, v95\n"
    "v_pk_mul_f32 v[224:225], v[224:225], s[58:59] op_sel_hi:[1,0]\n"
    "v_pk_mul_f32 v[226:227], v[226:227], s[58:59] op_sel_hi:[1,0]\n"
    "v_pk_mul_f32 v[228:229], v[228:229], s[58:59] op_sel_hi:[1,0]\n"
    "v_pk_mul_f32 v[230:231], v[230:231], s[58:59] op_sel_hi:[1,0]\n"
    "v_cvt_pk_bf16_f32 v92, v224, v225\n"
    "v_cvt_pk_bf16_f32 v93, v226, v227\n"
    "v_cvt_pk_bf16_f32 v94, v228, v229\n"
    "v_cvt_pk_bf16_f32 v95, v230, v231\n"
    "v_mfma_f32_16x16x32_bf16 v[192:195], v[128:131], v[64:67], v[248:251]\n"
    "v_exp_f32 v221, v221\n"
    "v_mfma_f32_16x16x32_bf16 v[192:195], v[132:135], v[68:71], v[192:195]\n"
    "ds_read_b64_tr_b16 v[188:189], v253 offset:9032\n"
    "v_cvt_pk_bf16_f32 v238, v220, v221\n"
    "v_mfma_f32_16x16x32_bf16 v[192:195], v[136:139], v[72:75], v[192:195]\n"
    "v_exp_f32 v222, v222\n"
    "v_mfma_f32_16x16x32_bf16 v[192:195], v[140:143], v[76:79], v[192:195]\n"
    "ds_read_b64_tr_b16 v[190:191], v253 offset:13160\n"
    "v_add_f32 v242, v242, v219\n"
    "v_mfma_f32_16x16x32_bf16 v[196:199], v[140:143], v[92:95], v[248:251]\n"
    "v_exp_f32 v223, v223\n"
    "v_mfma_f32_16x16x32_bf16 v[196:199], v[136:139], v[88:91], v[196:199]\n"
    "ds_read_b128 v[136:139], v252 offset:512\n"
    "v_cvt_pk_bf16_f32 v239, v222, v223\n"
    "v_mfma_f32_16x16x32_bf16 v[196:199], v[132:135], v[84:87], v[196:199]\n"
    "v_exp_f32 v192, v192\n"
    "v_mfma_f32_16x16x32_bf16 v[196:199], v[128:131], v[80:83], v[196:199]\n"
    "ds_read_b128 v[128:131], v252 offset:0\n"
    "ds_read_b128 v[132:135], v252 offset:256\n"
    "v_add_f32 v243, v243, v220\n"
    "v_mfma_f32_16x16x32_bf16 a[64:67], v[160:163], v[232:235], a[64:67]\n"
    "v_exp_f32 v193, v193\n"
    "v_mfma_f32_16x16x32_bf16 a[96:99], v[160:163], v[236:239], a[96:99]\n"
    "ds_read_b128 v[140:143], v252 offset:768\n"
    "v_cvt_pk_bf16_f32 v224, v192, v193\n"
    "v_mfma_f32_16x16x32_bf16 a[100:103], v[164:167], v[236:239], a[100:103]\n"
    "v_exp_f32 v194, v194\n"
    "v_mfma_f32_16x16x32_bf16 a[68:71], v[164:167], v[232:235], a[68:71]\n"
    "v_add_f32 v243, v243, v221\n"
    "v_add_f32 v244, v244, v192\n"
    "v_mfma_f32_16x16x32_bf16 a[72:75], v[168:171], v[232:235], a[72:75]\n"
    "v_exp_f32 v195, v195\n"
    "v_mfma_f32_16x16x32_bf16 a[104:107], v[168:171], v[236:239], a[104:107]\n"
    "v_cvt_pk_bf16_f32 v225, v194, v195\n"
    "v_add_f32 v243, v243, v222\n"
    "v_mfma_f32_16x16x32_bf16 a[108:111], v[172:175], v[236:239], a[108:111]\n"
    "v_exp_f32 v196, v196\n"
    "v_mfma_f32_16x16x32_bf16 a[76:79], v[172:175], v[232:235], a[76:79]\n"
    "v_add_f32 v243, v243, v223\n"
    "v_add_f32 v244, v244, v193\n"
    "v_mfma_f32_16x16x32_bf16 v[200:203], v[144:147], v[64:67], v[248:251]\n"
    "v_exp_f32 v197, v197\n"
    "v_mfma_f32_16x16x32_bf16 v[200:203], v[148:151], v[68:71], v[200:203]\n"
    "v_cvt_pk_bf16_f32 v228, v196, v197\n"
    "v_add_f32 v244, v244, v194\n"
    "v_mfma_f32_16x16x32_bf16 v[200:203], v[152:155], v[72:75], v[200:203]\n"
    "v_exp_f32 v198, v198\n"
    "v_mfma_f32_16x16x32_bf16 v[200:203], v[156:159], v[76:79], v[200:203]\n"
    "v_add_f32 v244, v244, v195\n"
    "v_add_f32 v245, v245, v196\n"
    "v_mfma_f32_16x16x32_bf16 v[204:207], v[156:159], v[92:95], v[248:251]\n"
    "v_exp_f32 v199, v199\n"
    "v_mfma_f32_16x16x32_bf16 v[204:207], v[152:155], v[88:91], v[204:207]\n"
    "ds_read_b128 v[152:155], v252 offset:4640\n"
    "v_cvt_pk_bf16_f32 v229, v198, v199\n"
    "v_mfma_f32_16x16x32_bf16 v[204:207], v[148:151], v[84:87], v[204:207]\n"
    "v_exp_f32 v200, v200\n"
    "v_mfma_f32_16x16x32_bf16 v[204:207], v[144:147], v[80:83], v[204:207]\n"
    "ds_read_b128 v[144:147], v252 offset:4128\n"
    "ds_read_b128 v[148:151], v252 offset:4384\n"
    "v_add_f32 v245, v245, v197\n"
    "v_mfma_f32_16x16x32_bf16 a[80:83], v[176:179], v[232:235], a[80:83]\n"
    "v_exp_f32 v201, v201\n"
    "v_mfma_f32_16x16x32_bf16 a[112:115], v[176:179], v[236:239], a[112:115]\n"
    "ds_read_b128 v[156:159], v252 offset:4896\n"
    "v_cvt_pk_bf16_f32 v226, v200, v201\n"
    "v_mfma_f32_16x16x32_bf16 a[116:119], v[180:183], v[236:239], a[116:119]\n"
    "v_exp_f32 v202, v202\n"
    "v_mfma_f32_16x16x32_bf16 a[84:87], v[180:183], v[232:235], a[84:87]\n"
    "v_add_f32 v245, v245, v198\n"
    "v_add_f32 v244, v244, v200\n"
    "v_mfma_f32_16x16x32_bf16 a[88:91], v[184:187], v[232:235], a[88:91]\n"
    "v_exp_f32 v203, v203\n"
    "v_mfma_f32_16x16x32_bf16 a[120:123], v[184:187], v[236:239], a[120:123]\n"
    "s_waitcnt lgkmcnt(8)\n"
    "v_cvt_pk_bf16_f32 v227, v202, v203\n"
    "v_add_f32 v245, v245, v199\n"
    "v_mfma_f32_16x16x32_bf16 a[124:127], v[188:191], v[236:239], a[124:127]\n"
    "v_exp_f32 v204, v204\n"
    "v_mfma_f32_16x16x32_bf16 a[92:95], v[188:191], v[232:235], a[92:95]\n"
    "s_waitcnt lgkmcnt(6)\n"
    "v_add_f32 v244, v244, v201\n"
    "v_add_f32 v245, v245, v204\n"
    "v_mfma_f32_16x16x32_bf16 v[208:211], v[128:131], v[64:67], v[248:251]\n"
    "s_waitcnt lgkmcnt(5)\n"
    "v_exp_f32 v205, v205\n"
    "v_mfma_f32_16x16x32_bf16 v[208:211], v[132:135], v[68:71], v[208:211]\n"
    "v_cvt_pk_bf16_f32 v230, v204, v205\n"
    "v_add_f32 v244, v244, v202\n"
    "v_mfma_f32_16x16x32_bf16 v[208:211], v[136:139], v[72:75], v[208:211]\n"
    "s_waitcnt lgkmcnt(4)\n"
    "v_exp_f32 v206, v206\n"
    "v_mfma_f32_16x16x32_bf16 v[208:211], v[140:143], v[76:79], v[208:211]\n"
    "v_add_f32 v244, v244, v203\n"
    "v_add_f32 v245, v245, v205\n"
    "v_mfma_f32_16x16x32_bf16 v[212:215], v[140:143], v[92:95], v[248:251]\n"
    "v_exp_f32 v207, v207\n"
    "v_mfma_f32_16x16x32_bf16 v[212:215], v[136:139], v[88:91], v[212:215]\n"
    "v_cvt_pk_bf16_f32 v231, v206, v207\n"
    "v_add_f32 v245, v245, v206\n"
    "v_mfma_f32_16x16x32_bf16 v[212:215], v[132:135], v[84:87], v[212:215]\n"
    "v_exp_f32 v208, v208\n"
    "v_mfma_f32_16x16x32_bf16 v[212:215], v[128:131], v[80:83], v[212:215]\n"
    "v_add_f32 v245, v245, v207\n"
    "v_add_f32 v244, v244, v208\n"
    "v_mfma_f32_16x16x32_bf16 a[128:131], v[160:163], v[224:227], 0\n"
    "v_exp_f32 v209, v209\n"
    "v_mfma_f32_16x16x32_bf16 a[160:163], v[160:163], v[228:231], 0\n"
    "ds_read_b64_tr_b16 v[160:161], v253 offset:0\n"
    "ds_read_b64_tr_b16 v[162:163], v253 offset:4128\n"
    "v_cvt_pk_bf16_f32 v232, v208, v209\n"
    "v_mfma_f32_16x16x32_bf16 a[164:167], v[164:167], v[228:231], 0\n"
    "v_exp_f32 v210, v210\n"
    "v_mfma_f32_16x16x32_bf16 a[132:135], v[164:167], v[224:227], 0\n"
    "ds_read_b64_tr_b16 v[164:165], v253 offset:8\n"
    "ds_read_b64_tr_b16 v[166:167], v253 offset:4136\n"
    "v_add_f32 v244, v244, v209\n"
    "v_mfma_f32_16x16x32_bf16 a[136:139], v[168:171], v[224:227], 0\n"
    "v_exp_f32 v211, v211\n"
    "v_mfma_f32_16x16x32_bf16 a[168:171], v[168:171], v[228:231], 0\n"
    "ds_read_b64_tr_b16 v[168:169], v253 offset:256\n"
    "ds_read_b64_tr_b16 v[170:171], v253 offset:4384\n"
    "v_cvt_pk_bf16_f32 v233, v210, v211\n"
    "v_mfma_f32_16x16x32_bf16 a[172:175], v[172:175], v[228:231], 0\n"
    "v_exp_f32 v212, v212\n"
    "v_mfma_f32_16x16x32_bf16 a[140:143], v[172:175], v[224:227], 0\n"
    "s_waitcnt lgkmcnt(8)\n"
    "ds_read_b64_tr_b16 v[172:173], v253 offset:264\n"
    "ds_read_b64_tr_b16 v[174:175], v253 offset:4392\n"
    "v_add_f32 v244, v244, v210\n"
    "v_mfma_f32_16x16x32_bf16 v[216:219], v[144:147], v[64:67], v[248:251]\n"
    "s_waitcnt lgkmcnt(9)\n"
    "v_exp_f32 v213, v213\n"
    "v_mfma_f32_16x16x32_bf16 v[216:219], v[148:151], v[68:71], v[216:219]\n"
    "v_cvt_pk_bf16_f32 v236, v212, v213\n"
    "v_add_f32 v244, v244, v211\n"
    "v_mfma_f32_16x16x32_bf16 v[216:219], v[152:155], v[72:75], v[216:219]\n"
    "s_waitcnt lgkmcnt(8)\n"
    "v_exp_f32 v214, v214\n"
    "v_mfma_f32_16x16x32_bf16 v[216:219], v[156:159], v[76:79], v[216:219]\n"
    "v_add_f32 v245, v245, v212\n"
    "v_add_f32 v245, v245, v213\n"
    "v_mfma_f32_16x16x32_bf16 v[220:223], v[156:159], v[92:95], v[248:251]\n"
    "v_exp_f32 v215, v215\n"
    "v_mfma_f32_16x16x32_bf16 v[220:223], v[152:155], v[88:91], v[220:223]\n"
    "v_cvt_pk_bf16_f32 v237, v214, v215\n"
    "v_add_f32 v245, v245, v214\n"
    "v_mfma_f32_16x16x32_bf16 v[220:223], v[148:151], v[84:87], v[220:223]\n"
    "v_exp_f32 v216, v216\n"
    "v_mfma_f32_16x16x32_bf16 v[220:223], v[144:147], v[80:83], v[220:223]\n"
    "v_add_f32 v245, v245, v215\n"
    "v_add_f32 v244, v244, v216\n"
    "v_mfma_f32_16x16x32_bf16 a[144:147], v[176:179], v[224:227], 0\n"
    "v_exp_f32 v217, v217\n"
    "v_mfma_f32_16x16x32_bf16 a[176:179], v[176:179], v[228:231], 0\n"
    "ds_read_b64_tr_b16 v[176:177], v253 offset:512\n"
    "v_cvt_pk_bf16_f32 v234, v216, v217\n"
    "v_mfma_f32_16x16x32_bf16 a[180:183], v[180:183], v[228:231], 0\n"
    "v_exp_f32 v218, v218\n"
    "v_mfma_f32_16x16x32_bf16 a[148:151], v[180:183], v[224:227], 0\n"
    "ds_read_b64_tr_b16 v[178:179], v253 offset:4640\n"
    "ds_read_b64_tr_b16 v[180:181], v253 offset:520\n"
    "v_add_f32 v244, v244, v217\n"
    "v_mfma_f32_16x16x32_bf16 a[152:155], v[184:187], v[224:227], 0\n"
    "v_exp_f32 v219, v219\n"
    "v_mfma_f32_16x16x32_bf16 a[184:187], v[184:187], v[228:231], 0\n"
    "ds_read_b64_tr_b16 v[182:183], v253 offset:4648\n"
    "v_cvt_pk_bf16_f32 v235, v218, v219\n"
    "v_mfma_f32_16x16x32_bf16 a[188:191], v[188:191], v[228:231], 0\n"
    "v_exp_f32 v220, v220\n"
    "v_mfma_f32_16x16x32_bf16 a[156:159], v[188:191], v[224:227], 0\n"
    "ds_read_b64_tr_b16 v[184:185], v253 offset:768\n"
    "ds_read_b64_tr_b16 v[186:187], v253 offset:4896\n"
    "v_add_f32 v244, v244, v218\n"
    "v_add_u32 v231, s56, v252\n"
    "s_add_u32 s53, s66, 24576\n"
    "s_add_u32 m0, s54, 24768\n"
    "s_nop 0\n"
    "buffer_load_dwordx4 v254, s[40:43], s53 offen nt lds\n"
    "buffer_load_dwordx4 v254, s[40:43], s53 offen offset:1024 nt lds\n"
    "s_add_u32 m0, s55, 82560\n"
    "s_nop 0\n"
    "buffer_load_dwordx4 v254, s[44:47], s53 offen nt lds\n"
    "s_add_u32 m0, s55, 82568\n"
    "s_nop 0\n"
    "buffer_load_dwordx4 v254, s[44:47], s53 offen offset:1024 nt lds\n"
    "s_waitcnt vmcnt(12)\n"
    "ds_read_b128 v[96:99], v231 offset:8256\n"
    "ds_read_b128 v[100:103], v231 offset:8512\n"
    "ds_read_b128 v[104:107], v231 offset:8768\n"
    "ds_read_b128 v[108:111], v231 offset:9024\n"
    "s_waitcnt lgkmcnt(3)\n"
    "v_lshlrev_b32 v224, 16, v96\n"
    "v_and_b32 v225, 0xffff0000, v96\n"
    "v_lshlrev_b32 v226, 16, v97\n"
    "v_and_b32 v227, 0xffff0000, v97\n"
    "v_lshlrev_b32 v228, 16, v98\n"
    "v_and_b32 v229, 0xffff0000, v98\n"
    "v_lshlrev_b32 v230, 16, v99\n"
    "v_and_b32 v231, 0xffff0000, v99\n"
    "v_pk_mul_f32 v[224:225], v[224:225], s[58:59] op_sel_hi:[1,0]\n"
    "v_pk_mul_f32 v[226:227], v[226:227], s[58:59] op_sel_hi:[1,0]\n"
    "v_pk_mul_f32 v[228:229], v[228:229], s[58:59] op_sel_hi:[1,0]\n"
    "v_pk_mul_f32 v[230:231], v[230:231], s[58:59] op_sel_hi:[1,0]\n"
    "v_cvt_pk_bf16_f32 v96, v224, v225\n"
    "v_cvt_pk_bf16_f32 v97, v226, v227\n"
    "v_cvt_pk_bf16_f32 v98, v228, v229\n"
    "v_cvt_pk_bf16_f32 v99, v230, v231\n"
    "s_waitcnt lgkmcnt(2)\n"
    "v_lshlrev_b32 v224, 16, v100\n"
    "v_and_b32 v225, 0xffff0000, v100\n"
    "v_lshlrev_b32 v226, 16, v101\n"
    "v_and_b32 v227, 0xffff0000, v101\n"
    "v_lshlrev_b32 v228, 16, v102\n"
    "v_and_b32 v229, 0xffff0000, v102\n"
    "v_lshlrev_b32 v230, 16, v103\n"
    "v_and_b32 v231, 0xffff0000, v103\n"
    "v_pk_mul_f32 v[224:225], v[224:225], s[58:59] op_sel_hi:[1,0]\n"
    "v_pk_mul_f32 v[226:227], v[226:227], s[58:59] op_sel_hi:[1,0]\n"
    "v_pk_mul_f32 v[228:229], v[228:229], s[58:59] op_sel_hi:[1,0]\n"
    "v_pk_mul_f32 v[230:231], v[230:231], s[58:59] op_sel_hi:[1,0]\n"
    "v_cvt_pk_bf16_f32 v100, v224, v225\n"
    "v_cvt_pk_bf16_f32 v101, v226, v227\n"
    "v_cvt_pk_bf16_f32 v102, v228, v229\n"
    "v_cvt_pk_bf16_f32 v103, v230, v231\n"
    "s_waitcnt lgkmcnt(1)\n"
    "v_lshlrev_b32 v224, 16, v104\n"
    "v_and_b32 v225, 0xffff0000, v104\n"
    "v_lshlrev_b32 v226, 16, v105\n"
    "v_and_b32 v227, 0xffff0000, v105\n"
    "v_lshlrev_b32 v228, 16, v106\n"
    "v_and_b32 v229, 0xffff0000, v106\n"
    "v_lshlrev_b32 v230, 16, v107\n"
    "v_and_b32 v231, 0xffff0000, v107\n"
    "v_pk_mul_f32 v[224:225], v[224:225], s[58:59] op_sel_hi:[1,0]\n"
    "v_pk_mul_f32 v[226:227], v[226:227], s[58:59] op_sel_hi:[1,0]\n"
    "v_pk_mul_f32 v[228:229], v[228:229], s[58:59] op_sel_hi:[1,0]\n"
    "v_pk_mul_f32 v[230:231], v[230:231], s[58:59] op_sel_hi:[1,0]\n"
    "v_cvt_pk_bf16_f32 v104, v224, v225\n"
    "v_cvt_pk_bf16_f32 v105, v226, v227\n"
    "v_cvt_pk_bf16_f32 v106, v228, v229\n"
    "v_cvt_pk_bf16_f32 v107, v230, v231\n"
    "s_waitcnt lgkmcnt(0)\n"
    "v_lshlrev_b32 v224, 16, v108\n"
    "v_and_b32 v225, 0xffff0000, v108\n"
    "v_lshlrev_b32 v226, 16, v109\n"
    "v_and_b32 v227, 0xffff0000, v109\n"
    "v_lshlrev_b32 v228, 16, v110\n"
    "v_and_b32 v229, 0xffff0000, v110\n"
    "v_lshlrev_b32 v230, 16, v111\n"
    "v_and_b32 v231, 0xffff0000, v111\n"
    "v_pk_mul_f32 v[224:225], v[224:225], s[58:59] op_sel_hi:[1,0]\n"
    "v_pk_mul_f32 v[226:227], v[226:227], s[58:59] op_sel_hi:[1,0]\n"
    "v_pk_mul_f32 v[228:229], v[228:229], s[58:59] op_sel_hi:[1,0]\n"
    "v_pk_mul_f32 v[230:231], v[230:231], s[58:59] op_sel_hi:[1,0]\n"
    "v_cvt_pk_bf16_f32 v108, v224, v225\n"
    "v_cvt_pk_bf16_f32 v109, v226, v227\n"
    "v_cvt_pk_bf16_f32 v110, v228, v229\n"
    "v_cvt_pk_bf16_f32 v111, v230, v231\n"
    "v_add_u32 v231, s56, v252\n"
    "s_waitcnt vmcnt(8)\n"
    "ds_read_b128 v[112:115], v231 offset:12384\n"
    "ds_read_b128 v[116:119], v231 offset:12640\n"
    "ds_read_b128 v[120:123], v231 offset:12896\n"
    "ds_read_b128 v[124:127], v231 offset:13152\n"
    "s_waitcnt lgkmcnt(3)\n"
    "v_lshlrev_b32 v224, 16, v112\n"
    "v_and_b32 v225, 0xffff0000, v112\n"
    "v_lshlrev_b32 v226, 16, v113\n"
    "v_and_b32 v227, 0xffff0000, v113\n"
    "v_lshlrev_b32 v228, 16, v114\n"
    "v_and_b32 v229, 0xffff0000, v114\n"
    "v_lshlrev_b32 v230, 16, v115\n"
    "v_and_b32 v231, 0xffff0000, v115\n"
    "v_pk_mul_f32 v[224:225], v[224:225], s[58:59] op_sel_hi:[1,0]\n"
    "v_pk_mul_f32 v[226:227], v[226:227], s[58:59] op_sel_hi:[1,0]\n"
    "v_pk_mul_f32 v[228:229], v[228:229], s[58:59] op_sel_hi:[1,0]\n"
    "v_pk_mul_f32 v[230:231], v[230:231], s[58:59] op_sel_hi:[1,0]\n"
    "v_cvt_pk_bf16_f32 v112, v224, v225\n"
    "v_cvt_pk_bf16_f32 v113, v226, v227\n"
    "v_cvt_pk_bf16_f32 v114, v228, v229\n"
    "v_cvt_pk_bf16_f32 v115, v230, v231\n"
    "s_waitcnt lgkmcnt(2)\n"
    "v_lshlrev_b32 v224, 16, v116\n"
    "v_and_b32 v225, 0xffff0000, v116\n"
    "v_lshlrev_b32 v226, 16, v117\n"
    "v_and_b32 v227, 0xffff0000, v117\n"
    "v_lshlrev_b32 v228, 16, v118\n"
    "v_and_b32 v229, 0xffff0000, v118\n"
    "v_lshlrev_b32 v230, 16, v119\n"
    "v_and_b32 v231, 0xffff0000, v119\n"
    "v_pk_mul_f32 v[224:225], v[224:225], s[58:59] op_sel_hi:[1,0]\n"
    "v_pk_mul_f32 v[226:227], v[226:227], s[58:59] op_sel_hi:[1,0]\n"
    "v_pk_mul_f32 v[228:229], v[228:229], s[58:59] op_sel_hi:[1,0]\n"
    "v_pk_mul_f32 v[230:231], v[230:231], s[58:59] op_sel_hi:[1,0]\n"
    "v_cvt_pk_bf16_f32 v116, v224, v225\n"
    "v_cvt_pk_bf16_f32 v117, v226, v227\n"
    "v_cvt_pk_bf16_f32 v118, v228, v229\n"
    "v_cvt_pk_bf16_f32 v119, v230, v231\n"
    "s_waitcnt lgkmcnt(1)\n"
    "v_lshlrev_b32 v224, 16, v120\n"
    "v_and_b32 v225, 0xffff0000, v120\n"
    "v_lshlrev_b32 v226, 16, v121\n"
    "v_and_b32 v227, 0xffff0000, v121\n"
    "v_lshlrev_b32 v228, 16, v122\n"
    "v_and_b32 v229, 0xffff0000, v122\n"
    "v_lshlrev_b32 v230, 16, v123\n"
    "v_and_b32 v231, 0xffff0000, v123\n"
    "v_pk_mul_f32 v[224:225], v[224:225], s[58:59] op_sel_hi:[1,0]\n"
    "v_pk_mul_f32 v[226:227], v[226:227], s[58:59] op_sel_hi:[1,0]\n"
    "v_pk_mul_f32 v[228:229], v[228:229], s[58:59] op_sel_hi:[1,0]\n"
    "v_pk_mul_f32 v[230:231], v[230:231], s[58:59] op_sel_hi:[1,0]\n"
    "v_cvt_pk_bf16_f32 v120, v224, v225\n"
    "v_cvt_pk_bf16_f32 v121, v226, v227\n"
    "v_cvt_pk_bf16_f32 v122, v228, v229\n"
    "v_cvt_pk_bf16_f32 v123, v230, v231\n"
    "s_waitcnt lgkmcnt(0)\n"
    "v_lshlrev_b32 v224, 16, v124\n"
    "v_and_b32 v225, 0xffff0000, v124\n"
    "v_lshlrev_b32 v226, 16, v125\n"
    "v_and_b32 v227, 0xffff0000, v125\n"
    "v_lshlrev_b32 v228, 16, v126\n"
    "v_and_b32 v229, 0xffff0000, v126\n"
    "v_lshlrev_b32 v230, 16, v127\n"
    "v_and_b32 v231, 0xffff0000, v127\n"
    "v_pk_mul_f32 v[224:225], v[224:225], s[58:59] op_sel_hi:[1,0]\n"
    "v_pk_mul_f32 v[226:227], v[226:227], s[58:59] op_sel_hi:[1,0]\n"
    "v_pk_mul_f32 v[228:229], v[228:229], s[58:59] op_sel_hi:[1,0]\n"
    "v_pk_mul_f32 v[230:231], v[230:231], s[58:59] op_sel_hi:[1,0]\n"
    "v_cvt_pk_bf16_f32 v124, v224, v225\n"
    "v_cvt_pk_bf16_f32 v125, v226, v227\n"
    "v_cvt_pk_bf16_f32 v126, v228, v229\n"
    "v_cvt_pk_bf16_f32 v127, v230, v231\n"
    "v_mfma_f32_16x16x32_bf16 v[192:195], v[128:131], v[96:99], v[248:251]\n"
    "v_exp_f32 v221, v221\n"
    "v_mfma_f32_16x16x32_bf16 v[192:195], v[132:135], v[100:103], v[192:195]\n"
    "ds_read_b64_tr_b16 v[188:189], v253 offset:776\n"
    "v_cvt_pk_bf16_f32 v238, v220, v221\n"
    "v_mfma_f32_16x16x32_bf16 v[192:195], v[136:139], v[104:107], v[192:195]\n"
    "v_exp_f32 v222, v222\n"
    "v_mfma_f32_16x16x32_bf16 v[192:195], v[140:143], v[108:111], v[192:195]\n"
    "ds_read_b64_tr_b16 v[190:191], v253 offset:4904\n"
    "v_add_f32 v244, v244, v219\n"
    "v_mfma_f32_16x16x32_bf16 v[196:199], v[140:143], v[124:127], v[248:251]\n"
    "v_exp_f32 v223, v223\n"
    "v_mfma_f32_16x16x32_bf16 v[196:199], v[136:139], v[120:123], v[196:199]\n"
    "ds_read_b128 v[136:139], v252 offset:8768\n"
    "v_cvt_pk_bf16_f32 v239, v222, v223\n"
    "v_mfma_f32_16x16x32_bf16 v[196:199], v[132:135], v[116:119], v[196:199]\n"
    "v_exp_f32 v192, v192\n"
    "v_mfma_f32_16x16x32_bf16 v[196:199], v[128:131], v[112:115], v[196:199]\n"
    "ds_read_b128 v[128:131], v252 offset:8256\n"
    "ds_read_b128 v[132:135], v252 offset:8512\n"
    "v_add_f32 v245, v245, v220\n"
    "v_mfma_f32_16x16x32_bf16 a[128:131], v[160:163], v[232:235], a[128:131]\n"
    "v_exp_f32 v193, v193\n"
    "v_mfma_f32_16x16x32_bf16 a[160:163], v[160:163], v[236:239], a[160:163]\n"
    "ds_read_b128 v[140:143], v252 offset:9024\n"
    "v_cvt_pk_bf16_f32 v224, v192, v193\n"
    "v_mfma_f32_16x16x32_bf16 a[164:167], v[164:167], v[236:239], a[164:167]\n"
    "v_exp_f32 v194, v194\n"
    "v_mfma_f32_16x16x32_bf16 a[132:135], v[164:167], v[232:235], a[132:135]\n"
    "v_add_f32 v245, v245, v221\n"
    "v_add_f32 v246, v246, v192\n"
    "v_mfma_f32_16x16x32_bf16 a[136:139], v[168:171], v[232:235], a[136:139]\n"
    "v_exp_f32 v195, v195\n"
    "v_mfma_f32_16x16x32_bf16 a[168:171], v[168:171], v[236:239], a[168:171]\n"
    "v_cvt_pk_bf16_f32 v225, v194, v195\n"
    "v_add_f32 v245, v245, v222\n"
    "v_mfma_f32_16x16x32_bf16 a[172:175], v[172:175], v[236:239], a[172:175]\n"
    "v_exp_f32 v196, v196\n"
    "v_mfma_f32_16x16x32_bf16 a[140:143], v[172:175], v[232:235], a[140:143]\n"
    "v_add_f32 v245, v245, v223\n"
    "v_add_f32 v246, v246, v193\n"
    "v_mfma_f32_16x16x32_bf16 v[200:203], v[144:147], v[96:99], v[248:251]\n"
    "v_exp_f32 v197, v197\n"
    "v_mfma_f32_16x16x32_bf16 v[200:203], v[148:151], v[100:103], v[200:203]\n"
    "v_cvt_pk_bf16_f32 v228, v196, v197\n"
    "v_add_f32 v246, v246, v194\n"
    "v_mfma_f32_16x16x32_bf16 v[200:203], v[152:155], v[104:107], v[200:203]\n"
    "v_exp_f32 v198, v198\n"
    "v_mfma_f32_16x16x32_bf16 v[200:203], v[156:159], v[108:111], v[200:203]\n"
    "v_add_f32 v246, v246, v195\n"
    "v_add_f32 v247, v247, v196\n"
    "v_mfma_f32_16x16x32_bf16 v[204:207], v[156:159], v[124:127], v[248:251]\n"
    "v_exp_f32 v199, v199\n"
    "v_mfma_f32_16x16x32_bf16 v[204:207], v[152:155], v[120:123], v[204:207]\n"
    "ds_read_b128 v[152:155], v252 offset:12896\n"
    "v_cvt_pk_bf16_f32 v229, v198, v199\n"
    "v_mfma_f32_16x16x32_bf16 v[204:207], v[148:151], v[116:119], v[204:207]\n"
    "v_exp_f32 v200, v200\n"
    "v_mfma_f32_16x16x32_bf16 v[204:207], v[144:147], v[112:115], v[204:207]\n"
    "ds_read_b128 v[144:147], v252 offset:12384\n"
    "ds_read_b128 v[148:151], v252 offset:12640\n"
    "v_add_f32 v247, v247, v197\n"
    "v_mfma_f32_16x16x32_bf16 a[144:147], v[176:179], v[232:235], a[144:147]\n"
    "v_exp_f32 v201, v201\n"
    "v_mfma_f32_16x16x32_bf16 a[176:179], v[176:179], v[236:239], a[176:179]\n"
    "ds_read_b128 v[156:159], v252 offset:13152\n"
    "v_cvt_pk_bf16_f32 v226, v200, v201\n"
    "v_mfma_f32_16x16x32_bf16 a[180:183], v[180:183], v[236:239], a[180:183]\n"
    "v_exp_f32 v202, v202\n"
    "v_mfma_f32_16x16x32_bf16 a[148:151], v[180:183], v[232:235], a[148:151]\n"
    "v_add_f32 v247, v247, v198\n"
    "v_add_f32 v246, v246, v200\n"
    "v_mfma_f32_16x16x32_bf16 a[152:155], v[184:187], v[232:235], a[152:155]\n"
    "v_exp_f32 v203, v203\n"
    "v_mfma_f32_16x16x32_bf16 a[184:187], v[184:187], v[236:239], a[184:187]\n"
    "s_waitcnt lgkmcnt(8)\n"
    "v_cvt_pk_bf16_f32 v227, v202, v203\n"
    "v_add_f32 v247, v247, v199\n"
    "v_mfma_f32_16x16x32_bf16 a[188:191], v[188:191], v[236:239], a[188:191]\n"
    "v_exp_f32 v204, v204\n"
    "v_mfma_f32_16x16x32_bf16 a[156:159], v[188:191], v[232:235], a[156:159]\n"
    "s_waitcnt lgkmcnt(6)\n"
    "v_add_f32 v246, v246, v201\n"
    "v_add_f32 v247, v247, v204\n"
    "v_mfma_f32_16x16x32_bf16 v[208:211], v[128:131], v[96:99], v[248:251]\n"
    "s_waitcnt lgkmcnt(5)\n"
    "v_exp_f32 v205, v205\n"
    "v_mfma_f32_16x16x32_bf16 v[208:211], v[132:135], v[100:103], v[208:211]\n"
    "v_cvt_pk_bf16_f32 v230, v204, v205\n"
    "v_add_f32 v246, v246, v202\n"
    "v_add_f32 v247, v247, v205\n"
    "v_mfma_f32_16x16x32_bf16 v[208:211], v[136:139], v[104:107], v[208:211]\n"
    "s_waitcnt vmcnt(4) lgkmcnt(4)\n"
    "v_exp_f32 v206, v206\n"
    "v_mfma_f32_16x16x32_bf16 v[208:211], v[140:143], v[108:111], v[208:211]\n"
    "s_barrier\n"
    "v_add_f32 v246, v246, v203\n"
    "v_add_f32 v247, v247, v206\n"
    "v_mfma_f32_16x16x32_bf16 v[212:215], v[140:143], v[124:127], v[248:251]\n"
    "s_add_u32 s53, s66, 32768\n"
    "s_add_u32 m0, s54, 33024\n"
    "v_exp_f32 v207, v207\n"
    "buffer_load_dwordx4 v254, s[40:43], s53 offen nt lds\n"
    "v_mfma_f32_16x16x32_bf16 v[212:215], v[136:139], v[120:123], v[212:215]\n"
    "ds_read_b128 v[136:139], v252 offset:17024\n"
    "ds_read_b128 v[140:143], v252 offset:17280\n"
    "v_cvt_pk_bf16_f32 v231, v206, v207\n"
    "v_mfma_f32_16x16x32_bf16 v[212:215], v[132:135], v[116:119], v[212:215]\n"
    "v_exp_f32 v208, v208\n"
    "buffer_load_dwordx4 v254, s[40:43], s53 offen offset:1024 nt lds\n"
    "v_mfma_f32_16x16x32_bf16 v[212:215], v[128:131], v[112:115], v[212:215]\n"
    "ds_read_b128 v[128:131], v252 offset:16512\n"
    "ds_read_b128 v[132:135], v252 offset:16768\n"
    "v_add_f32 v247, v247, v207\n"
    "v_mfma_f32_16x16x32_bf16 a[192:195], v[160:163], v[224:227], 0\n"
    "s_add_u32 m0, s55, 90816\n"
    "v_exp_f32 v209, v209\n"
    "buffer_load_dwordx4 v254, s[44:47], s53 offen nt lds\n"
    "v_mfma_f32_16x16x32_bf16 a[224:227], v[160:163], v[228:231], 0\n"
    "ds_read_b64_tr_b16 v[160:161], v253 offset:8256\n"
    "ds_read_b64_tr_b16 v[162:163], v253 offset:12384\n"
    "v_cvt_pk_bf16_f32 v232, v208, v209\n"
    "v_mfma_f32_16x16x32_bf16 a[228:231], v[164:167], v[228:231], 0\n"
    "s_add_u32 m0, s55, 90824\n"
    "v_exp_f32 v210, v210\n"
    "buffer_load_dwordx4 v254, s[44:47], s53 offen offset:1024 nt lds\n"
    "v_mfma_f32_16x16x32_bf16 a[196:199], v[164:167], v[224:227], 0\n"
    "ds_read_b64_tr_b16 v[164:165], v253 offset:8264\n"
    "ds_read_b64_tr_b16 v[166:167], v253 offset:12392\n"
    "v_add_f32 v246, v246, v208\n"
    "v_mfma_f32_16x16x32_bf16 a[200:203], v[168:171], v[224:227], 0\n"
    "s_add_u32 s53, s66, 40960\n"
    "s_add_u32 m0, s54, 41280\n"
    "v_exp_f32 v211, v211\n"
    "buffer_load_dwordx4 v254, s[40:43], s53 offen nt lds\n"
    "v_mfma_f32_16x16x32_bf16 a[232:235], v[168:171], v[228:231], 0\n"
    "ds_read_b64_tr_b16 v[168:169], v253 offset:8512\n"
    "ds_read_b64_tr_b16 v[170:171], v253 offset:12640\n"
    "v_cvt_pk_bf16_f32 v233, v210, v211\n"
    "v_mfma_f32_16x16x32_bf16 a[236:239], v[172:175], v[228:231], 0\n"
    "v_exp_f32 v212, v212\n"
    "buffer_load_dwordx4 v254, s[40:43], s53 offen offset:1024 nt lds\n"
    "v_mfma_f32_16x16x32_bf16 a[204:207], v[172:175], v[224:227], 0\n"
    "s_waitcnt lgkmcnt(12)\n"
    "ds_read_b64_tr_b16 v[172:173], v253 offset:8520\n"
    "ds_read_b64_tr_b16 v[174:175], v253 offset:12648\n"
    "v_add_f32 v246, v246, v209\n"
    "v_mfma_f32_16x16x32_bf16 v[216:219], v[144:147], v[96:99], v[248:251]\n"
    "s_add_u32 m0, s55, 99072\n"
    "s_waitcnt lgkmcnt(13)\n"
    "v_exp_f32 v213, v213\n"
    "buffer_load_dwordx4 v254, s[44:47], s53 offen nt lds\n"
    "v_mfma_f32_16x16x32_bf16 v[216:219], v[148:151], v[100:103], v[216:219]\n"
    "v_cvt_pk_bf16_f32 v236, v212, v213\n"
    "v_add_f32 v246, v246, v210\n"
    "v_add_f32 v247, v247, v212\n"
    "v_mfma_f32_16x16x32_bf16 v[216:219], v[152:155], v[104:107], v[216:219]\n"
    "s_add_u32 m0, s55, 99080\n"
    "s_waitcnt lgkmcnt(12)\n"
    "v_exp_f32 v214, v214\n"
    "buffer_load_dwordx4 v254, s[44:47], s53 offen offset:1024 nt lds\n"
    "s_add_u32 s53, s53, 8192\n"
    "v_mfma_f32_16x16x32_bf16 v[216:219], v[156:159], v[108:111], v[216:219]\n"
    "v_add_f32 v246, v246, v211\n"
    "v_add_f32 v247, v247, v213\n"
    "v_add_f32 v247, v247, v214\n"
    "v_mfma_f32_16x16x32_bf16 v[220:223], v[156:159], v[124:127], v[248:251]\n"
    "v_exp_f32 v215, v215\n"
    "v_mfma_f32_16x16x32_bf16 v[220:223], v[152:155], v[120:123], v[220:223]\n"
    "ds_read_b128 v[152:155], v252 offset:21152\n"
    "ds_read_b128 v[156:159], v252 offset:21408\n"
    "v_cvt_pk_bf16_f32 v237, v214, v215\n"
    "v_mfma_f32_16x16x32_bf16 v[220:223], v[148:151], v[116:119], v[220:223]\n"
    "v_exp_f32 v216, v216\n"
    "v_mfma_f32_16x16x32_bf16 v[220:223], v[144:147], v[112:115], v[220:223]\n"
    "ds_read_b128 v[144:147], v252 offset:20640\n"
    "ds_read_b128 v[148:151], v252 offset:20896\n"
    "v_add_f32 v247, v247, v215\n"
    "v_mfma_f32_16x16x32_bf16 a[208:211], v[176:179], v[224:227], 0\n"
    "v_exp_f32 v217, v217\n"
    "v_mfma_f32_16x16x32_bf16 a[240:243], v[176:179], v[228:231], 0\n"
    "ds_read_b64_tr_b16 v[176:177], v253 offset:8768\n"
    "ds_read_b64_tr_b16 v[178:179], v253 offset:12896\n"
    "v_cvt_pk_bf16_f32 v234, v216, v217\n"
    "v_mfma_f32_16x16x32_bf16 a[244:247], v[180:183], v[228:231], 0\n"
    "v_exp_f32 v218, v218\n"
    "v_mfma_f32_16x16x32_bf16 a[212:215], v[180:183], v[224:227], 0\n"
    "ds_read_b64_tr_b16 v[180:181], v253 offset:8776\n"
    "ds_read_b64_tr_b16 v[182:183], v253 offset:12904\n"
    "v_add_f32 v246, v246, v216\n"
    "v_mfma_f32_16x16x32_bf16 a[216:219], v[184:187], v[224:227], 0\n"
    "v_exp_f32 v219, v219\n"
    "v_mfma_f32_16x16x32_bf16 a[248:251], v[184:187], v[228:231], 0\n"
    "ds_read_b64_tr_b16 v[184:185], v253 offset:9024\n"
    "ds_read_b64_tr_b16 v[186:187], v253 offset:13152\n"
    "v_cvt_pk_bf16_f32 v235, v218, v219\n"
    "v_mfma_f32_16x16x32_bf16 a[252:255], v[188:191], v[228:231], 0\n"
    "v_exp_f32 v220, v220\n"
    "v_mfma_f32_16x16x32_bf16 a[220:223], v[188:191], v[224:227], 0\n"
    "s_waitcnt lgkmcnt(15)\n"
    "ds_read_b64_tr_b16 v[188:189], v253 offset:9032\n"
    "ds_read_b64_tr_b16 v[190:191], v253 offset:13160\n"
    "v_add_f32 v246, v246, v217\n"
    "v_add_f32 v247, v247, v220\n"
    "v_mfma_f32_16x16x32_bf16 v[192:195], v[128:131], v[0:3], v[248:251]\n"
    "s_lshl_b32 s86, s52, 12\n"
    "v_exp_f32 v221, v221\n"
    "v_mfma_f32_16x16x32_bf16 v[192:195], v[132:135], v[4:7], v[192:195]\n"
    "v_cvt_pk_bf16_f32 v238, v220, v221\n"
    "v_add_f32 v246, v246, v218\n"
    "v_mfma_f32_16x16x32_bf16 v[192:195], v[136:139], v[8:11], v[192:195]\n"
    "v_exp_f32 v222, v222\n"
    "v_mfma_f32_16x16x32_bf16 v[192:195], v[140:143], v[12:15], v[192:195]\n"
    "v_add_f32 v246, v246, v219\n"
    "v_add_f32 v247, v247, v221\n"
    "v_mfma_f32_16x16x32_bf16 v[196:199], v[140:143], v[28:31], v[248:251]\n"
    "v_exp_f32 v223, v223\n"
    "v_mfma_f32_16x16x32_bf16 v[196:199], v[136:139], v[24:27], v[196:199]\n"
    "v_cvt_pk_bf16_f32 v239, v222, v223\n"
    "v_add_f32 v247, v247, v222\n"
    "v_mfma_f32_16x16x32_bf16 v[196:199], v[132:135], v[20:23], v[196:199]\n"
    "v_exp_f32 v192, v192\n"
    "v_mfma_f32_16x16x32_bf16 v[196:199], v[128:131], v[16:19], v[196:199]\n"
    "v_add_f32 v247, v247, v223\n"
    "v_add_f32 v240, v240, v192\n"
    "v_mfma_f32_16x16x32_bf16 a[192:195], v[160:163], v[232:235], a[192:195]\n"
    "s_add_u32 s87, s86, 16384\n"
    "v_exp_f32 v193, v193\n"
    "v_mfma_f32_16x16x32_bf16 a[224:227], v[160:163], v[236:239], a[224:227]\n"
    "s_waitcnt lgkmcnt(15)\n"
    "ds_read_b64_tr_b16 v[160:161], v253 offset:16512\n"
    "v_cvt_pk_bf16_f32 v224, v192, v193\n"
    "v_mfma_f32_16x16x32_bf16 a[228:231], v[164:167], v[236:239], a[228:231]\n"
    "s_add_u32 s88, s86, 32768\n"
    "v_exp_f32 v194, v194\n"
    "v_mfma_f32_16x16x32_bf16 a[196:199], v[164:167], v[232:235], a[196:199]\n"
    "s_waitcnt lgkmcnt(15)\n"
    "ds_read_b64_tr_b16 v[162:163], v253 offset:20640\n"
    "v_add_f32 v240, v240, v193\n"
    "v_mfma_f32_16x16x32_bf16 a[200:203], v[168:171], v[232:235], a[200:203]\n"
    "s_add_u32 s89, s86, 49152\n"
    "v_exp_f32 v195, v195\n"
    "v_mfma_f32_16x16x32_bf16 a[232:235], v[168:171], v[236:239], a[232:235]\n"
    "s_waitcnt lgkmcnt(14)\n"
    "ds_read_b64_tr_b16 v[164:165], v253 offset:16520\n"
    "v_cvt_pk_bf16_f32 v225, v194, v195\n"
    "v_mfma_f32_16x16x32_bf16 a[236:239], v[172:175], v[236:239], a[236:239]\n"
    "s_add_u32 s90, s86, 65536\n"
    "v_exp_f32 v196, v196\n"
    "v_mfma_f32_16x16x32_bf16 a[204:207], v[172:175], v[232:235], a[204:207]\n"
    "s_waitcnt lgkmcnt(12)\n"
    "ds_read_b64_tr_b16 v[166:167], v253 offset:20648\n"
    "v_add_f32 v240, v240, v194\n"
    "v_mfma_f32_16x16x32_bf16 v[200:203], v[144:147], v[0:3], v[248:251]\n"
    "s_waitcnt lgkmcnt(12)\n"
    "v_exp_f32 v197, v197\n"
    "v_mfma_f32_16x16x32_bf16 v[200:203], v[148:151], v[4:7], v[200:203]\n"
    "s_add_u32 s91, s86, 81920\n"
    "ds_read_b64_tr_b16 v[168:169], v253 offset:16768\n"
    "v_cvt_pk_bf16_f32 v228, v196, v197\n"
    "v_mfma_f32_16x16x32_bf16 v[200:203], v[152:155], v[8:11], v[200:203]\n"
    "s_add_u32 s92, s86, 98304\n"
    "v_exp_f32 v198, v198\n"
    "v_mfma_f32_16x16x32_bf16 v[200:203], v[156:159], v[12:15], v[200:203]\n"
    "s_add_u32 s93, s86, 114688\n"
    "ds_read_b64_tr_b16 v[170:171], v253 offset:20896\n"
    "v_add_f32 v240, v240, v195\n"
    "v_mfma_f32_16x16x32_bf16 v[204:207], v[156:159], v[28:31], v[248:251]\n"
    "s_mov_b32 s58, 4\n"
    "v_exp_f32 v199, v199\n"
    "v_mfma_f32_16x16x32_bf16 v[204:207], v[152:155], v[24:27], v[204:207]\n"
    "s_mov_b32 s62, 0x30800000\n"
    "ds_read_b64_tr_b16 v[172:173], v253 offset:16776\n"
    "v_cvt_pk_bf16_f32 v229, v198, v199\n"
    "v_mfma_f32_16x16x32_bf16 v[204:207], v[148:151], v[20:23], v[204:207]\n"
    "s_mov_b32 s63, 0x4e800000\n"
    "v_exp_f32 v200, v200\n"
    "v_mfma_f32_16x16x32_bf16 v[204:207], v[144:147], v[16:19], v[204:207]\n"
    "s_waitcnt lgkmcnt(13)\n"
    "ds_read_b64_tr_b16 v[174:175], v253 offset:20904\n"
    "v_add_f32 v241, v241, v196\n"
    "v_mfma_f32_16x16x32_bf16 a[208:211], v[176:179], v[232:235], a[208:211]\n"
    "v_exp_f32 v201, v201\n"
    "v_mfma_f32_16x16x32_bf16 a[240:243], v[176:179], v[236:239], a[240:243]\n"
    "s_waitcnt lgkmcnt(12)\n"
    "ds_read_b64_tr_b16 v[176:177], v253 offset:17024\n"
    "v_cvt_pk_bf16_f32 v226, v200, v201\n"
    "v_mfma_f32_16x16x32_bf16 a[244:247], v[180:183], v[236:239], a[244:247]\n"
    "v_exp_f32 v202, v202\n"
    "v_mfma_f32_16x16x32_bf16 a[212:215], v[180:183], v[232:235], a[212:215]\n"
    "s_waitcnt lgkmcnt(11)\n"
    "ds_read_b64_tr_b16 v[178:179], v253 offset:21152\n"
    "v_add_f32 v241, v241, v197\n"
    "v_mfma_f32_16x16x32_bf16 a[216:219], v[184:187], v[232:235], a[216:219]\n"
    "v_exp_f32 v203, v203\n"
    "v_mfma_f32_16x16x32_bf16 a[248:251], v[184:187], v[236:239], a[248:251]\n"
    "s_waitcnt lgkmcnt(10)\n"
    "ds_read_b64_tr_b16 v[180:181], v253 offset:17032\n"
    "v_cvt_pk_bf16_f32 v227, v202, v203\n"
    "v_mfma_f32_16x16x32_bf16 a[252:255], v[188:191], v[236:239], a[252:255]\n"
    "v_exp_f32 v204, v204\n"
    "v_mfma_f32_16x16x32_bf16 a[220:223], v[188:191], v[232:235], a[220:223]\n"
    "ds_read_b64_tr_b16 v[182:183], v253 offset:21160\n"
    "v_add_f32 v241, v241, v198\n"
    "v_mfma_f32_16x16x32_bf16 v[208:211], v[128:131], v[32:35], v[248:251]\n"
    "v_exp_f32 v205, v205\n"
    "v_mfma_f32_16x16x32_bf16 v[208:211], v[132:135], v[36:39], v[208:211]\n"
    "ds_read_b64_tr_b16 v[184:185], v253 offset:17280\n"
    "v_cvt_pk_bf16_f32 v230, v204, v205\n"
    "v_mfma_f32_16x16x32_bf16 v[208:211], v[136:139], v[40:43], v[208:211]\n"
    "v_exp_f32 v206, v206\n"
    "v_mfma_f32_16x16x32_bf16 v[208:211], v[140:143], v[44:47], v[208:211]\n"
    "ds_read_b64_tr_b16 v[186:187], v253 offset:21408\n"
    "v_add_f32 v241, v241, v199\n"
    "v_mfma_f32_16x16x32_bf16 v[212:215], v[140:143], v[60:63], v[248:251]\n"
    "v_exp_f32 v207, v207\n"
    "v_mfma_f32_16x16x32_bf16 v[212:215], v[136:139], v[56:59], v[212:215]\n"
    "ds_read_b64_tr_b16 v[188:189], v253 offset:17288\n"
    "v_cvt_pk_bf16_f32 v231, v206, v207\n"
    "v_mfma_f32_16x16x32_bf16 v[212:215], v[132:135], v[52:55], v[212:215]\n"
    "v_exp_f32 v208, v208\n"
    "v_mfma_f32_16x16x32_bf16 v[212:215], v[128:131], v[48:51], v[212:215]\n"
    "s_waitcnt lgkmcnt(13)\n"
    "ds_read_b64_tr_b16 v[190:191], v253 offset:21416\n"
    "v_add_f32 v240, v240, v200\n"
    "v_mfma_f32_16x16x32_bf16 a[0:3], v[160:163], v[224:227], a[0:3]\n"
    "v_exp_f32 v209, v209\n"
    "v_mfma_f32_16x16x32_bf16 a[32:35], v[160:163], v[228:231], a[32:35]\n"
    "s_waitcnt lgkmcnt(12)\n"
    "v_cvt_pk_bf16_f32 v232, v208, v209\n"
    "v_add_f32 v240, v240, v201\n"
    "v_mfma_f32_16x16x32_bf16 a[36:39], v[164:167], v[228:231], a[36:39]\n"
    "v_exp_f32 v210, v210\n"
    "v_mfma_f32_16x16x32_bf16 a[4:7], v[164:167], v[224:227], a[4:7]\n"
    "s_waitcnt lgkmcnt(10)\n"
    "v_add_f32 v240, v240, v202\n"
    "v_add_f32 v241, v241, v204\n"
    "v_mfma_f32_16x16x32_bf16 a[8:11], v[168:171], v[224:227], a[8:11]\n"
    "v_exp_f32 v211, v211\n"
    "v_mfma_f32_16x16x32_bf16 a[40:43], v[168:171], v[228:231], a[40:43]\n"
    "s_waitcnt lgkmcnt(8)\n"
    "v_cvt_pk_bf16_f32 v233, v210, v211\n"
    "v_add_f32 v240, v240, v203\n"
    "v_mfma_f32_16x16x32_bf16 a[44:47], v[172:175], v[228:231], a[44:47]\n"
    "s_add_u32 m0, s54, 0\n"
    "v_exp_f32 v212, v212\n"
    "v_mfma_f32_16x16x32_bf16 a[12:15], v[172:175], v[224:227], a[12:15]\n"
    "buffer_load_dwordx4 v254, s[40:43], s53 offen nt lds\n"
    "v_mfma_f32_16x16x32_bf16 v[216:219], v[144:147], v[32:35], v[248:251]\n"
    "v_exp_f32 v213, v213\n"
    "v_mfma_f32_16x16x32_bf16 v[216:219], v[148:151], v[36:39], v[216:219]\n"
    "v_cvt_pk_bf16_f32 v236, v212, v213\n"
    "v_add_f32 v241, v241, v205\n"
    "v_mfma_f32_16x16x32_bf16 v[216:219], v[152:155], v[40:43], v[216:219]\n"
    "v_exp_f32 v214, v214\n"
    "v_mfma_f32_16x16x32_bf16 v[216:219], v[156:159], v[44:47], v[216:219]\n"
    "v_add_f32 v241, v241, v206\n"
    "v_add_f32 v242, v242, v208\n"
    "v_mfma_f32_16x16x32_bf16 v[220:223], v[156:159], v[60:63], v[248:251]\n"
    "v_exp_f32 v215, v215\n"
    "v_mfma_f32_16x16x32_bf16 v[220:223], v[152:155], v[56:59], v[220:223]\n"
    "v_cvt_pk_bf16_f32 v237, v214, v215\n"
    "v_add_f32 v241, v241, v207\n"
    "v_mfma_f32_16x16x32_bf16 v[220:223], v[148:151], v[52:55], v[220:223]\n"
    "v_exp_f32 v216, v216\n"
    "v_mfma_f32_16x16x32_bf16 v[220:223], v[144:147], v[48:51], v[220:223]\n"
    "s_waitcnt lgkmcnt(6)\n"
    "v_add_f32 v242, v242, v209\n"
    "v_add_f32 v243, v243, v212\n"
    "v_mfma_f32_16x16x32_bf16 a[16:19], v[176:179], v[224:227], a[16:19]\n"
    "v_exp_f32 v217, v217\n"
    "v_mfma_f32_16x16x32_bf16 a[48:51], v[176:179], v[228:231], a[48:51]\n"
    "s_waitcnt lgkmcnt(4)\n"
    "v_cvt_pk_bf16_f32 v234, v216, v217\n"
    "v_add_f32 v242, v242, v210\n"
    "v_mfma_f32_16x16x32_bf16 a[52:55], v[180:183], v[228:231], a[52:55]\n"
    "v_exp_f32 v218, v218\n"
    "v_mfma_f32_16x16x32_bf16 a[20:23], v[180:183], v[224:227], a[20:23]\n"
    "s_waitcnt lgkmcnt(2)\n"
    "buffer_load_dwordx4 v254, s[40:43], s53 offen offset:1024 nt lds\n"
    "v_mfma_f32_16x16x32_bf16 a[24:27], v[184:187], v[224:227], a[24:27]\n"
    "v_exp_f32 v219, v219\n"
    "v_mfma_f32_16x16x32_bf16 a[56:59], v[184:187], v[228:231], a[56:59]\n"
    "s_waitcnt lgkmcnt(0)\n"
    "v_cvt_pk_bf16_f32 v235, v218, v219\n"
    "v_add_f32 v242, v242, v211\n"
    "v_mfma_f32_16x16x32_bf16 a[60:63], v[188:191], v[228:231], a[60:63]\n"
    "v_exp_f32 v220, v220\n"
    "v_mfma_f32_16x16x32_bf16 a[28:31], v[188:191], v[224:227], a[28:31]\n"
    "v_add_f32 v243, v243, v213\n"
    "v_add_f32 v242, v242, v216\n"
    "v_mfma_f32_16x16x32_bf16 v[192:195], v[128:131], v[64:67], v[248:251]\n"
    "v_exp_f32 v221, v221\n"
    "v_mfma_f32_16x16x32_bf16 v[192:195], v[132:135], v[68:71], v[192:195]\n"
    "v_cvt_pk_bf16_f32 v238, v220, v221\n"
    "v_add_f32 v243, v243, v214\n"
    "v_mfma_f32_16x16x32_bf16 v[192:195], v[136:139], v[72:75], v[192:195]\n"
    "v_exp_f32 v222, v222\n"
    "v_mfma_f32_16x16x32_bf16 v[192:195], v[140:143], v[76:79], v[192:195]\n"
    "v_add_f32 v243, v243, v215\n"
    "v_add_f32 v242, v242, v217\n"
    "v_mfma_f32_16x16x32_bf16 v[196:199], v[140:143], v[92:95], v[248:251]\n"
    "v_exp_f32 v223, v223\n"
    "v_mfma_f32_16x16x32_bf16 v[196:199], v[136:139], v[88:91], v[196:199]\n"
    "v_cvt_pk_bf16_f32 v239, v222, v223\n"
    "v_add_f32 v242, v242, v218\n"
    "v_mfma_f32_16x16x32_bf16 v[196:199], v[132:135], v[84:87], v[196:199]\n"
    "v_exp_f32 v192, v192\n"
    "v_mfma_f32_16x16x32_bf16 v[196:199], v[128:131], v[80:83], v[196:199]\n"
    "v_add_f32 v242, v242, v219\n"
    "v_add_f32 v243, v243, v220\n"
    "v_mfma_f32_16x16x32_bf16 a[64:67], v[160:163], v[232:235], a[64:67]\n"
    "v_exp_f32 v193, v193\n"
    "v_mfma_f32_16x16x32_bf16 a[96:99], v[160:163], v[236:239], a[96:99]\n"
    "v_cvt_pk_bf16_f32 v224, v192, v193\n"
    "v_add_f32 v243, v243, v221\n"
    "v_mfma_f32_16x16x32_bf16 a[100:103], v[164:167], v[236:239], a[100:103]\n"
    "s_add_u32 m0, s55, 57792\n"
    "v_exp_f32 v194, v194\n"
    "v_mfma_f32_16x16x32_bf16 a[68:71], v[164:167], v[232:235], a[68:71]\n"
    "buffer_load_dwordx4 v254, s[44:47], s53 offen nt lds\n"
    "v_mfma_f32_16x16x32_bf16 a[72:75], v[168:171], v[232:235], a[72:75]\n"
    "v_exp_f32 v195, v195\n"
    "v_mfma_f32_16x16x32_bf16 a[104:107], v[168:171], v[236:239], a[104:107]\n"
    "v_cvt_pk_bf16_f32 v225, v194, v195\n"
    "v_add_f32 v243, v243, v222\n"
    "v_mfma_f32_16x16x32_bf16 a[108:111], v[172:175], v[236:239], a[108:111]\n"
    "v_exp_f32 v196, v196\n"
    "v_mfma_f32_16x16x32_bf16 a[76:79], v[172:175], v[232:235], a[76:79]\n"
    "v_add_f32 v243, v243, v223\n"
    "v_add_f32 v244, v244, v192\n"
    "v_mfma_f32_16x16x32_bf16 v[200:203], v[144:147], v[64:67], v[248:251]\n"
    "v_exp_f32 v197, v197\n"
    "v_mfma_f32_16x16x32_bf16 v[200:203], v[148:151], v[68:71], v[200:203]\n"
    "v_cvt_pk_bf16_f32 v228, v196, v197\n"
    "v_add_f32 v244, v244, v193\n"
    "v_mfma_f32_16x16x32_bf16 v[200:203], v[152:155], v[72:75], v[200:203]\n"
    "v_exp_f32 v198, v198\n"
    "v_mfma_f32_16x16x32_bf16 v[200:203], v[156:159], v[76:79], v[200:203]\n"
    "v_add_f32 v244, v244, v194\n"
    "v_add_f32 v245, v245, v196\n"
    "v_mfma_f32_16x16x32_bf16 v[204:207], v[156:159], v[92:95], v[248:251]\n"
    "v_exp_f32 v199, v199\n"
    "v_mfma_f32_16x16x32_bf16 v[204:207], v[152:155], v[88:91], v[204:207]\n"
    "v_cvt_pk_bf16_f32 v229, v198, v199\n"
    "v_add_f32 v244, v244, v195\n"
    "v_mfma_f32_16x16x32_bf16 v[204:207], v[148:151], v[84:87], v[204:207]\n"
    "v_exp_f32 v200, v200\n"
    "v_mfma_f32_16x16x32_bf16 v[204:207], v[144:147], v[80:83], v[204:207]\n"
    "v_add_f32 v245, v245, v197\n"
    "v_add_f32 v244, v244, v200\n"
    "v_mfma_f32_16x16x32_bf16 a[80:83], v[176:179], v[232:235], a[80:83]\n"
    "v_exp_f32 v201, v201\n"
    "v_mfma_f32_16x16x32_bf16 a[112:115], v[176:179], v[236:239], a[112:115]\n"
    "v_cvt_pk_bf16_f32 v226, v200, v201\n"
    "v_add_f32 v245, v245, v198\n"
    "v_mfma_f32_16x16x32_bf16 a[116:119], v[180:183], v[236:239], a[116:119]\n"
    "s_add_u32 m0, s55, 57800\n"
    "v_exp_f32 v202, v202\n"
    "v_mfma_f32_16x16x32_bf16 a[84:87], v[180:183], v[232:235], a[84:87]\n"
    "buffer_load_dwordx4 v254, s[44:47], s53 offen offset:1024 nt lds\n"
    "v_mfma_f32_16x16x32_bf16 a[88:91], v[184:187], v[232:235], a[88:91]\n"
    "s_add_u32 s53, s53, 8192\n"
    "v_exp_f32 v203, v203\n"
    "v_mfma_f32_16x16x32_bf16 a[120:123], v[184:187], v[236:239], a[120:123]\n"
    "v_cvt_pk_bf16_f32 v227, v202, v203\n"
    "v_add_f32 v245, v245, v199\n"
    "v_mfma_f32_16x16x32_bf16 a[124:127], v[188:191], v[236:239], a[124:127]\n"
    "v_exp_f32 v204, v204\n"
    "v_mfma_f32_16x16x32_bf16 a[92:95], v[188:191], v[232:235], a[92:95]\n"
    "v_add_f32 v244, v244, v201\n"
    "v_add_f32 v245, v245, v204\n"
    "v_mfma_f32_16x16x32_bf16 v[208:211], v[128:131], v[96:99], v[248:251]\n"
    "v_exp_f32 v205, v205\n"
    "v_mfma_f32_16x16x32_bf16 v[208:211], v[132:135], v[100:103], v[208:211]\n"
    "v_cvt_pk_bf16_f32 v230, v204, v205\n"
    "v_add_f32 v244, v244, v202\n"
    "v_mfma_f32_16x16x32_bf16 v[208:211], v[136:139], v[104:107], v[208:211]\n"
    "s_waitcnt vmcnt(8)\n"
    "v_exp_f32 v206, v206\n"
    "v_mfma_f32_16x16x32_bf16 v[208:211], v[140:143], v[108:111], v[208:211]\n"
    "s_barrier\n"
    "v_add_f32 v244, v244, v203\n"
    "v_add_f32 v245, v245, v205\n"
    "v_mfma_f32_16x16x32_bf16 v[212:215], v[140:143], v[124:127], v[248:251]\n"
    "v_exp_f32 v207, v207\n"
    "v_mfma_f32_16x16x32_bf16 v[212:215], v[136:139], v[120:123], v[212:215]\n"
    "v_cvt_pk_bf16_f32 v231, v206, v207\n"
    "v_add_f32 v245, v245, v206\n"
    "v_mfma_f32_16x16x32_bf16 v[212:215], v[132:135], v[116:119], v[212:215]\n"
    "v_exp_f32 v208, v208\n"
    "v_mfma_f32_16x16x32_bf16 v[212:215], v[128:131], v[112:115], v[212:215]\n"
    "ds_read_b128 v[140:143], v252 offset:25536\n"
    "v_add_f32 v245, v245, v207\n"
    "v_mfma_f32_16x16x32_bf16 a[128:131], v[160:163], v[224:227], a[128:131]\n"
    "v_exp_f32 v209, v209\n"
    "v_mfma_f32_16x16x32_bf16 a[160:163], v[160:163], v[228:231], a[160:163]\n"
    "ds_read_b128 v[136:139], v252 offset:25280\n"
    "v_cvt_pk_bf16_f32 v232, v208, v209\n"
    "v_mfma_f32_16x16x32_bf16 a[164:167], v[164:167], v[228:231], a[164:167]\n"
    "v_exp_f32 v210, v210\n"
    "v_mfma_f32_16x16x32_bf16 a[132:135], v[164:167], v[224:227], a[132:135]\n"
    "ds_read_b128 v[132:135], v252 offset:25024\n"
    "v_add_f32 v246, v246, v208\n"
    "v_mfma_f32_16x16x32_bf16 a[136:139], v[168:171], v[224:227], a[136:139]\n"
    "v_exp_f32 v211, v211\n"
    "v_mfma_f32_16x16x32_bf16 a[168:171], v[168:171], v[228:231], a[168:171]\n"
    "ds_read_b128 v[128:131], v252 offset:24768\n"
    "v_cvt_pk_bf16_f32 v233, v210, v211\n"
    "v_mfma_f32_16x16x32_bf16 a[172:175], v[172:175], v[228:231], a[172:175]\n"
    "v_exp_f32 v212, v212\n"
    "v_mfma_f32_16x16x32_bf16 a[140:143], v[172:175], v[224:227], a[140:143]\n"
    "v_add_f32 v246, v246, v209\n"
    "v_add_f32 v247, v247, v212\n"
    "v_mfma_f32_16x16x32_bf16 v[216:219], v[144:147], v[96:99], v[248:251]\n"
    "v_exp_f32 v213, v213\n"
    "v_mfma_f32_16x16x32_bf16 v[216:219], v[148:151], v[100:103], v[216:219]\n"
    "v_cvt_pk_bf16_f32 v236, v212, v213\n"
    "v_add_f32 v246, v246, v210\n"
    "v_mfma_f32_16x16x32_bf16 v[216:219], v[152:155], v[104:107], v[216:219]\n"
    "v_exp_f32 v214, v214\n"
    "v_mfma_f32_16x16x32_bf16 v[216:219], v[156:159], v[108:111], v[216:219]\n"
    "v_add_f32 v246, v246, v211\n"
    "v_add_f32 v247, v247, v213\n"
    "v_mfma_f32_16x16x32_bf16 v[220:223], v[156:159], v[124:127], v[248:251]\n"
    "v_exp_f32 v215, v215\n"
    "v_mfma_f32_16x16x32_bf16 v[220:223], v[152:155], v[120:123], v[220:223]\n"
    "v_cvt_pk_bf16_f32 v237, v214, v215\n"
    "v_add_f32 v247, v247, v214\n"
    "v_mfma_f32_16x16x32_bf16 v[220:223], v[148:151], v[116:119], v[220:223]\n"
    "v_exp_f32 v216, v216\n"
    "v_mfma_f32_16x16x32_bf16 v[220:223], v[144:147], v[112:115], v[220:223]\n"
    "ds_read_b128 v[156:159], v252 offset:29664\n"
    "v_add_f32 v247, v247, v215\n"
    "v_mfma_f32_16x16x32_bf16 a[144:147], v[176:179], v[224:227], a[144:147]\n"
    "v_exp_f32 v217, v217\n"
    "v_mfma_f32_16x16x32_bf16 a[176:179], v[176:179], v[228:231], a[176:179]\n"
    "ds_read_b128 v[152:155], v252 offset:29408\n"
    "v_cvt_pk_bf16_f32 v234, v216, v217\n"
    "v_mfma_f32_16x16x32_bf16 a[180:183], v[180:183], v[228:231], a[180:183]\n"
    "v_exp_f32 v218, v218\n"
    "v_mfma_f32_16x16x32_bf16 a[148:151], v[180:183], v[224:227], a[148:151]\n"
    "ds_read_b128 v[148:151], v252 offset:29152\n"
    "v_add_f32 v246, v246, v216\n"
    "v_mfma_f32_16x16x32_bf16 a[152:155], v[184:187], v[224:227], a[152:155]\n"
    "v_exp_f32 v219, v219\n"
    "v_mfma_f32_16x16x32_bf16 a[184:187], v[184:187], v[228:231], a[184:187]\n"
    "ds_read_b128 v[144:147], v252 offset:28896\n"
    "v_cvt_pk_bf16_f32 v235, v218, v219\n"
    "v_mfma_f32_16x16x32_bf16 a[188:191], v[188:191], v[228:231], a[188:191]\n"
    "v_exp_f32 v220, v220\n"
    "v_mfma_f32_16x16x32_bf16 a[156:159], v[188:191], v[224:227], a[156:159]\n"
    "s_cmp_eq_u32 s52, 3\n"
    "s_cbranch_scc1 L_e3_%=\n"
    ".p2align 3\n"
    "s_nop 0\n"
    "L_loop_%=:\n"
    "s_waitcnt lgkmcnt(4)\n"
    "v_add_f32 v246, v246, v217\n"
    "v_add_f32 v247, v247, v220\n"
    "v_mfma_f32_16x16x32_bf16 v[192:195], v[128:131], v[0:3], v[248:251]\n"
    "v_exp_f32 v221, v221\n"
    "v_mfma_f32_16x16x32_bf16 v[192:195], v[132:135], v[4:7], v[192:195]\n"
    "v_cvt_pk_bf16_f32 v238, v220, v221\n"
    "v_add_f32 v246, v246, v218\n"
    "v_mfma_f32_16x16x32_bf16 v[192:195], v[136:139], v[8:11], v[192:195]\n"
    "v_exp_f32 v222, v222\n"
    "v_mfma_f32_16x16x32_bf16 v[192:195], v[140:143], v[12:15], v[192:195]\n"
    "v_add_f32 v246, v246, v219\n"
    "v_add_f32 v247, v247, v221\n"
    "v_mfma_f32_16x16x32_bf16 v[196:199], v[140:143], v[28:31], v[248:251]\n"
    "v_exp_f32 v223, v223\n"
    "v_mfma_f32_16x16x32_bf16 v[196:199], v[136:139], v[24:27], v[196:199]\n"
    "v_cvt_pk_bf16_f32 v239, v222, v223\n"
    "v_add_f32 v247, v247, v222\n"
    "v_mfma_f32_16x16x32_bf16 v[196:199], v[132:135], v[20:23], v[196:199]\n"
    "v_exp_f32 v192, v192\n"
    "v_mfma_f32_16x16x32_bf16 v[196:199], v[128:131], v[16:19], v[196:199]\n"
    "v_add_f32 v247, v247, v223\n"
    "v_add_f32 v240, v240, v192\n"
    "v_mfma_f32_16x16x32_bf16 a[192:195], v[160:163], v[232:235], a[192:195]\n"
    "v_exp_f32 v193, v193\n"
    "v_mfma_f32_16x16x32_bf16 a[224:227], v[160:163], v[236:239], a[224:227]\n"
    "ds_read_b64_tr_b16 v[160:161], v253 offset:24768\n"
    "v_cvt_pk_bf16_f32 v224, v192, v193\n"
    "v_mfma_f32_16x16x32_bf16 a[228:231], v[164:167], v[236:239], a[228:231]\n"
    "v_exp_f32 v194, v194\n"
    "v_mfma_f32_16x16x32_bf16 a[196:199], v[164:167], v[232:235], a[196:199]\n"
    "ds_read_b64_tr_b16 v[162:163], v253 offset:28896\n"
    "v_add_f32 v240, v240, v193\n"
    "v_mfma_f32_16x16x32_bf16 a[200:203], v[168:171], v[232:235], a[200:203]\n"
    "v_exp_f32 v195, v195\n"
    "v_mfma_f32_16x16x32_bf16 a[232:235], v[168:171], v[236:239], a[232:235]\n"
    "ds_read_b64_tr_b16 v[164:165], v253 offset:24776\n"
    "v_cvt_pk_bf16_f32 v225, v194, v195\n"
    "v_mfma_f32_16x16x32_bf16 a[236:239], v[172:175], v[236:239], a[236:239]\n"
    "v_exp_f32 v196, v196\n"
    "v_mfma_f32_16x16x32_bf16 a[204:207], v[172:175], v[232:235], a[204:207]\n"
    "s_waitcnt lgkmcnt(3)\n"
    "ds_read_b64_tr_b16 v[166:167], v253 offset:28904\n"
    "v_add_f32 v240, v240, v194\n"
    "v_mfma_f32_16x16x32_bf16 v[200:203], v[144:147], v[0:3], v[248:251]\n"
    "v_exp_f32 v197, v197\n"
    "v_mfma_f32_16x16x32_bf16 v[200:203], v[148:151], v[4:7], v[200:203]\n"
    "ds_read_b64_tr_b16 v[168:169], v253 offset:25024\n"
    "v_cvt_pk_bf16_f32 v228, v196, v197\n"
    "v_mfma_f32_16x16x32_bf16 v[200:203], v[152:155], v[8:11], v[200:203]\n"
    "v_exp_f32 v198, v198\n"
    "v_mfma_f32_16x16x32_bf16 v[200:203], v[156:159], v[12:15], v[200:203]\n"
    "ds_read_b64_tr_b16 v[170:171], v253 offset:29152\n"
    "v_add_f32 v240, v240, v195\n"
    "v_mfma_f32_16x16x32_bf16 v[204:207], v[156:159], v[28:31], v[248:251]\n"
    "v_exp_f32 v199, v199\n"
    "v_mfma_f32_16x16x32_bf16 v[204:207], v[152:155], v[24:27], v[204:207]\n"
    "ds_read_b64_tr_b16 v[172:173], v253 offset:25032\n"
    "v_cvt_pk_bf16_f32 v229, v198, v199\n"
    "v_mfma_f32_16x16x32_bf16 v[204:207], v[148:151], v[20:23], v[204:207]\n"
    "v_exp_f32 v200, v200\n"
    "v_mfma_f32_16x16x32_bf16 v[204:207], v[144:147], v[16:19], v[204:207]\n"
    "ds_read_b64_tr_b16 v[174:175], v253 offset:29160\n"
    "v_add_f32 v241, v241, v196\n"
    "v_mfma_f32_16x16x32_bf16 a[208:211], v[176:179], v[232:235], a[208:211]\n"
    "v_exp_f32 v201, v201\n"
    "v_mfma_f32_16x16x32_bf16 a[240:243], v[176:179], v[236:239], a[240:243]\n"
    "ds_read_b64_tr_b16 v[176:177], v253 offset:25280\n"
    "v_cvt_pk_bf16_f32 v226, v200, v201\n"
    "v_mfma_f32_16x16x32_bf16 a[244:247], v[180:183], v[236:239], a[244:247]\n"
    "v_exp_f32 v202, v202\n"
    "v_mfma_f32_16x16x32_bf16 a[212:215], v[180:183], v[232:235], a[212:215]\n"
    "ds_read_b64_tr_b16 v[178:179], v253 offset:29408\n"
    "v_add_f32 v241, v241, v197\n"
    "v_mfma_f32_16x16x32_bf16 a[216:219], v[184:187], v[232:235], a[216:219]\n"
    "v_exp_f32 v203, v203\n"
    "v_mfma_f32_16x16x32_bf16 a[248:251], v[184:187], v[236:239], a[248:251]\n"
    "ds_read_b64_tr_b16 v[180:181], v253 offset:25288\n"
    "v_cvt_pk_bf16_f32 v227, v202, v203\n"
    "v_mfma_f32_16x16x32_bf16 a[252:255], v[188:191], v[236:239], a[252:255]\n"
    "v_exp_f32 v204, v204\n"
    "v_mfma_f32_16x16x32_bf16 a[220:223], v[188:191], v[232:235], a[220:223]\n"
    "ds_read_b64_tr_b16 v[182:183], v253 offset:29416\n"
    "v_add_f32 v241, v241, v198\n"
    "v_mfma_f32_16x16x32_bf16 v[208:211], v[128:131], v[32:35], v[248:251]\n"
    "v_exp_f32 v205, v205\n"
    "v_mfma_f32_16x16x32_bf16 v[208:211], v[132:135], v[36:39], v[208:211]\n"
    "ds_read_b64_tr_b16 v[184:185], v253 offset:25536\n"
    "v_cvt_pk_bf16_f32 v230, v204, v205\n"
    "v_mfma_f32_16x16x32_bf16 v[208:211], v[136:139], v[40:43], v[208:211]\n"
    "v_exp_f32 v206, v206\n"
    "v_mfma_f32_16x16x32_bf16 v[208:211], v[140:143], v[44:47], v[208:211]\n"
    "ds_read_b64_tr_b16 v[186:187], v253 offset:29664\n"
    "v_add_f32 v241, v241, v199\n"
    "v_mfma_f32_16x16x32_bf16 v[212:215], v[140:143], v[60:63], v[248:251]\n"
    "v_exp_f32 v207, v207\n"
    "v_mfma_f32_16x16x32_bf16 v[212:215], v[136:139], v[56:59], v[212:215]\n"
    "ds_read_b64_tr_b16 v[188:189], v253 offset:25544\n"
    "v_cvt_pk_bf16_f32 v231, v206, v207\n"
    "v_mfma_f32_16x16x32_bf16 v[212:215], v[132:135], v[52:55], v[212:215]\n"
    "v_exp_f32 v208, v208\n"
    "v_mfma_f32_16x16x32_bf16 v[212:215], v[128:131], v[48:51], v[212:215]\n"
    "s_waitcnt lgkmcnt(13)\n"
    "ds_read_b64_tr_b16 v[190:191], v253 offset:29672\n"
    "v_add_f32 v240, v240, v200\n"
    "v_mfma_f32_16x16x32_bf16 a[0:3], v[160:163], v[224:227], a[0:3]\n"
    "v_exp_f32 v209, v209\n"
    "v_mfma_f32_16x16x32_bf16 a[32:35], v[160:163], v[228:231], a[32:35]\n"
    "s_waitcnt lgkmcnt(12)\n"
    "v_cvt_pk_bf16_f32 v232, v208, v209\n"
    "v_add_f32 v240, v240, v201\n"
    "v_mfma_f32_16x16x32_bf16 a[36:39], v[164:167], v[228:231], a[36:39]\n"
    "v_exp_f32 v210, v210\n"
    "v_mfma_f32_16x16x32_bf16 a[4:7], v[164:167], v[224:227], a[4:7]\n"
    "s_waitcnt lgkmcnt(10)\n"
    "v_add_f32 v240, v240, v202\n"
    "v_add_f32 v241, v241, v204\n"
    "v_mfma_f32_16x16x32_bf16 a[8:11], v[168:171], v[224:227], a[8:11]\n"
    "v_exp_f32 v211, v211\n"
    "v_mfma_f32_16x16x32_bf16 a[40:43], v[168:171], v[228:231], a[40:43]\n"
    "s_waitcnt lgkmcnt(8)\n"
    "v_cvt_pk_bf16_f32 v233, v210, v211\n"
    "v_add_f32 v240, v240, v203\n"
    "v_mfma_f32_16x16x32_bf16 a[44:47], v[172:175], v[228:231], a[44:47]\n"
    "s_add_u32 m0, s54, 8256\n"
    "v_exp_f32 v212, v212\n"
    "v_mfma_f32_16x16x32_bf16 a[12:15], v[172:175], v[224:227], a[12:15]\n"
    "buffer_load_dwordx4 v254, s[40:43], s53 offen nt lds\n"
    "v_mfma_f32_16x16x32_bf16 v[216:219], v[144:147], v[32:35], v[248:251]\n"
    "v_exp_f32 v213, v213\n"
    "v_mfma_f32_16x16x32_bf16 v[216:219], v[148:151], v[36:39], v[216:219]\n"
    "v_cvt_pk_bf16_f32 v236, v212, v213\n"
    "v_add_f32 v241, v241, v205\n"
    "v_mfma_f32_16x16x32_bf16 v[216:219], v[152:155], v[40:43], v[216:219]\n"
    "v_exp_f32 v214, v214\n"
    "v_mfma_f32_16x16x32_bf16 v[216:219], v[156:159], v[44:47], v[216:219]\n"
    "v_add_f32 v241, v241, v206\n"
    "v_add_f32 v242, v242, v208\n"
    "v_mfma_f32_16x16x32_bf16 v[220:223], v[156:159], v[60:63], v[248:251]\n"
    "v_exp_f32 v215, v215\n"
    "v_mfma_f32_16x16x32_bf16 v[220:223], v[152:155], v[56:59], v[220:223]\n"
    "v_cvt_pk_bf16_f32 v237, v214, v215\n"
    "v_add_f32 v241, v241, v207\n"
    "v_mfma_f32_16x16x32_bf16 v[220:223], v[148:151], v[52:55], v[220:223]\n"
    "v_exp_f32 v216, v216\n"
    "v_mfma_f32_16x16x32_bf16 v[220:223], v[144:147], v[48:51], v[220:223]\n"
    "s_waitcnt lgkmcnt(6)\n"
    "v_add_f32 v242, v242, v209\n"
    "v_add_f32 v243, v243, v212\n"
    "v_mfma_f32_16x16x32_bf16 a[16:19], v[176:179], v[224:227], a[16:19]\n"
    "v_exp_f32 v217, v217\n"
    "v_mfma_f32_16x16x32_bf16 a[48:51], v[176:179], v[228:231], a[48:51]\n"
    "s_waitcnt lgkmcnt(4)\n"
    "v_cvt_pk_bf16_f32 v234, v216, v217\n"
    "v_add_f32 v242, v242, v210\n"
    "v_mfma_f32_16x16x32_bf16 a[52:55], v[180:183], v[228:231], a[52:55]\n"
    "v_exp_f32 v218, v218\n"
    "v_mfma_f32_16x16x32_bf16 a[20:23], v[180:183], v[224:227], a[20:23]\n"
    "s_waitcnt lgkmcnt(2)\n"
    "buffer_load_dwordx4 v254, s[40:43], s53 offen offset:1024 nt lds\n"
    "v_mfma_f32_16x16x32_bf16 a[24:27], v[184:187], v[224:227], a[24:27]\n"
    "v_exp_f32 v219, v219\n"
    "v_mfma_f32_16x16x32_bf16 a[56:59], v[184:187], v[228:231], a[56:59]\n"
    "s_waitcnt lgkmcnt(0)\n"
    "v_cvt_pk_bf16_f32 v235, v218, v219\n"
    "v_add_f32 v242, v242, v211\n"
    "v_mfma_f32_16x16x32_bf16 a[60:63], v[188:191], v[228:231], a[60:63]\n"
    "v_exp_f32 v220, v220\n"
    "v_mfma_f32_16x16x32_bf16 a[28:31], v[188:191], v[224:227], a[28:31]\n"
    "v_add_f32 v243, v243, v213\n"
    "v_add_f32 v242, v242, v216\n"
    "v_mfma_f32_16x16x32_bf16 v[192:195], v[128:131], v[64:67], v[248:251]\n"
    "v_exp_f32 v221, v221\n"
    "v_mfma_f32_16x16x32_bf16 v[192:195], v[132:135], v[68:71], v[192:195]\n"
    "v_cvt_pk_bf16_f32 v238, v220, v221\n"
    "v_add_f32 v243, v243, v214\n"
    "v_mfma_f32_16x16x32_bf16 v[192:195], v[136:139], v[72:75], v[192:195]\n"
    "v_exp_f32 v222, v222\n"
    "v_mfma_f32_16x16x32_bf16 v[192:195], v[140:143], v[76:79], v[192:195]\n"
    "v_add_f32 v243, v243, v215\n"
    "v_add_f32 v242, v242, v217\n"
    "v_mfma_f32_16x16x32_bf16 v[196:199], v[140:143], v[92:95], v[248:251]\n"
    "v_exp_f32 v223, v223\n"
    "v_mfma_f32_16x16x32_bf16 v[196:199], v[136:139], v[88:91], v[196:199]\n"
    "v_cvt_pk_bf16_f32 v239, v222, v223\n"
    "v_add_f32 v242, v242, v218\n"
    "v_mfma_f32_16x16x32_bf16 v[196:199], v[132:135], v[84:87], v[196:199]\n"
    "v_exp_f32 v192, v192\n"
    "v_mfma_f32_16x16x32_bf16 v[196:199], v[128:131], v[80:83], v[196:199]\n"
    "v_add_f32 v242, v242, v219\n"
    "v_add_f32 v243, v243, v220\n"
    "v_mfma_f32_16x16x32_bf16 a[64:67], v[160:163], v[232:235], a[64:67]\n"
    "v_exp_f32 v193, v193\n"
    "v_mfma_f32_16x16x32_bf16 a[96:99], v[160:163], v[236:239], a[96:99]\n"
    "v_cvt_pk_bf16_f32 v224, v192, v193\n"
    "v_add_f32 v243, v243, v221\n"
    "v_mfma_f32_16x16x32_bf16 a[100:103], v[164:167], v[236:239], a[100:103]\n"
    "s_add_u32 m0, s55, 66048\n"
    "v_exp_f32 v194, v194\n"
    "v_mfma_f32_16x16x32_bf16 a[68:71], v[164:167], v[232:235], a[68:71]\n"
    "buffer_load_dwordx4 v254, s[44:47], s53 offen nt lds\n"
    "v_mfma_f32_16x16x32_bf16 a[72:75], v[168:171], v[232:235], a[72:75]\n"
    "v_exp_f32 v195, v195\n"
    "v_mfma_f32_16x16x32_bf16 a[104:107], v[168:171], v[236:239], a[104:107]\n"
    "v_cvt_pk_bf16_f32 v225, v194, v195\n"
    "v_add_f32 v243, v243, v222\n"
    "v_mfma_f32_16x16x32_bf16 a[108:111], v[172:175], v[236:239], a[108:111]\n"
    "v_exp_f32 v196, v196\n"
    "v_mfma_f32_16x16x32_bf16 a[76:79], v[172:175], v[232:235], a[76:79]\n"
    "v_add_f32 v243, v243, v223\n"
    "v_add_f32 v244, v244, v192\n"
    "v_mfma_f32_16x16x32_bf16 v[200:203], v[144:147], v[64:67], v[248:251]\n"
    "v_exp_f32 v197, v197\n"
    "v_mfma_f32_16x16x32_bf16 v[200:203], v[148:151], v[68:71], v[200:203]\n"
    "v_cvt_pk_bf16_f32 v228, v196, v197\n"
    "v_add_f32 v244, v244, v193\n"
    "v_mfma_f32_16x16x32_bf16 v[200:203], v[152:155], v[72:75], v[200:203]\n"
    "v_exp_f32 v198, v198\n"
    "v_mfma_f32_16x16x32_bf16 v[200:203], v[156:159], v[76:79], v[200:203]\n"
    "v_add_f32 v244, v244, v194\n"
    "v_add_f32 v245, v245, v196\n"
    "v_mfma_f32_16x16x32_bf16 v[204:207], v[156:159], v[92:95], v[248:251]\n"
    "v_exp_f32 v199, v199\n"
    "v_mfma_f32_16x16x32_bf16 v[204:207], v[152:155], v[88:91], v[204:207]\n"
    "v_cvt_pk_bf16_f32 v229, v198, v199\n"
    "v_add_f32 v244, v244, v195\n"
    "v_mfma_f32_16x16x32_bf16 v[204:207], v[148:151], v[84:87], v[204:207]\n"
    "v_exp_f32 v200, v200\n"
    "v_mfma_f32_16x16x32_bf16 v[204:207], v[144:147], v[80:83], v[204:207]\n"
    "v_add_f32 v245, v245, v197\n"
    "v_add_f32 v244, v244, v200\n"
    "v_mfma_f32_16x16x32_bf16 a[80:83], v[176:179], v[232:235], a[80:83]\n"
    "v_exp_f32 v201, v201\n"
    "v_mfma_f32_16x16x32_bf16 a[112:115], v[176:179], v[236:239], a[112:115]\n"
    "v_cvt_pk_bf16_f32 v226, v200, v201\n"
    "v_add_f32 v245, v245, v198\n"
    "v_mfma_f32_16x16x32_bf16 a[116:119], v[180:183], v[236:239], a[116:119]\n"
    "s_add_u32 m0, s55, 66056\n"
    "v_exp_f32 v202, v202\n"
    "v_mfma_f32_16x16x32_bf16 a[84:87], v[180:183], v[232:235], a[84:87]\n"
    "buffer_load_dwordx4 v254, s[44:47], s53 offen offset:1024 nt lds\n"
    "v_mfma_f32_16x16x32_bf16 a[88:91], v[184:187], v[232:235], a[88:91]\n"
    "s_add_u32 s53, s53, 8192\n"
    "v_exp_f32 v203, v203\n"
    "v_mfma_f32_16x16x32_bf16 a[120:123], v[184:187], v[236:239], a[120:123]\n"
    "v_cvt_pk_bf16_f32 v227, v202, v203\n"
    "v_add_f32 v245, v245, v199\n"
    "v_mfma_f32_16x16x32_bf16 a[124:127], v[188:191], v[236:239], a[124:127]\n"
    "v_exp_f32 v204, v204\n"
    "v_mfma_f32_16x16x32_bf16 a[92:95], v[188:191], v[232:235], a[92:95]\n"
    "v_add_f32 v244, v244, v201\n"
    "v_add_f32 v245, v245, v204\n"
    "v_mfma_f32_16x16x32_bf16 v[208:211], v[128:131], v[96:99], v[248:251]\n"
    "v_exp_f32 v205, v205\n"
    "v_mfma_f32_16x16x32_bf16 v[208:211], v[132:135], v[100:103], v[208:211]\n"
    "v_cvt_pk_bf16_f32 v230, v204, v205\n"
    "v_add_f32 v244, v244, v202\n"
    "v_mfma_f32_16x16x32_bf16 v[208:211], v[136:139], v[104:107], v[208:211]\n"
    "v_exp_f32 v206, v206\n"
    "v_mfma_f32_16x16x32_bf16 v[208:211], v[140:143], v[108:111], v[208:211]\n"
    "v_add_f32 v244, v244, v203\n"
    "v_add_f32 v245, v245, v205\n"
    "v_mfma_f32_16x16x32_bf16 v[212:215], v[140:143], v[124:127], v[248:251]\n"
    "v_exp_f32 v207, v207\n"
    "v_mfma_f32_16x16x32_bf16 v[212:215], v[136:139], v[120:123], v[212:215]\n"
    "v_cvt_pk_bf16_f32 v231, v206, v207\n"
    "v_add_f32 v245, v245, v206\n"
    "v_mfma_f32_16x16x32_bf16 v[212:215], v[132:135], v[116:119], v[212:215]\n"
    "v_exp_f32 v208, v208\n"
    "v_mfma_f32_16x16x32_bf16 v[212:215], v[128:131], v[112:115], v[212:215]\n"
    "ds_read_b128 v[140:143], v252 offset:33792\n"
    "v_add_f32 v245, v245, v207\n"
    "v_mfma_f32_16x16x32_bf16 a[128:131], v[160:163], v[224:227], a[128:131]\n"
    "v_exp_f32 v209, v209\n"
    "v_mfma_f32_16x16x32_bf16 a[160:163], v[160:163], v[228:231], a[160:163]\n"
    "ds_read_b128 v[136:139], v252 offset:33536\n"
    "v_cvt_pk_bf16_f32 v232, v208, v209\n"
    "v_mfma_f32_16x16x32_bf16 a[164:167], v[164:167], v[228:231], a[164:167]\n"
    "v_exp_f32 v210, v210\n"
    "v_mfma_f32_16x16x32_bf16 a[132:135], v[164:167], v[224:227], a[132:135]\n"
    "ds_read_b128 v[132:135], v252 offset:33280\n"
    "v_add_f32 v246, v246, v208\n"
    "v_mfma_f32_16x16x32_bf16 a[136:139], v[168:171], v[224:227], a[136:139]\n"
    "v_exp_f32 v211, v211\n"
    "v_mfma_f32_16x16x32_bf16 a[168:171], v[168:171], v[228:231], a[168:171]\n"
    "ds_read_b128 v[128:131], v252 offset:33024\n"
    "v_cvt_pk_bf16_f32 v233, v210, v211\n"
    "v_mfma_f32_16x16x32_bf16 a[172:175], v[172:175], v[228:231], a[172:175]\n"
    "v_exp_f32 v212, v212\n"
    "v_mfma_f32_16x16x32_bf16 a[140:143], v[172:175], v[224:227], a[140:143]\n"
    "v_add_f32 v246, v246, v209\n"
    "v_add_f32 v247, v247, v212\n"
    "v_mfma_f32_16x16x32_bf16 v[216:219], v[144:147], v[96:99], v[248:251]\n"
    "v_exp_f32 v213, v213\n"
    "v_mfma_f32_16x16x32_bf16 v[216:219], v[148:151], v[100:103], v[216:219]\n"
    "v_cvt_pk_bf16_f32 v236, v212, v213\n"
    "v_add_f32 v246, v246, v210\n"
    "v_mfma_f32_16x16x32_bf16 v[216:219], v[152:155], v[104:107], v[216:219]\n"
    "v_exp_f32 v214, v214\n"
    "v_mfma_f32_16x16x32_bf16 v[216:219], v[156:159], v[108:111], v[216:219]\n"
    "v_add_f32 v246, v246, v211\n"
    "v_add_f32 v247, v247, v213\n"
    "v_mfma_f32_16x16x32_bf16 v[220:223], v[156:159], v[124:127], v[248:251]\n"
    "v_exp_f32 v215, v215\n"
    "v_mfma_f32_16x16x32_bf16 v[220:223], v[152:155], v[120:123], v[220:223]\n"
    "v_cvt_pk_bf16_f32 v237, v214, v215\n"
    "v_add_f32 v247, v247, v214\n"
    "v_mfma_f32_16x16x32_bf16 v[220:223], v[148:151], v[116:119], v[220:223]\n"
    "v_exp_f32 v216, v216\n"
    "v_mfma_f32_16x16x32_bf16 v[220:223], v[144:147], v[112:115], v[220:223]\n"
    "ds_read_b128 v[156:159], v252 offset:37920\n"
    "v_add_f32 v247, v247, v215\n"
    "v_mfma_f32_16x16x32_bf16 a[144:147], v[176:179], v[224:227], a[144:147]\n"
    "v_exp_f32 v217, v217\n"
    "v_mfma_f32_16x16x32_bf16 a[176:179], v[176:179], v[228:231], a[176:179]\n"
    "ds_read_b128 v[152:155], v252 offset:37664\n"
    "v_cvt_pk_bf16_f32 v234, v216, v217\n"
    "v_mfma_f32_16x16x32_bf16 a[180:183], v[180:183], v[228:231], a[180:183]\n"
    "v_exp_f32 v218, v218\n"
    "v_mfma_f32_16x16x32_bf16 a[148:151], v[180:183], v[224:227], a[148:151]\n"
    "ds_read_b128 v[148:151], v252 offset:37408\n"
    "v_add_f32 v246, v246, v216\n"
    "v_mfma_f32_16x16x32_bf16 a[152:155], v[184:187], v[224:227], a[152:155]\n"
    "v_exp_f32 v219, v219\n"
    "v_mfma_f32_16x16x32_bf16 a[184:187], v[184:187], v[228:231], a[184:187]\n"
    "ds_read_b128 v[144:147], v252 offset:37152\n"
    "v_cvt_pk_bf16_f32 v235, v218, v219\n"
    "v_mfma_f32_16x16x32_bf16 a[188:191], v[188:191], v[228:231], a[188:191]\n"
    "s_sub_u32 s58, s58, 1\n"
    "v_exp_f32 v220, v220\n"
    "v_mfma_f32_16x16x32_bf16 a[156:159], v[188:191], v[224:227], a[156:159]\n"
    "s_cbranch_scc1 L_exit_%=\n"
    "s_waitcnt lgkmcnt(4)\n"
    "v_add_f32 v246, v246, v217\n"
    "v_add_f32 v247, v247, v220\n"
    "v_mfma_f32_16x16x32_bf16 v[192:195], v[128:131], v[0:3], v[248:251]\n"
    "v_exp_f32 v221, v221\n"
    "v_mfma_f32_16x16x32_bf16 v[192:195], v[132:135], v[4:7], v[192:195]\n"
    "v_cvt_pk_bf16_f32 v238, v220, v221\n"
    "v_add_f32 v246, v246, v218\n"
    "v_mfma_f32_16x16x32_bf16 v[192:195], v[136:139], v[8:11], v[192:195]\n"
    "v_exp_f32 v222, v222\n"
    "v_mfma_f32_16x16x32_bf16 v[192:195], v[140:143], v[12:15], v[192:195]\n"
    "v_add_f32 v246, v246, v219\n"
    "v_add_f32 v247, v247, v221\n"
    "v_mfma_f32_16x16x32_bf16 v[196:199], v[140:143], v[28:31], v[248:251]\n"
    "v_exp_f32 v223, v223\n"
    "v_mfma_f32_16x16x32_bf16 v[196:199], v[136:139], v[24:27], v[196:199]\n"
    "v_cvt_pk_bf16_f32 v239, v222, v223\n"
    "v_add_f32 v247, v247, v222\n"
    "v_mfma_f32_16x16x32_bf16 v[196:199], v[132:135], v[20:23], v[196:199]\n"
    "v_exp_f32 v192, v192\n"
    "v_mfma_f32_16x16x32_bf16 v[196:199], v[128:131], v[16:19], v[196:199]\n"
    "v_add_f32 v247, v247, v223\n"
    "v_add_f32 v240, v240, v192\n"
    "v_mfma_f32_16x16x32_bf16 a[192:195], v[160:163], v[232:235], a[192:195]\n"
    "v_exp_f32 v193, v193\n"
    "v_mfma_f32_16x16x32_bf16 a[224:227], v[160:163], v[236:239], a[224:227]\n"
    "ds_read_b64_tr_b16 v[160:161], v253 offset:33024\n"
    "v_cvt_pk_bf16_f32 v224, v192, v193\n"
    "v_mfma_f32_16x16x32_bf16 a[228:231], v[164:167], v[236:239], a[228:231]\n"
    "v_exp_f32 v194, v194\n"
    "v_mfma_f32_16x16x32_bf16 a[196:199], v[164:167], v[232:235], a[196:199]\n"
    "ds_read_b64_tr_b16 v[162:163], v253 offset:37152\n"
    "v_add_f32 v240, v240, v193\n"
    "v_mfma_f32_16x16x32_bf16 a[200:203], v[168:171], v[232:235], a[200:203]\n"
    "v_exp_f32 v195, v195\n"
    "v_mfma_f32_16x16x32_bf16 a[232:235], v[168:171], v[236:239], a[232:235]\n"
    "ds_read_b64_tr_b16 v[164:165], v253 offset:33032\n"
    "v_cvt_pk_bf16_f32 v225, v194, v195\n"
    "v_mfma_f32_16x16x32_bf16 a[236:239], v[172:175], v[236:239], a[236:239]\n"
    "v_exp_f32 v196, v196\n"
    "v_mfma_f32_16x16x32_bf16 a[204:207], v[172:175], v[232:235], a[204:207]\n"
    "s_waitcnt lgkmcnt(3)\n"
    "ds_read_b64_tr_b16 v[166:167], v253 offset:37160\n"
    "v_add_f32 v240, v240, v194\n"
    "v_mfma_f32_16x16x32_bf16 v[200:203], v[144:147], v[0:3], v[248:251]\n"
    "v_exp_f32 v197, v197\n"
    "v_mfma_f32_16x16x32_bf16 v[200:203], v[148:151], v[4:7], v[200:203]\n"
    "ds_read_b64_tr_b16 v[168:169], v253 offset:33280\n"
    "v_cvt_pk_bf16_f32 v228, v196, v197\n"
    "v_mfma_f32_16x16x32_bf16 v[200:203], v[152:155], v[8:11], v[200:203]\n"
    "v_exp_f32 v198, v198\n"
    "v_mfma_f32_16x16x32_bf16 v[200:203], v[156:159], v[12:15], v[200:203]\n"
    "ds_read_b64_tr_b16 v[170:171], v253 offset:37408\n"
    "v_add_f32 v240, v240, v195\n"
    "v_mfma_f32_16x16x32_bf16 v[204:207], v[156:159], v[28:31], v[248:251]\n"
    "v_exp_f32 v199, v199\n"
    "v_mfma_f32_16x16x32_bf16 v[204:207], v[152:155], v[24:27], v[204:207]\n"
    "ds_read_b64_tr_b16 v[172:173], v253 offset:33288\n"
    "v_cvt_pk_bf16_f32 v229, v198, v199\n"
    "v_mfma_f32_16x16x32_bf16 v[204:207], v[148:151], v[20:23], v[204:207]\n"
    "v_exp_f32 v200, v200\n"
    "v_mfma_f32_16x16x32_bf16 v[204:207], v[144:147], v[16:19], v[204:207]\n"
    "ds_read_b64_tr_b16 v[174:175], v253 offset:37416\n"
    "v_add_f32 v241, v241, v196\n"
    "v_mfma_f32_16x16x32_bf16 a[208:211], v[176:179], v[232:235], a[208:211]\n"
    "v_exp_f32 v201, v201\n"
    "v_mfma_f32_16x16x32_bf16 a[240:243], v[176:179], v[236:239], a[240:243]\n"
    "ds_read_b64_tr_b16 v[176:177], v253 offset:33536\n"
    "v_cvt_pk_bf16_f32 v226, v200, v201\n"
    "v_mfma_f32_16x16x32_bf16 a[244:247], v[180:183], v[236:239], a[244:247]\n"
    "v_exp_f32 v202, v202\n"
    "v_mfma_f32_16x16x32_bf16 a[212:215], v[180:183], v[232:235], a[212:215]\n"
    "ds_read_b64_tr_b16 v[178:179], v253 offset:37664\n"
    "v_add_f32 v241, v241, v197\n"
    "v_mfma_f32_16x16x32_bf16 a[216:219], v[184:187], v[232:235], a[216:219]\n"
    "v_exp_f32 v203, v203\n"
    "v_mfma_f32_16x16x32_bf16 a[248:251], v[184:187], v[236:239], a[248:251]\n"
    "ds_read_b64_tr_b16 v[180:181], v253 offset:33544\n"
    "v_cvt_pk_bf16_f32 v227, v202, v203\n"
    "v_mfma_f32_16x16x32_bf16 a[252:255], v[188:191], v[236:239], a[252:255]\n"
    "v_exp_f32 v204, v204\n"
    "v_mfma_f32_16x16x32_bf16 a[220:223], v[188:191], v[232:235], a[220:223]\n"
    "ds_read_b64_tr_b16 v[182:183], v253 offset:37672\n"
    "v_add_f32 v241, v241, v198\n"
    "v_mfma_f32_16x16x32_bf16 v[208:211], v[128:131], v[32:35], v[248:251]\n"
    "v_exp_f32 v205, v205\n"
    "v_mfma_f32_16x16x32_bf16 v[208:211], v[132:135], v[36:39], v[208:211]\n"
    "ds_read_b64_tr_b16 v[184:185], v253 offset:33792\n"
    "v_cvt_pk_bf16_f32 v230, v204, v205\n"
    "v_mfma_f32_16x16x32_bf16 v[208:211], v[136:139], v[40:43], v[208:211]\n"
    "v_exp_f32 v206, v206\n"
    "v_mfma_f32_16x16x32_bf16 v[208:211], v[140:143], v[44:47], v[208:211]\n"
    "ds_read_b64_tr_b16 v[186:187], v253 offset:37920\n"
    "v_add_f32 v241, v241, v199\n"
    "v_mfma_f32_16x16x32_bf16 v[212:215], v[140:143], v[60:63], v[248:251]\n"
    "v_exp_f32 v207, v207\n"
    "v_mfma_f32_16x16x32_bf16 v[212:215], v[136:139], v[56:59], v[212:215]\n"
    "ds_read_b64_tr_b16 v[188:189], v253 offset:33800\n"
    "v_cvt_pk_bf16_f32 v231, v206, v207\n"
    "v_mfma_f32_16x16x32_bf16 v[212:215], v[132:135], v[52:55], v[212:215]\n"
    "v_exp_f32 v208, v208\n"
    "v_mfma_f32_16x16x32_bf16 v[212:215], v[128:131], v[48:51], v[212:215]\n"
    "s_waitcnt lgkmcnt(13)\n"
    "ds_read_b64_tr_b16 v[190:191], v253 offset:37928\n"
    "v_add_f32 v240, v240, v200\n"
    "v_mfma_f32_16x16x32_bf16 a[0:3], v[160:163], v[224:227], a[0:3]\n"
    "v_exp_f32 v209, v209\n"
    "v_mfma_f32_16x16x32_bf16 a[32:35], v[160:163], v[228:231], a[32:35]\n"
    "s_waitcnt lgkmcnt(12)\n"
    "v_cvt_pk_bf16_f32 v232, v208, v209\n"
    "v_add_f32 v240, v240, v201\n"
    "v_mfma_f32_16x16x32_bf16 a[36:39], v[164:167], v[228:231], a[36:39]\n"
    "v_exp_f32 v210, v210\n"
    "v_mfma_f32_16x16x32_bf16 a[4:7], v[164:167], v[224:227], a[4:7]\n"
    "s_waitcnt lgkmcnt(10)\n"
    "v_add_f32 v240, v240, v202\n"
    "v_add_f32 v241, v241, v204\n"
    "v_mfma_f32_16x16x32_bf16 a[8:11], v[168:171], v[224:227], a[8:11]\n"
    "v_exp_f32 v211, v211\n"
    "v_mfma_f32_16x16x32_bf16 a[40:43], v[168:171], v[228:231], a[40:43]\n"
    "s_waitcnt lgkmcnt(8)\n"
    "v_cvt_pk_bf16_f32 v233, v210, v211\n"
    "v_add_f32 v240, v240, v203\n"
    "v_mfma_f32_16x16x32_bf16 a[44:47], v[172:175], v[228:231], a[44:47]\n"
    "s_add_u32 m0, s54, 16512\n"
    "v_exp_f32 v212, v212\n"
    "v_mfma_f32_16x16x32_bf16 a[12:15], v[172:175], v[224:227], a[12:15]\n"
    "buffer_load_dwordx4 v254, s[40:43], s53 offen nt lds\n"
    "v_mfma_f32_16x16x32_bf16 v[216:219], v[144:147], v[32:35], v[248:251]\n"
    "v_exp_f32 v213, v213\n"
    "v_mfma_f32_16x16x32_bf16 v[216:219], v[148:151], v[36:39], v[216:219]\n"
    "v_cvt_pk_bf16_f32 v236, v212, v213\n"
    "v_add_f32 v241, v241, v205\n"
    "v_mfma_f32_16x16x32_bf16 v[216:219], v[152:155], v[40:43], v[216:219]\n"
    "v_exp_f32 v214, v214\n"
    "v_mfma_f32_16x16x32_bf16 v[216:219], v[156:159], v[44:47], v[216:219]\n"
    "v_add_f32 v241, v241, v206\n"
    "v_add_f32 v242, v242, v208\n"
    "v_mfma_f32_16x16x32_bf16 v[220:223], v[156:159], v[60:63], v[248:251]\n"
    "v_exp_f32 v215, v215\n"
    "v_mfma_f32_16x16x32_bf16 v[220:223], v[152:155], v[56:59], v[220:223]\n"
    "v_cvt_pk_bf16_f32 v237, v214, v215\n"
    "v_add_f32 v241, v241, v207\n"
    "v_mfma_f32_16x16x32_bf16 v[220:223], v[148:151], v[52:55], v[220:223]\n"
    "v_exp_f32 v216, v216\n"
    "v_mfma_f32_16x16x32_bf16 v[220:223], v[144:147], v[48:51], v[220:223]\n"
    "s_waitcnt lgkmcnt(6)\n"
    "v_add_f32 v242, v242, v209\n"
    "v_add_f32 v243, v243, v212\n"
    "v_mfma_f32_16x16x32_bf16 a[16:19], v[176:179], v[224:227], a[16:19]\n"
    "v_exp_f32 v217, v217\n"
    "v_mfma_f32_16x16x32_bf16 a[48:51], v[176:179], v[228:231], a[48:51]\n"
    "s_waitcnt lgkmcnt(4)\n"
    "v_cvt_pk_bf16_f32 v234, v216, v217\n"
    "v_add_f32 v242, v242, v210\n"
    "v_mfma_f32_16x16x32_bf16 a[52:55], v[180:183], v[228:231], a[52:55]\n"
    "v_exp_f32 v218, v218\n"
    "v_mfma_f32_16x16x32_bf16 a[20:23], v[180:183], v[224:227], a[20:23]\n"
    "s_waitcnt lgkmcnt(2)\n"
    "buffer_load_dwordx4 v254, s[40:43], s53 offen offset:1024 nt lds\n"
    "v_mfma_f32_16x16x32_bf16 a[24:27], v[184:187], v[224:227], a[24:27]\n"
    "v_exp_f32 v219, v219\n"
    "v_mfma_f32_16x16x32_bf16 a[56:59], v[184:187], v[228:231], a[56:59]\n"
    "s_waitcnt lgkmcnt(0)\n"
    "v_cvt_pk_bf16_f32 v235, v218, v219\n"
    "v_add_f32 v242, v242, v211\n"
    "v_mfma_f32_16x16x32_bf16 a[60:63], v[188:191], v[228:231], a[60:63]\n"
    "v_exp_f32 v220, v220\n"
    "v_mfma_f32_16x16x32_bf16 a[28:31], v[188:191], v[224:227], a[28:31]\n"
    "v_add_f32 v243, v243, v213\n"
    "v_add_f32 v242, v242, v216\n"
    "v_mfma_f32_16x16x32_bf16 v[192:195], v[128:131], v[64:67], v[248:251]\n"
    "v_exp_f32 v221, v221\n"
    "v_mfma_f32_16x16x32_bf16 v[192:195], v[132:135], v[68:71], v[192:195]\n"
    "v_cvt_pk_bf16_f32 v238, v220, v221\n"
    "v_add_f32 v243, v243, v214\n"
    "v_mfma_f32_16x16x32_bf16 v[192:195], v[136:139], v[72:75], v[192:195]\n"
    "v_exp_f32 v222, v222\n"
    "v_mfma_f32_16x16x32_bf16 v[192:195], v[140:143], v[76:79], v[192:195]\n"
    "v_add_f32 v243, v243, v215\n"
    "v_add_f32 v242, v242, v217\n"
    "v_mfma_f32_16x16x32_bf16 v[196:199], v[140:143], v[92:95], v[248:251]\n"
    "v_exp_f32 v223, v223\n"
    "v_mfma_f32_16x16x32_bf16 v[196:199], v[136:139], v[88:91], v[196:199]\n"
    "v_cvt_pk_bf16_f32 v239, v222, v223\n"
    "v_add_f32 v242, v242, v218\n"
    "v_mfma_f32_16x16x32_bf16 v[196:199], v[132:135], v[84:87], v[196:199]\n"
    "v_exp_f32 v192, v192\n"
    "v_mfma_f32_16x16x32_bf16 v[196:199], v[128:131], v[80:83], v[196:199]\n"
    "v_add_f32 v242, v242, v219\n"
    "v_add_f32 v243, v243, v220\n"
    "v_mfma_f32_16x16x32_bf16 a[64:67], v[160:163], v[232:235], a[64:67]\n"
    "v_exp_f32 v193, v193\n"
    "v_mfma_f32_16x16x32_bf16 a[96:99], v[160:163], v[236:239], a[96:99]\n"
    "v_cvt_pk_bf16_f32 v224, v192, v193\n"
    "v_add_f32 v243, v243, v221\n"
    "v_mfma_f32_16x16x32_bf16 a[100:103], v[164:167], v[236:239], a[100:103]\n"
    "s_add_u32 m0, s55, 74304\n"
    "v_exp_f32 v194, v194\n"
    "v_mfma_f32_16x16x32_bf16 a[68:71], v[164:167], v[232:235], a[68:71]\n"
    "buffer_load_dwordx4 v254, s[44:47], s53 offen nt lds\n"
    "v_mfma_f32_16x16x32_bf16 a[72:75], v[168:171], v[232:235], a[72:75]\n"
    "v_exp_f32 v195, v195\n"
    "v_mfma_f32_16x16x32_bf16 a[104:107], v[168:171], v[236:239], a[104:107]\n"
    "v_cvt_pk_bf16_f32 v225, v194, v195\n"
    "v_add_f32 v243, v243, v222\n"
    "v_mfma_f32_16x16x32_bf16 a[108:111], v[172:175], v[236:239], a[108:111]\n"
    "v_exp_f32 v196, v196\n"
    "v_mfma_f32_16x16x32_bf16 a[76:79], v[172:175], v[232:235], a[76:79]\n"
    "v_add_f32 v243, v243, v223\n"
    "v_add_f32 v244, v244, v192\n"
    "v_mfma_f32_16x16x32_bf16 v[200:203], v[144:147], v[64:67], v[248:251]\n"
    "v_exp_f32 v197, v197\n"
    "v_mfma_f32_16x16x32_bf16 v[200:203], v[148:151], v[68:71], v[200:203]\n"
    "v_cvt_pk_bf16_f32 v228, v196, v197\n"
    "v_add_f32 v244, v244, v193\n"
    "v_mfma_f32_16x16x32_bf16 v[200:203], v[152:155], v[72:75], v[200:203]\n"
    "v_exp_f32 v198, v198\n"
    "v_mfma_f32_16x16x32_bf16 v[200:203], v[156:159], v[76:79], v[200:203]\n"
    "v_add_f32 v244, v244, v194\n"
    "v_add_f32 v245, v245, v196\n"
    "v_mfma_f32_16x16x32_bf16 v[204:207], v[156:159], v[92:95], v[248:251]\n"
    "v_exp_f32 v199, v199\n"
    "v_mfma_f32_16x16x32_bf16 v[204:207], v[152:155], v[88:91], v[204:207]\n"
    "v_cvt_pk_bf16_f32 v229, v198, v199\n"
    "v_add_f32 v244, v244, v195\n"
    "v_mfma_f32_16x16x32_bf16 v[204:207], v[148:151], v[84:87], v[204:207]\n"
    "v_exp_f32 v200, v200\n"
    "v_mfma_f32_16x16x32_bf16 v[204:207], v[144:147], v[80:83], v[204:207]\n"
    "v_add_f32 v245, v245, v197\n"
    "v_add_f32 v244, v244, v200\n"
    "v_mfma_f32_16x16x32_bf16 a[80:83], v[176:179], v[232:235], a[80:83]\n"
    "v_exp_f32 v201, v201\n"
    "v_mfma_f32_16x16x32_bf16 a[112:115], v[176:179], v[236:239], a[112:115]\n"
    "v_cvt_pk_bf16_f32 v226, v200, v201\n"
    "v_add_f32 v245, v245, v198\n"
    "v_mfma_f32_16x16x32_bf16 a[116:119], v[180:183], v[236:239], a[116:119]\n"
    "s_add_u32 m0, s55, 74312\n"
    "v_exp_f32 v202, v202\n"
    "v_mfma_f32_16x16x32_bf16 a[84:87], v[180:183], v[232:235], a[84:87]\n"
    "buffer_load_dwordx4 v254, s[44:47], s53 offen offset:1024 nt lds\n"
    "v_mfma_f32_16x16x32_bf16 a[88:91], v[184:187], v[232:235], a[88:91]\n"
    "s_add_u32 s53, s53, 8192\n"
    "v_exp_f32 v203, v203\n"
    "v_mfma_f32_16x16x32_bf16 a[120:123], v[184:187], v[236:239], a[120:123]\n"
    "v_cvt_pk_bf16_f32 v227, v202, v203\n"
    "v_add_f32 v245, v245, v199\n"
    "v_mfma_f32_16x16x32_bf16 a[124:127], v[188:191], v[236:239], a[124:127]\n"
    "v_exp_f32 v204, v204\n"
    "v_mfma_f32_16x16x32_bf16 a[92:95], v[188:191], v[232:235], a[92:95]\n"
    "v_add_f32 v244, v244, v201\n"
    "v_add_f32 v245, v245, v204\n"
    "v_mfma_f32_16x16x32_bf16 v[208:211], v[128:131], v[96:99], v[248:251]\n"
    "v_exp_f32 v205, v205\n"
    "v_mfma_f32_16x16x32_bf16 v[208:211], v[132:135], v[100:103], v[208:211]\n"
    "v_cvt_pk_bf16_f32 v230, v204, v205\n"
    "v_add_f32 v244, v244, v202\n"
    "v_mfma_f32_16x16x32_bf16 v[208:211], v[136:139], v[104:107], v[208:211]\n"
    "s_waitcnt vmcnt(8)\n"
    "v_exp_f32 v206, v206\n"
    "v_mfma_f32_16x16x32_bf16 v[208:211], v[140:143], v[108:111], v[208:211]\n"
    "s_barrier\n"
    "v_add_f32 v244, v244, v203\n"
    "v_add_f32 v245, v245, v205\n"
    "v_mfma_f32_16x16x32_bf16 v[212:215], v[140:143], v[124:127], v[248:251]\n"
    "v_exp_f32 v207, v207\n"
    "v_mfma_f32_16x16x32_bf16 v[212:215], v[136:139], v[120:123], v[212:215]\n"
    "v_cvt_pk_bf16_f32 v231, v206, v207\n"
    "v_add_f32 v245, v245, v206\n"
    "v_mfma_f32_16x16x32_bf16 v[212:215], v[132:135], v[116:119], v[212:215]\n"
    "v_exp_f32 v208, v208\n"
    "v_mfma_f32_16x16x32_bf16 v[212:215], v[128:131], v[112:115], v[212:215]\n"
    "ds_read_b128 v[140:143], v252 offset:42048\n"
    "v_add_f32 v245, v245, v207\n"
    "v_mfma_f32_16x16x32_bf16 a[128:131], v[160:163], v[224:227], a[128:131]\n"
    "v_exp_f32 v209, v209\n"
    "v_mfma_f32_16x16x32_bf16 a[160:163], v[160:163], v[228:231], a[160:163]\n"
    "ds_read_b128 v[136:139], v252 offset:41792\n"
    "v_cvt_pk_bf16_f32 v232, v208, v209\n"
    "v_mfma_f32_16x16x32_bf16 a[164:167], v[164:167], v[228:231], a[164:167]\n"
    "v_exp_f32 v210, v210\n"
    "v_mfma_f32_16x16x32_bf16 a[132:135], v[164:167], v[224:227], a[132:135]\n"
    "ds_read_b128 v[132:135], v252 offset:41536\n"
    "v_add_f32 v246, v246, v208\n"
    "v_mfma_f32_16x16x32_bf16 a[136:139], v[168:171], v[224:227], a[136:139]\n"
    "v_exp_f32 v211, v211\n"
    "v_mfma_f32_16x16x32_bf16 a[168:171], v[168:171], v[228:231], a[168:171]\n"
    "ds_read_b128 v[128:131], v252 offset:41280\n"
    "v_cvt_pk_bf16_f32 v233, v210, v211\n"
    "v_mfma_f32_16x16x32_bf16 a[172:175], v[172:175], v[228:231], a[172:175]\n"
    "v_exp_f32 v212, v212\n"
    "v_mfma_f32_16x16x32_bf16 a[140:143], v[172:175], v[224:227], a[140:143]\n"
    "v_add_f32 v246, v246, v209\n"
    "v_add_f32 v247, v247, v212\n"
    "v_mfma_f32_16x16x32_bf16 v[216:219], v[144:147], v[96:99], v[248:251]\n"
    "v_exp_f32 v213, v213\n"
    "v_mfma_f32_16x16x32_bf16 v[216:219], v[148:151], v[100:103], v[216:219]\n"
    "v_cvt_pk_bf16_f32 v236, v212, v213\n"
    "v_add_f32 v246, v246, v210\n"
    "v_mfma_f32_16x16x32_bf16 v[216:219], v[152:155], v[104:107], v[216:219]\n"
    "v_exp_f32 v214, v214\n"
    "v_mfma_f32_16x16x32_bf16 v[216:219], v[156:159], v[108:111], v[216:219]\n"
    "v_add_f32 v246, v246, v211\n"
    "v_add_f32 v247, v247, v213\n"
    "v_mfma_f32_16x16x32_bf16 v[220:223], v[156:159], v[124:127], v[248:251]\n"
    "v_exp_f32 v215, v215\n"
    "v_mfma_f32_16x16x32_bf16 v[220:223], v[152:155], v[120:123], v[220:223]\n"
    "v_cvt_pk_bf16_f32 v237, v214, v215\n"
    "v_add_f32 v247, v247, v214\n"
    "v_mfma_f32_16x16x32_bf16 v[220:223], v[148:151], v[116:119], v[220:223]\n"
    "v_exp_f32 v216, v216\n"
    "v_mfma_f32_16x16x32_bf16 v[220:223], v[144:147], v[112:115], v[220:223]\n"
    "ds_read_b128 v[156:159], v252 offset:46176\n"
    "v_add_f32 v247, v247, v215\n"
    "v_mfma_f32_16x16x32_bf16 a[144:147], v[176:179], v[224:227], a[144:147]\n"
    "v_exp_f32 v217, v217\n"
    "v_mfma_f32_16x16x32_bf16 a[176:179], v[176:179], v[228:231], a[176:179]\n"
    "ds_read_b128 v[152:155], v252 offset:45920\n"
    "v_cvt_pk_bf16_f32 v234, v216, v217\n"
    "v_mfma_f32_16x16x32_bf16 a[180:183], v[180:183], v[228:231], a[180:183]\n"
    "v_exp_f32 v218, v218\n"
    "v_mfma_f32_16x16x32_bf16 a[148:151], v[180:183], v[224:227], a[148:151]\n"
    "ds_read_b128 v[148:151], v252 offset:45664\n"
    "v_add_f32 v246, v246, v216\n"
    "v_mfma_f32_16x16x32_bf16 a[152:155], v[184:187], v[224:227], a[152:155]\n"
    "v_exp_f32 v219, v219\n"
    "v_mfma_f32_16x16x32_bf16 a[184:187], v[184:187], v[228:231], a[184:187]\n"
    "ds_read_b128 v[144:147], v252 offset:45408\n"
    "v_cvt_pk_bf16_f32 v235, v218, v219\n"
    "v_mfma_f32_16x16x32_bf16 a[188:191], v[188:191], v[228:231], a[188:191]\n"
    "v_exp_f32 v220, v220\n"
    "v_mfma_f32_16x16x32_bf16 a[156:159], v[188:191], v[224:227], a[156:159]\n"
    "s_waitcnt lgkmcnt(4)\n"
    "v_add_f32 v246, v246, v217\n"
    "v_add_f32 v247, v247, v220\n"
    "v_mfma_f32_16x16x32_bf16 v[192:195], v[128:131], v[0:3], v[248:251]\n"
    "v_exp_f32 v221, v221\n"
    "v_mfma_f32_16x16x32_bf16 v[192:195], v[132:135], v[4:7], v[192:195]\n"
    "v_cvt_pk_bf16_f32 v238, v220, v221\n"
    "v_add_f32 v246, v246, v218\n"
    "v_mfma_f32_16x16x32_bf16 v[192:195], v[136:139], v[8:11], v[192:195]\n"
    "v_exp_f32 v222, v222\n"
    "v_mfma_f32_16x16x32_bf16 v[192:195], v[140:143], v[12:15], v[192:195]\n"
    "v_add_f32 v246, v246, v219\n"
    "v_add_f32 v247, v247, v221\n"
    "v_mfma_f32_16x16x32_bf16 v[196:199], v[140:143], v[28:31], v[248:251]\n"
    "v_exp_f32 v223, v223\n"
    "v_mfma_f32_16x16x32_bf16 v[196:199], v[136:139], v[24:27], v[196:199]\n"
    "v_cvt_pk_bf16_f32 v239, v222, v223\n"
    "v_add_f32 v247, v247, v222\n"
    "v_mfma_f32_16x16x32_bf16 v[196:199], v[132:135], v[20:23], v[196:199]\n"
    "v_exp_f32 v192, v192\n"
    "v_mfma_f32_16x16x32_bf16 v[196:199], v[128:131], v[16:19], v[196:199]\n"
    "v_add_f32 v247, v247, v223\n"
    "v_add_f32 v240, v240, v192\n"
    "v_mfma_f32_16x16x32_bf16 a[192:195], v[160:163], v[232:235], a[192:195]\n"
    "v_exp_f32 v193, v193\n"
    "v_mfma_f32_16x16x32_bf16 a[224:227], v[160:163], v[236:239], a[224:227]\n"
    "ds_read_b64_tr_b16 v[160:161], v253 offset:41280\n"
    "v_cvt_pk_bf16_f32 v224, v192, v193\n"
    "v_mfma_f32_16x16x32_bf16 a[228:231], v[164:167], v[236:239], a[228:231]\n"
    "v_exp_f32 v194, v194\n"
    "v_mfma_f32_16x16x32_bf16 a[196:199], v[164:167], v[232:235], a[196:199]\n"
    "ds_read_b64_tr_b16 v[162:163], v253 offset:45408\n"
    "v_add_f32 v240, v240, v193\n"
    "v_mfma_f32_16x16x32_bf16 a[200:203], v[168:171], v[232:235], a[200:203]\n"
    "v_exp_f32 v195, v195\n"
    "v_mfma_f32_16x16x32_bf16 a[232:235], v[168:171], v[236:239], a[232:235]\n"
    "ds_read_b64_tr_b16 v[164:165], v253 offset:41288\n"
    "v_cvt_pk_bf16_f32 v225, v194, v195\n"
    "v_mfma_f32_16x16x32_bf16 a[236:239], v[172:175], v[236:239], a[236:239]\n"
    "v_exp_f32 v196, v196\n"
    "v_mfma_f32_16x16x32_bf16 a[204:207], v[172:175], v[232:235], a[204:207]\n"
    "s_waitcnt lgkmcnt(3)\n"
    "ds_read_b64_tr_b16 v[166:167], v253 offset:45416\n"
    "v_add_f32 v240, v240, v194\n"
    "v_mfma_f32_16x16x32_bf16 v[200:203], v[144:147], v[0:3], v[248:251]\n"
    "v_exp_f32 v197, v197\n"
    "v_mfma_f32_16x16x32_bf16 v[200:203], v[148:151], v[4:7], v[200:203]\n"
    "ds_read_b64_tr_b16 v[168:169], v253 offset:41536\n"
    "v_cvt_pk_bf16_f32 v228, v196, v197\n"
    "v_mfma_f32_16x16x32_bf16 v[200:203], v[152:155], v[8:11], v[200:203]\n"
    "v_exp_f32 v198, v198\n"
    "v_mfma_f32_16x16x32_bf16 v[200:203], v[156:159], v[12:15], v[200:203]\n"
    "ds_read_b64_tr_b16 v[170:171], v253 offset:45664\n"
    "v_add_f32 v240, v240, v195\n"
    "v_mfma_f32_16x16x32_bf16 v[204:207], v[156:159], v[28:31], v[248:251]\n"
    "v_exp_f32 v199, v199\n"
    "v_mfma_f32_16x16x32_bf16 v[204:207], v[152:155], v[24:27], v[204:207]\n"
    "ds_read_b64_tr_b16 v[172:173], v253 offset:41544\n"
    "v_cvt_pk_bf16_f32 v229, v198, v199\n"
    "v_mfma_f32_16x16x32_bf16 v[204:207], v[148:151], v[20:23], v[204:207]\n"
    "v_exp_f32 v200, v200\n"
    "v_mfma_f32_16x16x32_bf16 v[204:207], v[144:147], v[16:19], v[204:207]\n"
    "ds_read_b64_tr_b16 v[174:175], v253 offset:45672\n"
    "v_add_f32 v241, v241, v196\n"
    "v_mfma_f32_16x16x32_bf16 a[208:211], v[176:179], v[232:235], a[208:211]\n"
    "v_exp_f32 v201, v201\n"
    "v_mfma_f32_16x16x32_bf16 a[240:243], v[176:179], v[236:239], a[240:243]\n"
    "ds_read_b64_tr_b16 v[176:177], v253 offset:41792\n"
    "v_cvt_pk_bf16_f32 v226, v200, v201\n"
    "v_mfma_f32_16x16x32_bf16 a[244:247], v[180:183], v[236:239], a[244:247]\n"
    "v_exp_f32 v202, v202\n"
    "v_mfma_f32_16x16x32_bf16 a[212:215], v[180:183], v[232:235], a[212:215]\n"
    "ds_read_b64_tr_b16 v[178:179], v253 offset:45920\n"
    "v_add_f32 v241, v241, v197\n"
    "v_mfma_f32_16x16x32_bf16 a[216:219], v[184:187], v[232:235], a[216:219]\n"
    "v_exp_f32 v203, v203\n"
    "v_mfma_f32_16x16x32_bf16 a[248:251], v[184:187], v[236:239], a[248:251]\n"
    "ds_read_b64_tr_b16 v[180:181], v253 offset:41800\n"
    "v_cvt_pk_bf16_f32 v227, v202, v203\n"
    "v_mfma_f32_16x16x32_bf16 a[252:255], v[188:191], v[236:239], a[252:255]\n"
    "v_exp_f32 v204, v204\n"
    "v_mfma_f32_16x16x32_bf16 a[220:223], v[188:191], v[232:235], a[220:223]\n"
    "ds_read_b64_tr_b16 v[182:183], v253 offset:45928\n"
    "v_add_f32 v241, v241, v198\n"
    "v_mfma_f32_16x16x32_bf16 v[208:211], v[128:131], v[32:35], v[248:251]\n"
    "v_exp_f32 v205, v205\n"
    "v_mfma_f32_16x16x32_bf16 v[208:211], v[132:135], v[36:39], v[208:211]\n"
    "ds_read_b64_tr_b16 v[184:185], v253 offset:42048\n"
    "v_cvt_pk_bf16_f32 v230, v204, v205\n"
    "v_mfma_f32_16x16x32_bf16 v[208:211], v[136:139], v[40:43], v[208:211]\n"
    "v_exp_f32 v206, v206\n"
    "v_mfma_f32_16x16x32_bf16 v[208:211], v[140:143], v[44:47], v[208:211]\n"
    "ds_read_b64_tr_b16 v[186:187], v253 offset:46176\n"
    "v_add_f32 v241, v241, v199\n"
    "v_mfma_f32_16x16x32_bf16 v[212:215], v[140:143], v[60:63], v[248:251]\n"
    "v_exp_f32 v207, v207\n"
    "v_mfma_f32_16x16x32_bf16 v[212:215], v[136:139], v[56:59], v[212:215]\n"
    "ds_read_b64_tr_b16 v[188:189], v253 offset:42056\n"
    "v_cvt_pk_bf16_f32 v231, v206, v207\n"
    "v_mfma_f32_16x16x32_bf16 v[212:215], v[132:135], v[52:55], v[212:215]\n"
    "v_exp_f32 v208, v208\n"
    "v_mfma_f32_16x16x32_bf16 v[212:215], v[128:131], v[48:51], v[212:215]\n"
    "s_waitcnt lgkmcnt(13)\n"
    "ds_read_b64_tr_b16 v[190:191], v253 offset:46184\n"
    "v_add_f32 v240, v240, v200\n"
    "v_mfma_f32_16x16x32_bf16 a[0:3], v[160:163], v[224:227], a[0:3]\n"
    "v_exp_f32 v209, v209\n"
    "v_mfma_f32_16x16x32_bf16 a[32:35], v[160:163], v[228:231], a[32:35]\n"
    "s_waitcnt lgkmcnt(12)\n"
    "v_cvt_pk_bf16_f32 v232, v208, v209\n"
    "v_add_f32 v240, v240, v201\n"
    "v_mfma_f32_16x16x32_bf16 a[36:39], v[164:167], v[228:231], a[36:39]\n"
    "v_exp_f32 v210, v210\n"
    "v_mfma_f32_16x16x32_bf16 a[4:7], v[164:167], v[224:227], a[4:7]\n"
    "s_waitcnt lgkmcnt(10)\n"
    "v_add_f32 v240, v240, v202\n"
    "v_add_f32 v241, v241, v204\n"
    "v_mfma_f32_16x16x32_bf16 a[8:11], v[168:171], v[224:227], a[8:11]\n"
    "v_exp_f32 v211, v211\n"
    "v_mfma_f32_16x16x32_bf16 a[40:43], v[168:171], v[228:231], a[40:43]\n"
    "s_waitcnt lgkmcnt(8)\n"
    "v_cvt_pk_bf16_f32 v233, v210, v211\n"
    "v_add_f32 v240, v240, v203\n"
    "v_mfma_f32_16x16x32_bf16 a[44:47], v[172:175], v[228:231], a[44:47]\n"
    "s_add_u32 m0, s54, 24768\n"
    "v_exp_f32 v212, v212\n"
    "v_mfma_f32_16x16x32_bf16 a[12:15], v[172:175], v[224:227], a[12:15]\n"
    "buffer_load_dwordx4 v254, s[40:43], s53 offen nt lds\n"
    "v_mfma_f32_16x16x32_bf16 v[216:219], v[144:147], v[32:35], v[248:251]\n"
    "v_exp_f32 v213, v213\n"
    "v_mfma_f32_16x16x32_bf16 v[216:219], v[148:151], v[36:39], v[216:219]\n"
    "v_cvt_pk_bf16_f32 v236, v212, v213\n"
    "v_add_f32 v241, v241, v205\n"
    "v_mfma_f32_16x16x32_bf16 v[216:219], v[152:155], v[40:43], v[216:219]\n"
    "v_exp_f32 v214, v214\n"
    "v_mfma_f32_16x16x32_bf16 v[216:219], v[156:159], v[44:47], v[216:219]\n"
    "v_add_f32 v241, v241, v206\n"
    "v_add_f32 v242, v242, v208\n"
    "v_mfma_f32_16x16x32_bf16 v[220:223], v[156:159], v[60:63], v[248:251]\n"
    "v_exp_f32 v215, v215\n"
    "v_mfma_f32_16x16x32_bf16 v[220:223], v[152:155], v[56:59], v[220:223]\n"
    "v_cvt_pk_bf16_f32 v237, v214, v215\n"
    "v_add_f32 v241, v241, v207\n"
    "v_mfma_f32_16x16x32_bf16 v[220:223], v[148:151], v[52:55], v[220:223]\n"
    "v_exp_f32 v216, v216\n"
    "v_mfma_f32_16x16x32_bf16 v[220:223], v[144:147], v[48:51], v[220:223]\n"
    "s_waitcnt lgkmcnt(6)\n"
    "v_add_f32 v242, v242, v209\n"
    "v_add_f32 v243, v243, v212\n"
    "v_mfma_f32_16x16x32_bf16 a[16:19], v[176:179], v[224:227], a[16:19]\n"
    "v_exp_f32 v217, v217\n"
    "v_mfma_f32_16x16x32_bf16 a[48:51], v[176:179], v[228:231], a[48:51]\n"
    "s_waitcnt lgkmcnt(4)\n"
    "v_cvt_pk_bf16_f32 v234, v216, v217\n"
    "v_add_f32 v242, v242, v210\n"
    "v_mfma_f32_16x16x32_bf16 a[52:55], v[180:183], v[228:231], a[52:55]\n"
    "v_exp_f32 v218, v218\n"
    "v_mfma_f32_16x16x32_bf16 a[20:23], v[180:183], v[224:227], a[20:23]\n"
    "s_waitcnt lgkmcnt(2)\n"
    "buffer_load_dwordx4 v254, s[40:43], s53 offen offset:1024 nt lds\n"
    "v_mfma_f32_16x16x32_bf16 a[24:27], v[184:187], v[224:227], a[24:27]\n"
    "v_exp_f32 v219, v219\n"
    "v_mfma_f32_16x16x32_bf16 a[56:59], v[184:187], v[228:231], a[56:59]\n"
    "s_waitcnt lgkmcnt(0)\n"
    "v_cvt_pk_bf16_f32 v235, v218, v219\n"
    "v_add_f32 v242, v242, v211\n"
    "v_mfma_f32_16x16x32_bf16 a[60:63], v[188:191], v[228:231], a[60:63]\n"
    "v_exp_f32 v220, v220\n"
    "v_mfma_f32_16x16x32_bf16 a[28:31], v[188:191], v[224:227], a[28:31]\n"
    "v_add_f32 v243, v243, v213\n"
    "v_add_f32 v242, v242, v216\n"
    "v_mfma_f32_16x16x32_bf16 v[192:195], v[128:131], v[64:67], v[248:251]\n"
    "v_exp_f32 v221, v221\n"
    "v_mfma_f32_16x16x32_bf16 v[192:195], v[132:135], v[68:71], v[192:195]\n"
    "v_cvt_pk_bf16_f32 v238, v220, v221\n"
    "v_add_f32 v243, v243, v214\n"
    "v_mfma_f32_16x16x32_bf16 v[192:195], v[136:139], v[72:75], v[192:195]\n"
    "v_exp_f32 v222, v222\n"
    "v_mfma_f32_16x16x32_bf16 v[192:195], v[140:143], v[76:79], v[192:195]\n"
    "v_add_f32 v243, v243, v215\n"
    "v_add_f32 v242, v242, v217\n"
    "v_mfma_f32_16x16x32_bf16 v[196:199], v[140:143], v[92:95], v[248:251]\n"
    "v_exp_f32 v223, v223\n"
    "v_mfma_f32_16x16x32_bf16 v[196:199], v[136:139], v[88:91], v[196:199]\n"
    "v_cvt_pk_bf16_f32 v239, v222, v223\n"
    "v_add_f32 v242, v242, v218\n"
    "v_mfma_f32_16x16x32_bf16 v[196:199], v[132:135], v[84:87], v[196:199]\n"
    "v_exp_f32 v192, v192\n"
    "v_mfma_f32_16x16x32_bf16 v[196:199], v[128:131], v[80:83], v[196:199]\n"
    "v_add_f32 v242, v242, v219\n"
    "v_add_f32 v243, v243, v220\n"
    "v_mfma_f32_16x16x32_bf16 a[64:67], v[160:163], v[232:235], a[64:67]\n"
    "v_exp_f32 v193, v193\n"
    "v_mfma_f32_16x16x32_bf16 a[96:99], v[160:163], v[236:239], a[96:99]\n"
    "v_cvt_pk_bf16_f32 v224, v192, v193\n"
    "v_add_f32 v243, v243, v221\n"
    "v_mfma_f32_16x16x32_bf16 a[100:103], v[164:167], v[236:239], a[100:103]\n"
    "s_add_u32 m0, s55, 82560\n"
    "v_exp_f32 v194, v194\n"
    "v_mfma_f32_16x16x32_bf16 a[68:71], v[164:167], v[232:235], a[68:71]\n"
    "buffer_load_dwordx4 v254, s[44:47], s53 offen nt lds\n"
    "v_mfma_f32_16x16x32_bf16 a[72:75], v[168:171], v[232:235], a[72:75]\n"
    "v_exp_f32 v195, v195\n"
    "v_mfma_f32_16x16x32_bf16 a[104:107], v[168:171], v[236:239], a[104:107]\n"
    "v_cvt_pk_bf16_f32 v225, v194, v195\n"
    "v_add_f32 v243, v243, v222\n"
    "v_mfma_f32_16x16x32_bf16 a[108:111], v[172:175], v[236:239], a[108:111]\n"
    "v_exp_f32 v196, v196\n"
    "v_mfma_f32_16x16x32_bf16 a[76:79], v[172:175], v[232:235], a[76:79]\n"
    "v_add_f32 v243, v243, v223\n"
    "v_add_f32 v244, v244, v192\n"
    "v_mfma_f32_16x16x32_bf16 v[200:203], v[144:147], v[64:67], v[248:251]\n"
    "v_exp_f32 v197, v197\n"
    "v_mfma_f32_16x16x32_bf16 v[200:203], v[148:151], v[68:71], v[200:203]\n"
    "v_cvt_pk_bf16_f32 v228, v196, v197\n"
    "v_add_f32 v244, v244, v193\n"
    "v_mfma_f32_16x16x32_bf16 v[200:203], v[152:155], v[72:75], v[200:203]\n"
    "v_exp_f32 v198, v198\n"
    "v_mfma_f32_16x16x32_bf16 v[200:203], v[156:159], v[76:79], v[200:203]\n"
    "v_add_f32 v244, v244, v194\n"
    "v_add_f32 v245, v245, v196\n"
    "v_mfma_f32_16x16x32_bf16 v[204:207], v[156:159], v[92:95], v[248:251]\n"
    "v_exp_f32 v199, v199\n"
    "v_mfma_f32_16x16x32_bf16 v[204:207], v[152:155], v[88:91], v[204:207]\n"
    "v_cvt_pk_bf16_f32 v229, v198, v199\n"
    "v_add_f32 v244, v244, v195\n"
    "v_mfma_f32_16x16x32_bf16 v[204:207], v[148:151], v[84:87], v[204:207]\n"
    "v_exp_f32 v200, v200\n"
    "v_mfma_f32_16x16x32_bf16 v[204:207], v[144:147], v[80:83], v[204:207]\n"
    "v_add_f32 v245, v245, v197\n"
    "v_add_f32 v244, v244, v200\n"
    "v_mfma_f32_16x16x32_bf16 a[80:83], v[176:179], v[232:235], a[80:83]\n"
    "v_exp_f32 v201, v201\n"
    "v_mfma_f32_16x16x32_bf16 a[112:115], v[176:179], v[236:239], a[112:115]\n"
    "v_cvt_pk_bf16_f32 v226, v200, v201\n"
    "v_add_f32 v245, v245, v198\n"
    "v_mfma_f32_16x16x32_bf16 a[116:119], v[180:183], v[236:239], a[116:119]\n"
    "s_add_u32 m0, s55, 82568\n"
    "v_exp_f32 v202, v202\n"
    "v_mfma_f32_16x16x32_bf16 a[84:87], v[180:183], v[232:235], a[84:87]\n"
    "buffer_load_dwordx4 v254, s[44:47], s53 offen offset:1024 nt lds\n"
    "v_mfma_f32_16x16x32_bf16 a[88:91], v[184:187], v[232:235], a[88:91]\n"
    "s_add_u32 s53, s53, 8192\n"
    "v_exp_f32 v203, v203\n"
    "v_mfma_f32_16x16x32_bf16 a[120:123], v[184:187], v[236:239], a[120:123]\n"
    "v_cvt_pk_bf16_f32 v227, v202, v203\n"
    "v_add_f32 v245, v245, v199\n"
    "v_mfma_f32_16x16x32_bf16 a[124:127], v[188:191], v[236:239], a[124:127]\n"
    "v_exp_f32 v204, v204\n"
    "v_mfma_f32_16x16x32_bf16 a[92:95], v[188:191], v[232:235], a[92:95]\n"
    "v_add_f32 v244, v244, v201\n"
    "v_add_f32 v245, v245, v204\n"
    "v_mfma_f32_16x16x32_bf16 v[208:211], v[128:131], v[96:99], v[248:251]\n"
    "v_exp_f32 v205, v205\n"
    "v_mfma_f32_16x16x32_bf16 v[208:211], v[132:135], v[100:103], v[208:211]\n"
    "v_cvt_pk_bf16_f32 v230, v204, v205\n"
    "v_add_f32 v244, v244, v202\n"
    "v_mfma_f32_16x16x32_bf16 v[208:211], v[136:139], v[104:107], v[208:211]\n"
    "v_exp_f32 v206, v206\n"
    "v_mfma_f32_16x16x32_bf16 v[208:211], v[140:143], v[108:111], v[208:211]\n"
    "v_add_f32 v244, v244, v203\n"
    "v_add_f32 v245, v245, v205\n"
    "v_mfma_f32_16x16x32_bf16 v[212:215], v[140:143], v[124:127], v[248:251]\n"
    "v_exp_f32 v207, v207\n"
    "v_mfma_f32_16x16x32_bf16 v[212:215], v[136:139], v[120:123], v[212:215]\n"
    "v_cvt_pk_bf16_f32 v231, v206, v207\n"
    "v_add_f32 v245, v245, v206\n"
    "v_mfma_f32_16x16x32_bf16 v[212:215], v[132:135], v[116:119], v[212:215]\n"
    "v_exp_f32 v208, v208\n"
    "v_mfma_f32_16x16x32_bf16 v[212:215], v[128:131], v[112:115], v[212:215]\n"
    "ds_read_b128 v[140:143], v252 offset:768\n"
    "v_add_f32 v245, v245, v207\n"
    "v_mfma_f32_16x16x32_bf16 a[128:131], v[160:163], v[224:227], a[128:131]\n"
    "v_exp_f32 v209, v209\n"
    "v_mfma_f32_16x16x32_bf16 a[160:163], v[160:163], v[228:231], a[160:163]\n"
    "ds_read_b128 v[136:139], v252 offset:512\n"
    "v_cvt_pk_bf16_f32 v232, v208, v209\n"
    "v_mfma_f32_16x16x32_bf16 a[164:167], v[164:167], v[228:231], a[164:167]\n"
    "v_exp_f32 v210, v210\n"
    "v_mfma_f32_16x16x32_bf16 a[132:135], v[164:167], v[224:227], a[132:135]\n"
    "ds_read_b128 v[132:135], v252 offset:256\n"
    "v_add_f32 v246, v246, v208\n"
    "v_mfma_f32_16x16x32_bf16 a[136:139], v[168:171], v[224:227], a[136:139]\n"
    "v_exp_f32 v211, v211\n"
    "v_mfma_f32_16x16x32_bf16 a[168:171], v[168:171], v[228:231], a[168:171]\n"
    "ds_read_b128 v[128:131], v252 offset:0\n"
    "v_cvt_pk_bf16_f32 v233, v210, v211\n"
    "v_mfma_f32_16x16x32_bf16 a[172:175], v[172:175], v[228:231], a[172:175]\n"
    "v_exp_f32 v212, v212\n"
    "v_mfma_f32_16x16x32_bf16 a[140:143], v[172:175], v[224:227], a[140:143]\n"
    "v_add_f32 v246, v246, v209\n"
    "v_add_f32 v247, v247, v212\n"
    "v_mfma_f32_16x16x32_bf16 v[216:219], v[144:147], v[96:99], v[248:251]\n"
    "v_exp_f32 v213, v213\n"
    "v_mfma_f32_16x16x32_bf16 v[216:219], v[148:151], v[100:103], v[216:219]\n"
    "v_cvt_pk_bf16_f32 v236, v212, v213\n"
    "v_add_f32 v246, v246, v210\n"
    "v_mfma_f32_16x16x32_bf16 v[216:219], v[152:155], v[104:107], v[216:219]\n"
    "v_exp_f32 v214, v214\n"
    "v_mfma_f32_16x16x32_bf16 v[216:219], v[156:159], v[108:111], v[216:219]\n"
    "v_add_f32 v246, v246, v211\n"
    "v_add_f32 v247, v247, v213\n"
    "v_mfma_f32_16x16x32_bf16 v[220:223], v[156:159], v[124:127], v[248:251]\n"
    "v_exp_f32 v215, v215\n"
    "v_mfma_f32_16x16x32_bf16 v[220:223], v[152:155], v[120:123], v[220:223]\n"
    "v_cvt_pk_bf16_f32 v237, v214, v215\n"
    "v_add_f32 v247, v247, v214\n"
    "v_mfma_f32_16x16x32_bf16 v[220:223], v[148:151], v[116:119], v[220:223]\n"
    "v_exp_f32 v216, v216\n"
    "v_mfma_f32_16x16x32_bf16 v[220:223], v[144:147], v[112:115], v[220:223]\n"
    "ds_read_b128 v[156:159], v252 offset:4896\n"
    "v_add_f32 v247, v247, v215\n"
    "v_mfma_f32_16x16x32_bf16 a[144:147], v[176:179], v[224:227], a[144:147]\n"
    "v_exp_f32 v217, v217\n"
    "v_mfma_f32_16x16x32_bf16 a[176:179], v[176:179], v[228:231], a[176:179]\n"
    "ds_read_b128 v[152:155], v252 offset:4640\n"
    "v_cvt_pk_bf16_f32 v234, v216, v217\n"
    "v_mfma_f32_16x16x32_bf16 a[180:183], v[180:183], v[228:231], a[180:183]\n"
    "v_exp_f32 v218, v218\n"
    "v_mfma_f32_16x16x32_bf16 a[148:151], v[180:183], v[224:227], a[148:151]\n"
    "ds_read_b128 v[148:151], v252 offset:4384\n"
    "v_add_f32 v246, v246, v216\n"
    "v_mfma_f32_16x16x32_bf16 a[152:155], v[184:187], v[224:227], a[152:155]\n"
    "v_exp_f32 v219, v219\n"
    "v_mfma_f32_16x16x32_bf16 a[184:187], v[184:187], v[228:231], a[184:187]\n"
    "ds_read_b128 v[144:147], v252 offset:4128\n"
    "v_cvt_pk_bf16_f32 v235, v218, v219\n"
    "v_mfma_f32_16x16x32_bf16 a[188:191], v[188:191], v[228:231], a[188:191]\n"
    "v_exp_f32 v220, v220\n"
    "v_mfma_f32_16x16x32_bf16 a[156:159], v[188:191], v[224:227], a[156:159]\n"
    "s_waitcnt lgkmcnt(4)\n"
    "v_add_f32 v246, v246, v217\n"
    "v_add_f32 v247, v247, v220\n"
    "v_mfma_f32_16x16x32_bf16 v[192:195], v[128:131], v[0:3], v[248:251]\n"
    "v_exp_f32 v221, v221\n"
    "v_mfma_f32_16x16x32_bf16 v[192:195], v[132:135], v[4:7], v[192:195]\n"
    "v_cvt_pk_bf16_f32 v238, v220, v221\n"
    "v_add_f32 v246, v246, v218\n"
    "v_mfma_f32_16x16x32_bf16 v[192:195], v[136:139], v[8:11], v[192:195]\n"
    "v_exp_f32 v222, v222\n"
    "v_mfma_f32_16x16x32_bf16 v[192:195], v[140:143], v[12:15], v[192:195]\n"
    "v_add_f32 v246, v246, v219\n"
    "v_add_f32 v247, v247, v221\n"
    "v_mfma_f32_16x16x32_bf16 v[196:199], v[140:143], v[28:31], v[248:251]\n"
    "v_exp_f32 v223, v223\n"
    "v_mfma_f32_16x16x32_bf16 v[196:199], v[136:139], v[24:27], v[196:199]\n"
    "v_cvt_pk_bf16_f32 v239, v222, v223\n"
    "v_add_f32 v247, v247, v222\n"
    "v_mfma_f32_16x16x32_bf16 v[196:199], v[132:135], v[20:23], v[196:199]\n"
    "v_exp_f32 v192, v192\n"
    "v_mfma_f32_16x16x32_bf16 v[196:199], v[128:131], v[16:19], v[196:199]\n"
    "v_add_f32 v247, v247, v223\n"
    "v_add_f32 v240, v240, v192\n"
    "v_mfma_f32_16x16x32_bf16 a[192:195], v[160:163], v[232:235], a[192:195]\n"
    "v_exp_f32 v193, v193\n"
    "v_mfma_f32_16x16x32_bf16 a[224:227], v[160:163], v[236:239], a[224:227]\n"
    "ds_read_b64_tr_b16 v[160:161], v253 offset:0\n"
    "v_cvt_pk_bf16_f32 v224, v192, v193\n"
    "v_mfma_f32_16x16x32_bf16 a[228:231], v[164:167], v[236:239], a[228:231]\n"
    "v_exp_f32 v194, v194\n"
    "v_mfma_f32_16x16x32_bf16 a[196:199], v[164:167], v[232:235], a[196:199]\n"
    "ds_read_b64_tr_b16 v[162:163], v253 offset:4128\n"
    "v_add_f32 v240, v240, v193\n"
    "v_mfma_f32_16x16x32_bf16 a[200:203], v[168:171], v[232:235], a[200:203]\n"
    "v_exp_f32 v195, v195\n"
    "v_mfma_f32_16x16x32_bf16 a[232:235], v[168:171], v[236:239], a[232:235]\n"
    "ds_read_b64_tr_b16 v[164:165], v253 offset:8\n"
    "v_cvt_pk_bf16_f32 v225, v194, v195\n"
    "v_mfma_f32_16x16x32_bf16 a[236:239], v[172:175], v[236:239], a[236:239]\n"
    "v_exp_f32 v196, v196\n"
    "v_mfma_f32_16x16x32_bf16 a[204:207], v[172:175], v[232:235], a[204:207]\n"
    "s_waitcnt lgkmcnt(3)\n"
    "ds_read_b64_tr_b16 v[166:167], v253 offset:4136\n"
    "v_add_f32 v240, v240, v194\n"
    "v_mfma_f32_16x16x32_bf16 v[200:203], v[144:147], v[0:3], v[248:251]\n"
    "v_exp_f32 v197, v197\n"
    "v_mfma_f32_16x16x32_bf16 v[200:203], v[148:151], v[4:7], v[200:203]\n"
    "ds_read_b64_tr_b16 v[168:169], v253 offset:256\n"
    "v_cvt_pk_bf16_f32 v228, v196, v197\n"
    "v_mfma_f32_16x16x32_bf16 v[200:203], v[152:155], v[8:11], v[200:203]\n"
    "v_exp_f32 v198, v198\n"
    "v_mfma_f32_16x16x32_bf16 v[200:203], v[156:159], v[12:15], v[200:203]\n"
    "ds_read_b64_tr_b16 v[170:171], v253 offset:4384\n"
    "v_add_f32 v240, v240, v195\n"
    "v_mfma_f32_16x16x32_bf16 v[204:207], v[156:159], v[28:31], v[248:251]\n"
    "v_exp_f32 v199, v199\n"
    "v_mfma_f32_16x16x32_bf16 v[204:207], v[152:155], v[24:27], v[204:207]\n"
    "ds_read_b64_tr_b16 v[172:173], v253 offset:264\n"
    "v_cvt_pk_bf16_f32 v229, v198, v199\n"
    "v_mfma_f32_16x16x32_bf16 v[204:207], v[148:151], v[20:23], v[204:207]\n"
    "v_exp_f32 v200, v200\n"
    "v_mfma_f32_16x16x32_bf16 v[204:207], v[144:147], v[16:19], v[204:207]\n"
    "ds_read_b64_tr_b16 v[174:175], v253 offset:4392\n"
    "v_add_f32 v241, v241, v196\n"
    "v_mfma_f32_16x16x32_bf16 a[208:211], v[176:179], v[232:235], a[208:211]\n"
    "v_exp_f32 v201, v201\n"
    "v_mfma_f32_16x16x32_bf16 a[240:243], v[176:179], v[236:239], a[240:243]\n"
    "ds_read_b64_tr_b16 v[176:177], v253 offset:512\n"
    "v_cvt_pk_bf16_f32 v226, v200, v201\n"
    "v_mfma_f32_16x16x32_bf16 a[244:247], v[180:183], v[236:239], a[244:247]\n"
    "v_exp_f32 v202, v202\n"
    "v_mfma_f32_16x16x32_bf16 a[212:215], v[180:183], v[232:235], a[212:215]\n"
    "ds_read_b64_tr_b16 v[178:179], v253 offset:4640\n"
    "v_add_f32 v241, v241, v197\n"
    "v_mfma_f32_16x16x32_bf16 a[216:219], v[184:187], v[232:235], a[216:219]\n"
    "v_exp_f32 v203, v203\n"
    "v_mfma_f32_16x16x32_bf16 a[248:251], v[184:187], v[236:239], a[248:251]\n"
    "ds_read_b64_tr_b16 v[180:181], v253 offset:520\n"
    "v_cvt_pk_bf16_f32 v227, v202, v203\n"
    "v_mfma_f32_16x16x32_bf16 a[252:255], v[188:191], v[236:239], a[252:255]\n"
    "v_exp_f32 v204, v204\n"
    "v_mfma_f32_16x16x32_bf16 a[220:223], v[188:191], v[232:235], a[220:223]\n"
    "ds_read_b64_tr_b16 v[182:183], v253 offset:4648\n"
    "v_add_f32 v241, v241, v198\n"
    "v_mfma_f32_16x16x32_bf16 v[208:211], v[128:131], v[32:35], v[248:251]\n"
    "v_exp_f32 v205, v205\n"
    "v_mfma_f32_16x16x32_bf16 v[208:211], v[132:135], v[36:39], v[208:211]\n"
    "ds_read_b64_tr_b16 v[184:185], v253 offset:768\n"
    "v_cvt_pk_bf16_f32 v230, v204, v205\n"
    "v_mfma_f32_16x16x32_bf16 v[208:211], v[136:139], v[40:43], v[208:211]\n"
    "v_exp_f32 v206, v206\n"
    "v_mfma_f32_16x16x32_bf16 v[208:211], v[140:143], v[44:47], v[208:211]\n"
    "ds_read_b64_tr_b16 v[186:187], v253 offset:4896\n"
    "v_add_f32 v241, v241, v199\n"
    "v_mfma_f32_16x16x32_bf16 v[212:215], v[140:143], v[60:63], v[248:251]\n"
    "v_exp_f32 v207, v207\n"
    "v_mfma_f32_16x16x32_bf16 v[212:215], v[136:139], v[56:59], v[212:215]\n"
    "ds_read_b64_tr_b16 v[188:189], v253 offset:776\n"
    "v_cvt_pk_bf16_f32 v231, v206, v207\n"
    "v_mfma_f32_16x16x32_bf16 v[212:215], v[132:135], v[52:55], v[212:215]\n"
    "v_exp_f32 v208, v208\n"
    "v_mfma_f32_16x16x32_bf16 v[212:215], v[128:131], v[48:51], v[212:215]\n"
    "s_waitcnt lgkmcnt(13)\n"
    "ds_read_b64_tr_b16 v[190:191], v253 offset:4904\n"
    "v_add_f32 v240, v240, v200\n"
    "v_mfma_f32_16x16x32_bf16 a[0:3], v[160:163], v[224:227], a[0:3]\n"
    "v_exp_f32 v209, v209\n"
    "v_mfma_f32_16x16x32_bf16 a[32:35], v[160:163], v[228:231], a[32:35]\n"
    "s_waitcnt lgkmcnt(12)\n"
    "v_cvt_pk_bf16_f32 v232, v208, v209\n"
    "v_add_f32 v240, v240, v201\n"
    "v_mfma_f32_16x16x32_bf16 a[36:39], v[164:167], v[228:231], a[36:39]\n"
    "v_exp_f32 v210, v210\n"
    "v_mfma_f32_16x16x32_bf16 a[4:7], v[164:167], v[224:227], a[4:7]\n"
    "s_waitcnt lgkmcnt(10)\n"
    "v_add_f32 v240, v240, v202\n"
    "v_add_f32 v241, v241, v204\n"
    "v_mfma_f32_16x16x32_bf16 a[8:11], v[168:171], v[224:227], a[8:11]\n"
    "v_exp_f32 v211, v211\n"
    "v_mfma_f32_16x16x32_bf16 a[40:43], v[168:171], v[228:231], a[40:43]\n"
    "s_waitcnt lgkmcnt(8)\n"
    "v_cvt_pk_bf16_f32 v233, v210, v211\n"
    "v_add_f32 v240, v240, v203\n"
    "v_mfma_f32_16x16x32_bf16 a[44:47], v[172:175], v[228:231], a[44:47]\n"
    "s_add_u32 m0, s54, 33024\n"
    "v_exp_f32 v212, v212\n"
    "v_mfma_f32_16x16x32_bf16 a[12:15], v[172:175], v[224:227], a[12:15]\n"
    "buffer_load_dwordx4 v254, s[40:43], s53 offen nt lds\n"
    "v_mfma_f32_16x16x32_bf16 v[216:219], v[144:147], v[32:35], v[248:251]\n"
    "v_exp_f32 v213, v213\n"
    "v_mfma_f32_16x16x32_bf16 v[216:219], v[148:151], v[36:39], v[216:219]\n"
    "v_cvt_pk_bf16_f32 v236, v212, v213\n"
    "v_add_f32 v241, v241, v205\n"
    "v_mfma_f32_16x16x32_bf16 v[216:219], v[152:155], v[40:43], v[216:219]\n"
    "v_exp_f32 v214, v214\n"
    "v_mfma_f32_16x16x32_bf16 v[216:219], v[156:159], v[44:47], v[216:219]\n"
    "v_add_f32 v241, v241, v206\n"
    "v_add_f32 v242, v242, v208\n"
    "v_mfma_f32_16x16x32_bf16 v[220:223], v[156:159], v[60:63], v[248:251]\n"
    "v_exp_f32 v215, v215\n"
    "v_mfma_f32_16x16x32_bf16 v[220:223], v[152:155], v[56:59], v[220:223]\n"
    "v_cvt_pk_bf16_f32 v237, v214, v215\n"
    "v_add_f32 v241, v241, v207\n"
    "v_mfma_f32_16x16x32_bf16 v[220:223], v[148:151], v[52:55], v[220:223]\n"
    "v_exp_f32 v216, v216\n"
    "v_mfma_f32_16x16x32_bf16 v[220:223], v[144:147], v[48:51], v[220:223]\n"
    "s_waitcnt lgkmcnt(6)\n"
    "v_add_f32 v242, v242, v209\n"
    "v_add_f32 v243, v243, v212\n"
    "v_mfma_f32_16x16x32_bf16 a[16:19], v[176:179], v[224:227], a[16:19]\n"
    "v_exp_f32 v217, v217\n"
    "v_mfma_f32_16x16x32_bf16 a[48:51], v[176:179], v[228:231], a[48:51]\n"
    "s_waitcnt lgkmcnt(4)\n"
    "v_cvt_pk_bf16_f32 v234, v216, v217\n"
    "v_add_f32 v242, v242, v210\n"
    "v_mfma_f32_16x16x32_bf16 a[52:55], v[180:183], v[228:231], a[52:55]\n"
    "v_exp_f32 v218, v218\n"
    "v_mfma_f32_16x16x32_bf16 a[20:23], v[180:183], v[224:227], a[20:23]\n"
    "s_waitcnt lgkmcnt(2)\n"
    "buffer_load_dwordx4 v254, s[40:43], s53 offen offset:1024 nt lds\n"
    "v_mfma_f32_16x16x32_bf16 a[24:27], v[184:187], v[224:227], a[24:27]\n"
    "v_exp_f32 v219, v219\n"
    "v_mfma_f32_16x16x32_bf16 a[56:59], v[184:187], v[228:231], a[56:59]\n"
    "s_waitcnt lgkmcnt(0)\n"
    "v_cvt_pk_bf16_f32 v235, v218, v219\n"
    "v_add_f32 v242, v242, v211\n"
    "v_mfma_f32_16x16x32_bf16 a[60:63], v[188:191], v[228:231], a[60:63]\n"
    "v_exp_f32 v220, v220\n"
    "v_mfma_f32_16x16x32_bf16 a[28:31], v[188:191], v[224:227], a[28:31]\n"
    "v_add_f32 v243, v243, v213\n"
    "v_add_f32 v242, v242, v216\n"
    "v_mfma_f32_16x16x32_bf16 v[192:195], v[128:131], v[64:67], v[248:251]\n"
    "v_exp_f32 v221, v221\n"
    "v_mfma_f32_16x16x32_bf16 v[192:195], v[132:135], v[68:71], v[192:195]\n"
    "v_cvt_pk_bf16_f32 v238, v220, v221\n"
    "v_add_f32 v243, v243, v214\n"
    "v_mfma_f32_16x16x32_bf16 v[192:195], v[136:139], v[72:75], v[192:195]\n"
    "v_exp_f32 v222, v222\n"
    "v_mfma_f32_16x16x32_bf16 v[192:195], v[140:143], v[76:79], v[192:195]\n"
    "v_add_f32 v243, v243, v215\n"
    "v_add_f32 v242, v242, v217\n"
    "v_mfma_f32_16x16x32_bf16 v[196:199], v[140:143], v[92:95], v[248:251]\n"
    "v_exp_f32 v223, v223\n"
    "v_mfma_f32_16x16x32_bf16 v[196:199], v[136:139], v[88:91], v[196:199]\n"
    "v_cvt_pk_bf16_f32 v239, v222, v223\n"
    "v_add_f32 v242, v242, v218\n"
    "v_mfma_f32_16x16x32_bf16 v[196:199], v[132:135], v[84:87], v[196:199]\n"
    "v_exp_f32 v192, v192\n"
    "v_mfma_f32_16x16x32_bf16 v[196:199], v[128:131], v[80:83], v[196:199]\n"
    "v_add_f32 v242, v242, v219\n"
    "v_add_f32 v243, v243, v220\n"
    "v_mfma_f32_16x16x32_bf16 a[64:67], v[160:163], v[232:235], a[64:67]\n"
    "v_exp_f32 v193, v193\n"
    "v_mfma_f32_16x16x32_bf16 a[96:99], v[160:163], v[236:239], a[96:99]\n"
    "v_cvt_pk_bf16_f32 v224, v192, v193\n"
    "v_add_f32 v243, v243, v221\n"
    "v_mfma_f32_16x16x32_bf16 a[100:103], v[164:167], v[236:239], a[100:103]\n"
    "s_add_u32 m0, s55, 90816\n"
    "v_exp_f32 v194, v194\n"
    "v_mfma_f32_16x16x32_bf16 a[68:71], v[164:167], v[232:235], a[68:71]\n"
    "buffer_load_dwordx4 v254, s[44:47], s53 offen nt lds\n"
    "v_mfma_f32_16x16x32_bf16 a[72:75], v[168:171], v[232:235], a[72:75]\n"
    "v_exp_f32 v195, v195\n"
    "v_mfma_f32_16x16x32_bf16 a[104:107], v[168:171], v[236:239], a[104:107]\n"
    "v_cvt_pk_bf16_f32 v225, v194, v195\n"
    "v_add_f32 v243, v243, v222\n"
    "v_mfma_f32_16x16x32_bf16 a[108:111], v[172:175], v[236:239], a[108:111]\n"
    "v_exp_f32 v196, v196\n"
    "v_mfma_f32_16x16x32_bf16 a[76:79], v[172:175], v[232:235], a[76:79]\n"
    "v_add_f32 v243, v243, v223\n"
    "v_add_f32 v244, v244, v192\n"
    "v_mfma_f32_16x16x32_bf16 v[200:203], v[144:147], v[64:67], v[248:251]\n"
    "v_exp_f32 v197, v197\n"
    "v_mfma_f32_16x16x32_bf16 v[200:203], v[148:151], v[68:71], v[200:203]\n"
    "v_cvt_pk_bf16_f32 v228, v196, v197\n"
    "v_add_f32 v244, v244, v193\n"
    "v_mfma_f32_16x16x32_bf16 v[200:203], v[152:155], v[72:75], v[200:203]\n"
    "v_exp_f32 v198, v198\n"
    "v_mfma_f32_16x16x32_bf16 v[200:203], v[156:159], v[76:79], v[200:203]\n"
    "v_add_f32 v244, v244, v194\n"
    "v_add_f32 v245, v245, v196\n"
    "v_mfma_f32_16x16x32_bf16 v[204:207], v[156:159], v[92:95], v[248:251]\n"
    "v_exp_f32 v199, v199\n"
    "v_mfma_f32_16x16x32_bf16 v[204:207], v[152:155], v[88:91], v[204:207]\n"
    "v_cvt_pk_bf16_f32 v229, v198, v199\n"
    "v_add_f32 v244, v244, v195\n"
    "v_mfma_f32_16x16x32_bf16 v[204:207], v[148:151], v[84:87], v[204:207]\n"
    "v_exp_f32 v200, v200\n"
    "v_mfma_f32_16x16x32_bf16 v[204:207], v[144:147], v[80:83], v[204:207]\n"
    "v_add_f32 v245, v245, v197\n"
    "v_add_f32 v244, v244, v200\n"
    "v_mfma_f32_16x16x32_bf16 a[80:83], v[176:179], v[232:235], a[80:83]\n"
    "v_exp_f32 v201, v201\n"
    "v_mfma_f32_16x16x32_bf16 a[112:115], v[176:179], v[236:239], a[112:115]\n"
    "v_cvt_pk_bf16_f32 v226, v200, v201\n"
    "v_add_f32 v245, v245, v198\n"
    "v_mfma_f32_16x16x32_bf16 a[116:119], v[180:183], v[236:239], a[116:119]\n"
    "s_add_u32 m0, s55, 90824\n"
    "v_exp_f32 v202, v202\n"
    "v_mfma_f32_16x16x32_bf16 a[84:87], v[180:183], v[232:235], a[84:87]\n"
    "buffer_load_dwordx4 v254, s[44:47], s53 offen offset:1024 nt lds\n"
    "v_mfma_f32_16x16x32_bf16 a[88:91], v[184:187], v[232:235], a[88:91]\n"
    "s_add_u32 s53, s53, 8192\n"
    "v_exp_f32 v203, v203\n"
    "v_mfma_f32_16x16x32_bf16 a[120:123], v[184:187], v[236:239], a[120:123]\n"
    "v_cvt_pk_bf16_f32 v227, v202, v203\n"
    "v_add_f32 v245, v245, v199\n"
    "v_mfma_f32_16x16x32_bf16 a[124:127], v[188:191], v[236:239], a[124:127]\n"
    "v_exp_f32 v204, v204\n"
    "v_mfma_f32_16x16x32_bf16 a[92:95], v[188:191], v[232:235], a[92:95]\n"
    "v_add_f32 v244, v244, v201\n"
    "v_add_f32 v245, v245, v204\n"
    "v_mfma_f32_16x16x32_bf16 v[208:211], v[128:131], v[96:99], v[248:251]\n"
    "v_exp_f32 v205, v205\n"
    "v_mfma_f32_16x16x32_bf16 v[208:211], v[132:135], v[100:103], v[208:211]\n"
    "v_cvt_pk_bf16_f32 v230, v204, v205\n"
    "v_add_f32 v244, v244, v202\n"
    "v_mfma_f32_16x16x32_bf16 v[208:211], v[136:139], v[104:107], v[208:211]\n"
    "s_waitcnt vmcnt(8)\n"
    "v_exp_f32 v206, v206\n"
    "v_mfma_f32_16x16x32_bf16 v[208:211], v[140:143], v[108:111], v[208:211]\n"
    "s_barrier\n"
    "v_add_f32 v244, v244, v203\n"
    "v_add_f32 v245, v245, v205\n"
    "v_mfma_f32_16x16x32_bf16 v[212:215], v[140:143], v[124:127], v[248:251]\n"
    "v_exp_f32 v207, v207\n"
    "v_mfma_f32_16x16x32_bf16 v[212:215], v[136:139], v[120:123], v[212:215]\n"
    "v_cvt_pk_bf16_f32 v231, v206, v207\n"
    "v_add_f32 v245, v245, v206\n"
    "v_mfma_f32_16x16x32_bf16 v[212:215], v[132:135], v[116:119], v[212:215]\n"
    "v_exp_f32 v208, v208\n"
    "v_mfma_f32_16x16x32_bf16 v[212:215], v[128:131], v[112:115], v[212:215]\n"
    "ds_read_b128 v[140:143], v252 offset:9024\n"
    "v_add_f32 v245, v245, v207\n"
    "v_mfma_f32_16x16x32_bf16 a[128:131], v[160:163], v[224:227], a[128:131]\n"
    "v_exp_f32 v209, v209\n"
    "v_mfma_f32_16x16x32_bf16 a[160:163], v[160:163], v[228:231], a[160:163]\n"
    "ds_read_b128 v[136:139], v252 offset:8768\n"
    "v_cvt_pk_bf16_f32 v232, v208, v209\n"
    "v_mfma_f32_16x16x32_bf16 a[164:167], v[164:167], v[228:231], a[164:167]\n"
    "v_exp_f32 v210, v210\n"
    "v_mfma_f32_16x16x32_bf16 a[132:135], v[164:167], v[224:227], a[132:135]\n"
    "ds_read_b128 v[132:135], v252 offset:8512\n"
    "v_add_f32 v246, v246, v208\n"
    "v_mfma_f32_16x16x32_bf16 a[136:139], v[168:171], v[224:227], a[136:139]\n"
    "v_exp_f32 v211, v211\n"
    "v_mfma_f32_16x16x32_bf16 a[168:171], v[168:171], v[228:231], a[168:171]\n"
    "ds_read_b128 v[128:131], v252 offset:8256\n"
    "v_cvt_pk_bf16_f32 v233, v210, v211\n"
    "v_mfma_f32_16x16x32_bf16 a[172:175], v[172:175], v[228:231], a[172:175]\n"
    "v_exp_f32 v212, v212\n"
    "v_mfma_f32_16x16x32_bf16 a[140:143], v[172:175], v[224:227], a[140:143]\n"
    "v_add_f32 v246, v246, v209\n"
    "v_add_f32 v247, v247, v212\n"
    "v_mfma_f32_16x16x32_bf16 v[216:219], v[144:147], v[96:99], v[248:251]\n"
    "v_exp_f32 v213, v213\n"
    "v_mfma_f32_16x16x32_bf16 v[216:219], v[148:151], v[100:103], v[216:219]\n"
    "v_cvt_pk_bf16_f32 v236, v212, v213\n"
    "v_add_f32 v246, v246, v210\n"
    "v_mfma_f32_16x16x32_bf16 v[216:219], v[152:155], v[104:107], v[216:219]\n"
    "v_exp_f32 v214, v214\n"
    "v_mfma_f32_16x16x32_bf16 v[216:219], v[156:159], v[108:111], v[216:219]\n"
    "v_add_f32 v246, v246, v211\n"
    "v_add_f32 v247, v247, v213\n"
    "v_mfma_f32_16x16x32_bf16 v[220:223], v[156:159], v[124:127], v[248:251]\n"
    "v_exp_f32 v215, v215\n"
    "v_mfma_f32_16x16x32_bf16 v[220:223], v[152:155], v[120:123], v[220:223]\n"
    "v_cvt_pk_bf16_f32 v237, v214, v215\n"
    "v_add_f32 v247, v247, v214\n"
    "v_mfma_f32_16x16x32_bf16 v[220:223], v[148:151], v[116:119], v[220:223]\n"
    "v_exp_f32 v216, v216\n"
    "v_mfma_f32_16x16x32_bf16 v[220:223], v[144:147], v[112:115], v[220:223]\n"
    "ds_read_b128 v[156:159], v252 offset:13152\n"
    "v_add_f32 v247, v247, v215\n"
    "v_mfma_f32_16x16x32_bf16 a[144:147], v[176:179], v[224:227], a[144:147]\n"
    "v_exp_f32 v217, v217\n"
    "v_mfma_f32_16x16x32_bf16 a[176:179], v[176:179], v[228:231], a[176:179]\n"
    "ds_read_b128 v[152:155], v252 offset:12896\n"
    "v_cvt_pk_bf16_f32 v234, v216, v217\n"
    "v_mfma_f32_16x16x32_bf16 a[180:183], v[180:183], v[228:231], a[180:183]\n"
    "v_exp_f32 v218, v218\n"
    "v_mfma_f32_16x16x32_bf16 a[148:151], v[180:183], v[224:227], a[148:151]\n"
    "ds_read_b128 v[148:151], v252 offset:12640\n"
    "v_add_f32 v246, v246, v216\n"
    "v_mfma_f32_16x16x32_bf16 a[152:155], v[184:187], v[224:227], a[152:155]\n"
    "v_exp_f32 v219, v219\n"
    "v_mfma_f32_16x16x32_bf16 a[184:187], v[184:187], v[228:231], a[184:187]\n"
    "ds_read_b128 v[144:147], v252 offset:12384\n"
    "v_cvt_pk_bf16_f32 v235, v218, v219\n"
    "v_mfma_f32_16x16x32_bf16 a[188:191], v[188:191], v[228:231], a[188:191]\n"
    "v_exp_f32 v220, v220\n"
    "v_mfma_f32_16x16x32_bf16 a[156:159], v[188:191], v[224:227], a[156:159]\n"
    "s_waitcnt lgkmcnt(4)\n"
    "v_add_f32 v246, v246, v217\n"
    "v_add_f32 v247, v247, v220\n"
    "v_mfma_f32_16x16x32_bf16 v[192:195], v[128:131], v[0:3], v[248:251]\n"
    "v_exp_f32 v221, v221\n"
    "v_mfma_f32_16x16x32_bf16 v[192:195], v[132:135], v[4:7], v[192:195]\n"
    "v_cvt_pk_bf16_f32 v238, v220, v221\n"
    "v_add_f32 v246, v246, v218\n"
    "v_mfma_f32_16x16x32_bf16 v[192:195], v[136:139], v[8:11], v[192:195]\n"
    "v_exp_f32 v222, v222\n"
    "v_mfma_f32_16x16x32_bf16 v[192:195], v[140:143], v[12:15], v[192:195]\n"
    "v_add_f32 v246, v246, v219\n"
    "v_add_f32 v247, v247, v221\n"
    "v_mfma_f32_16x16x32_bf16 v[196:199], v[140:143], v[28:31], v[248:251]\n"
    "v_exp_f32 v223, v223\n"
    "v_mfma_f32_16x16x32_bf16 v[196:199], v[136:139], v[24:27], v[196:199]\n"
    "v_cvt_pk_bf16_f32 v239, v222, v223\n"
    "v_add_f32 v247, v247, v222\n"
    "v_mfma_f32_16x16x32_bf16 v[196:199], v[132:135], v[20:23], v[196:199]\n"
    "v_exp_f32 v192, v192\n"
    "v_mfma_f32_16x16x32_bf16 v[196:199], v[128:131], v[16:19], v[196:199]\n"
    "v_add_f32 v247, v247, v223\n"
    "v_add_f32 v240, v240, v192\n"
    "v_mfma_f32_16x16x32_bf16 a[192:195], v[160:163], v[232:235], a[192:195]\n"
    "v_exp_f32 v193, v193\n"
    "v_mfma_f32_16x16x32_bf16 a[224:227], v[160:163], v[236:239], a[224:227]\n"
    "ds_read_b64_tr_b16 v[160:161], v253 offset:8256\n"
    "v_cvt_pk_bf16_f32 v224, v192, v193\n"
    "v_mfma_f32_16x16x32_bf16 a[228:231], v[164:167], v[236:239], a[228:231]\n"
    "v_exp_f32 v194, v194\n"
    "v_mfma_f32_16x16x32_bf16 a[196:199], v[164:167], v[232:235], a[196:199]\n"
    "ds_read_b64_tr_b16 v[162:163], v253 offset:12384\n"
    "v_add_f32 v240, v240, v193\n"
    "v_mfma_f32_16x16x32_bf16 a[200:203], v[168:171], v[232:235], a[200:203]\n"
    "v_exp_f32 v195, v195\n"
    "v_mfma_f32_16x16x32_bf16 a[232:235], v[168:171], v[236:239], a[232:235]\n"
    "ds_read_b64_tr_b16 v[164:165], v253 offset:8264\n"
    "v_cvt_pk_bf16_f32 v225, v194, v195\n"
    "v_mfma_f32_16x16x32_bf16 a[236:239], v[172:175], v[236:239], a[236:239]\n"
    "v_exp_f32 v196, v196\n"
    "v_mfma_f32_16x16x32_bf16 a[204:207], v[172:175], v[232:235], a[204:207]\n"
    "s_waitcnt lgkmcnt(3)\n"
    "ds_read_b64_tr_b16 v[166:167], v253 offset:12392\n"
    "v_add_f32 v240, v240, v194\n"
    "v_mfma_f32_16x16x32_bf16 v[200:203], v[144:147], v[0:3], v[248:251]\n"
    "v_exp_f32 v197, v197\n"
    "v_mfma_f32_16x16x32_bf16 v[200:203], v[148:151], v[4:7], v[200:203]\n"
    "ds_read_b64_tr_b16 v[168:169], v253 offset:8512\n"
    "v_cvt_pk_bf16_f32 v228, v196, v197\n"
    "v_mfma_f32_16x16x32_bf16 v[200:203], v[152:155], v[8:11], v[200:203]\n"
    "v_exp_f32 v198, v198\n"
    "v_mfma_f32_16x16x32_bf16 v[200:203], v[156:159], v[12:15], v[200:203]\n"
    "ds_read_b64_tr_b16 v[170:171], v253 offset:12640\n"
    "v_add_f32 v240, v240, v195\n"
    "v_mfma_f32_16x16x32_bf16 v[204:207], v[156:159], v[28:31], v[248:251]\n"
    "v_exp_f32 v199, v199\n"
    "v_mfma_f32_16x16x32_bf16 v[204:207], v[152:155], v[24:27], v[204:207]\n"
    "ds_read_b64_tr_b16 v[172:173], v253 offset:8520\n"
    "v_cvt_pk_bf16_f32 v229, v198, v199\n"
    "v_mfma_f32_16x16x32_bf16 v[204:207], v[148:151], v[20:23], v[204:207]\n"
    "v_exp_f32 v200, v200\n"
    "v_mfma_f32_16x16x32_bf16 v[204:207], v[144:147], v[16:19], v[204:207]\n"
    "ds_read_b64_tr_b16 v[174:175], v253 offset:12648\n"
    "v_add_f32 v241, v241, v196\n"
    "v_mfma_f32_16x16x32_bf16 a[208:211], v[176:179], v[232:235], a[208:211]\n"
    "v_exp_f32 v201, v201\n"
    "v_mfma_f32_16x16x32_bf16 a[240:243], v[176:179], v[236:239], a[240:243]\n"
    "ds_read_b64_tr_b16 v[176:177], v253 offset:8768\n"
    "v_cvt_pk_bf16_f32 v226, v200, v201\n"
    "v_mfma_f32_16x16x32_bf16 a[244:247], v[180:183], v[236:239], a[244:247]\n"
    "v_exp_f32 v202, v202\n"
    "v_mfma_f32_16x16x32_bf16 a[212:215], v[180:183], v[232:235], a[212:215]\n"
    "ds_read_b64_tr_b16 v[178:179], v253 offset:12896\n"
    "v_add_f32 v241, v241, v197\n"
    "v_mfma_f32_16x16x32_bf16 a[216:219], v[184:187], v[232:235], a[216:219]\n"
    "v_exp_f32 v203, v203\n"
    "v_mfma_f32_16x16x32_bf16 a[248:251], v[184:187], v[236:239], a[248:251]\n"
    "ds_read_b64_tr_b16 v[180:181], v253 offset:8776\n"
    "v_cvt_pk_bf16_f32 v227, v202, v203\n"
    "v_mfma_f32_16x16x32_bf16 a[252:255], v[188:191], v[236:239], a[252:255]\n"
    "v_exp_f32 v204, v204\n"
    "v_mfma_f32_16x16x32_bf16 a[220:223], v[188:191], v[232:235], a[220:223]\n"
    "ds_read_b64_tr_b16 v[182:183], v253 offset:12904\n"
    "v_add_f32 v241, v241, v198\n"
    "v_mfma_f32_16x16x32_bf16 v[208:211], v[128:131], v[32:35], v[248:251]\n"
    "v_exp_f32 v205, v205\n"
    "v_mfma_f32_16x16x32_bf16 v[208:211], v[132:135], v[36:39], v[208:211]\n"
    "ds_read_b64_tr_b16 v[184:185], v253 offset:9024\n"
    "v_cvt_pk_bf16_f32 v230, v204, v205\n"
    "v_mfma_f32_16x16x32_bf16 v[208:211], v[136:139], v[40:43], v[208:211]\n"
    "v_exp_f32 v206, v206\n"
    "v_mfma_f32_16x16x32_bf16 v[208:211], v[140:143], v[44:47], v[208:211]\n"
    "ds_read_b64_tr_b16 v[186:187], v253 offset:13152\n"
    "v_add_f32 v241, v241, v199\n"
    "v_mfma_f32_16x16x32_bf16 v[212:215], v[140:143], v[60:63], v[248:251]\n"
    "v_exp_f32 v207, v207\n"
    "v_mfma_f32_16x16x32_bf16 v[212:215], v[136:139], v[56:59], v[212:215]\n"
    "ds_read_b64_tr_b16 v[188:189], v253 offset:9032\n"
    "v_cvt_pk_bf16_f32 v231, v206, v207\n"
    "v_mfma_f32_16x16x32_bf16 v[212:215], v[132:135], v[52:55], v[212:215]\n"
    "v_exp_f32 v208, v208\n"
    "v_mfma_f32_16x16x32_bf16 v[212:215], v[128:131], v[48:51], v[212:215]\n"
    "s_waitcnt lgkmcnt(13)\n"
    "ds_read_b64_tr_b16 v[190:191], v253 offset:13160\n"
    "v_add_f32 v240, v240, v200\n"
    "v_mfma_f32_16x16x32_bf16 a[0:3], v[160:163], v[224:227], a[0:3]\n"
    "v_exp_f32 v209, v209\n"
    "v_mfma_f32_16x16x32_bf16 a[32:35], v[160:163], v[228:231], a[32:35]\n"
    "s_waitcnt lgkmcnt(12)\n"
    "v_cvt_pk_bf16_f32 v232, v208, v209\n"
    "v_add_f32 v240, v240, v201\n"
    "v_mfma_f32_16x16x32_bf16 a[36:39], v[164:167], v[228:231], a[36:39]\n"
    "v_exp_f32 v210, v210\n"
    "v_mfma_f32_16x16x32_bf16 a[4:7], v[164:167], v[224:227], a[4:7]\n"
    "s_waitcnt lgkmcnt(10)\n"
    "v_add_f32 v240, v240, v202\n"
    "v_add_f32 v241, v241, v204\n"
    "v_mfma_f32_16x16x32_bf16 a[8:11], v[168:171], v[224:227], a[8:11]\n"
    "v_exp_f32 v211, v211\n"
    "v_mfma_f32_16x16x32_bf16 a[40:43], v[168:171], v[228:231], a[40:43]\n"
    "s_waitcnt lgkmcnt(8)\n"
    "v_cvt_pk_bf16_f32 v233, v210, v211\n"
    "v_add_f32 v240, v240, v203\n"
    "v_mfma_f32_16x16x32_bf16 a[44:47], v[172:175], v[228:231], a[44:47]\n"
    "s_add_u32 m0, s54, 41280\n"
    "v_exp_f32 v212, v212\n"
    "v_mfma_f32_16x16x32_bf16 a[12:15], v[172:175], v[224:227], a[12:15]\n"
    "buffer_load_dwordx4 v254, s[40:43], s53 offen nt lds\n"
    "v_mfma_f32_16x16x32_bf16 v[216:219], v[144:147], v[32:35], v[248:251]\n"
    "v_exp_f32 v213, v213\n"
    "v_mfma_f32_16x16x32_bf16 v[216:219], v[148:151], v[36:39], v[216:219]\n"
    "v_cvt_pk_bf16_f32 v236, v212, v213\n"
    "v_add_f32 v241, v241, v205\n"
    "v_mfma_f32_16x16x32_bf16 v[216:219], v[152:155], v[40:43], v[216:219]\n"
    "v_exp_f32 v214, v214\n"
    "v_mfma_f32_16x16x32_bf16 v[216:219], v[156:159], v[44:47], v[216:219]\n"
    "v_add_f32 v241, v241, v206\n"
    "v_add_f32 v242, v242, v208\n"
    "v_mfma_f32_16x16x32_bf16 v[220:223], v[156:159], v[60:63], v[248:251]\n"
    "v_exp_f32 v215, v215\n"
    "v_mfma_f32_16x16x32_bf16 v[220:223], v[152:155], v[56:59], v[220:223]\n"
    "v_cvt_pk_bf16_f32 v237, v214, v215\n"
    "v_add_f32 v241, v241, v207\n"
    "v_mfma_f32_16x16x32_bf16 v[220:223], v[148:151], v[52:55], v[220:223]\n"
    "v_exp_f32 v216, v216\n"
    "v_mfma_f32_16x16x32_bf16 v[220:223], v[144:147], v[48:51], v[220:223]\n"
    "s_waitcnt lgkmcnt(6)\n"
    "v_add_f32 v242, v242, v209\n"
    "v_add_f32 v243, v243, v212\n"
    "v_mfma_f32_16x16x32_bf16 a[16:19], v[176:179], v[224:227], a[16:19]\n"
    "v_exp_f32 v217, v217\n"
    "v_mfma_f32_16x16x32_bf16 a[48:51], v[176:179], v[228:231], a[48:51]\n"
    "s_waitcnt lgkmcnt(4)\n"
    "v_cvt_pk_bf16_f32 v234, v216, v217\n"
    "v_add_f32 v242, v242, v210\n"
    "v_mfma_f32_16x16x32_bf16 a[52:55], v[180:183], v[228:231], a[52:55]\n"
    "v_exp_f32 v218, v218\n"
    "v_mfma_f32_16x16x32_bf16 a[20:23], v[180:183], v[224:227], a[20:23]\n"
    "s_waitcnt lgkmcnt(2)\n"
    "buffer_load_dwordx4 v254, s[40:43], s53 offen offset:1024 nt lds\n"
    "v_mfma_f32_16x16x32_bf16 a[24:27], v[184:187], v[224:227], a[24:27]\n"
    "v_exp_f32 v219, v219\n"
    "v_mfma_f32_16x16x32_bf16 a[56:59], v[184:187], v[228:231], a[56:59]\n"
    "s_waitcnt lgkmcnt(0)\n"
    "v_cvt_pk_bf16_f32 v235, v218, v219\n"
    "v_add_f32 v242, v242, v211\n"
    "v_mfma_f32_16x16x32_bf16 a[60:63], v[188:191], v[228:231], a[60:63]\n"
    "v_exp_f32 v220, v220\n"
    "v_mfma_f32_16x16x32_bf16 a[28:31], v[188:191], v[224:227], a[28:31]\n"
    "v_add_f32 v243, v243, v213\n"
    "v_add_f32 v242, v242, v216\n"
    "v_mfma_f32_16x16x32_bf16 v[192:195], v[128:131], v[64:67], v[248:251]\n"
    "v_exp_f32 v221, v221\n"
    "v_mfma_f32_16x16x32_bf16 v[192:195], v[132:135], v[68:71], v[192:195]\n"
    "v_cvt_pk_bf16_f32 v238, v220, v221\n"
    "v_add_f32 v243, v243, v214\n"
    "v_mfma_f32_16x16x32_bf16 v[192:195], v[136:139], v[72:75], v[192:195]\n"
    "v_exp_f32 v222, v222\n"
    "v_mfma_f32_16x16x32_bf16 v[192:195], v[140:143], v[76:79], v[192:195]\n"
    "v_add_f32 v243, v243, v215\n"
    "v_add_f32 v242, v242, v217\n"
    "v_mfma_f32_16x16x32_bf16 v[196:199], v[140:143], v[92:95], v[248:251]\n"
    "v_exp_f32 v223, v223\n"
    "v_mfma_f32_16x16x32_bf16 v[196:199], v[136:139], v[88:91], v[196:199]\n"
    "v_cvt_pk_bf16_f32 v239, v222, v223\n"
    "v_add_f32 v242, v242, v218\n"
    "v_mfma_f32_16x16x32_bf16 v[196:199], v[132:135], v[84:87], v[196:199]\n"
    "v_exp_f32 v192, v192\n"
    "v_mfma_f32_16x16x32_bf16 v[196:199], v[128:131], v[80:83], v[196:199]\n"
    "v_add_f32 v242, v242, v219\n"
    "v_add_f32 v243, v243, v220\n"
    "v_mfma_f32_16x16x32_bf16 a[64:67], v[160:163], v[232:235], a[64:67]\n"
    "v_exp_f32 v193, v193\n"
    "v_mfma_f32_16x16x32_bf16 a[96:99], v[160:163], v[236:239], a[96:99]\n"
    "v_cvt_pk_bf16_f32 v224, v192, v193\n"
    "v_add_f32 v243, v243, v221\n"
    "v_mfma_f32_16x16x32_bf16 a[100:103], v[164:167], v[236:239], a[100:103]\n"
    "s_add_u32 m0, s55, 99072\n"
    "v_exp_f32 v194, v194\n"
    "v_mfma_f32_16x16x32_bf16 a[68:71], v[164:167], v[232:235], a[68:71]\n"
    "buffer_load_dwordx4 v254, s[44:47], s53 offen nt lds\n"
    "v_mfma_f32_16x16x32_bf16 a[72:75], v[168:171], v[232:235], a[72:75]\n"
    "v_exp_f32 v195, v195\n"
    "v_mfma_f32_16x16x32_bf16 a[104:107], v[168:171], v[236:239], a[104:107]\n"
    "v_cvt_pk_bf16_f32 v225, v194, v195\n"
    "v_add_f32 v243, v243, v222\n"
    "v_mfma_f32_16x16x32_bf16 a[108:111], v[172:175], v[236:239], a[108:111]\n"
    "v_exp_f32 v196, v196\n"
    "v_mfma_f32_16x16x32_bf16 a[76:79], v[172:175], v[232:235], a[76:79]\n"
    "v_add_f32 v243, v243, v223\n"
    "v_add_f32 v244, v244, v192\n"
    "v_mfma_f32_16x16x32_bf16 v[200:203], v[144:147], v[64:67], v[248:251]\n"
    "v_exp_f32 v197, v197\n"
    "v_mfma_f32_16x16x32_bf16 v[200:203], v[148:151], v[68:71], v[200:203]\n"
    "v_cvt_pk_bf16_f32 v228, v196, v197\n"
    "v_add_f32 v244, v244, v193\n"
    "v_mfma_f32_16x16x32_bf16 v[200:203], v[152:155], v[72:75], v[200:203]\n"
    "v_exp_f32 v198, v198\n"
    "v_mfma_f32_16x16x32_bf16 v[200:203], v[156:159], v[76:79], v[200:203]\n"
    "v_add_f32 v244, v244, v194\n"
    "v_add_f32 v245, v245, v196\n"
    "v_mfma_f32_16x16x32_bf16 v[204:207], v[156:159], v[92:95], v[248:251]\n"
    "v_exp_f32 v199, v199\n"
    "v_mfma_f32_16x16x32_bf16 v[204:207], v[152:155], v[88:91], v[204:207]\n"
    "v_cvt_pk_bf16_f32 v229, v198, v199\n"
    "v_add_f32 v244, v244, v195\n"
    "v_mfma_f32_16x16x32_bf16 v[204:207], v[148:151], v[84:87], v[204:207]\n"
    "v_exp_f32 v200, v200\n"
    "v_mfma_f32_16x16x32_bf16 v[204:207], v[144:147], v[80:83], v[204:207]\n"
    "v_add_f32 v245, v245, v197\n"
    "v_add_f32 v244, v244, v200\n"
    "v_mfma_f32_16x16x32_bf16 a[80:83], v[176:179], v[232:235], a[80:83]\n"
    "v_exp_f32 v201, v201\n"
    "v_mfma_f32_16x16x32_bf16 a[112:115], v[176:179], v[236:239], a[112:115]\n"
    "v_cvt_pk_bf16_f32 v226, v200, v201\n"
    "v_add_f32 v245, v245, v198\n"
    "v_mfma_f32_16x16x32_bf16 a[116:119], v[180:183], v[236:239], a[116:119]\n"
    "s_add_u32 m0, s55, 99080\n"
    "v_exp_f32 v202, v202\n"
    "v_mfma_f32_16x16x32_bf16 a[84:87], v[180:183], v[232:235], a[84:87]\n"
    "buffer_load_dwordx4 v254, s[44:47], s53 offen offset:1024 nt lds\n"
    "v_mfma_f32_16x16x32_bf16 a[88:91], v[184:187], v[232:235], a[88:91]\n"
    "s_add_u32 s53, s53, 8192\n"
    "v_exp_f32 v203, v203\n"
    "v_mfma_f32_16x16x32_bf16 a[120:123], v[184:187], v[236:239], a[120:123]\n"
    "v_cvt_pk_bf16_f32 v227, v202, v203\n"
    "v_add_f32 v245, v245, v199\n"
    "v_mfma_f32_16x16x32_bf16 a[124:127], v[188:191], v[236:239], a[124:127]\n"
    "v_exp_f32 v204, v204\n"
    "v_mfma_f32_16x16x32_bf16 a[92:95], v[188:191], v[232:235], a[92:95]\n"
    "v_add_f32 v244, v244, v201\n"
    "v_add_f32 v245, v245, v204\n"
    "v_mfma_f32_16x16x32_bf16 v[208:211], v[128:131], v[96:99], v[248:251]\n"
    "v_exp_f32 v205, v205\n"
    "v_mfma_f32_16x16x32_bf16 v[208:211], v[132:135], v[100:103], v[208:211]\n"
    "v_cvt_pk_bf16_f32 v230, v204, v205\n"
    "v_add_f32 v244, v244, v202\n"
    "v_mfma_f32_16x16x32_bf16 v[208:211], v[136:139], v[104:107], v[208:211]\n"
    "v_exp_f32 v206, v206\n"
    "v_mfma_f32_16x16x32_bf16 v[208:211], v[140:143], v[108:111], v[208:211]\n"
    "v_add_f32 v244, v244, v203\n"
    "v_add_f32 v245, v245, v205\n"
    "v_mfma_f32_16x16x32_bf16 v[212:215], v[140:143], v[124:127], v[248:251]\n"
    "v_exp_f32 v207, v207\n"
    "v_mfma_f32_16x16x32_bf16 v[212:215], v[136:139], v[120:123], v[212:215]\n"
    "v_cvt_pk_bf16_f32 v231, v206, v207\n"
    "v_add_f32 v245, v245, v206\n"
    "v_mfma_f32_16x16x32_bf16 v[212:215], v[132:135], v[116:119], v[212:215]\n"
    "v_exp_f32 v208, v208\n"
    "v_mfma_f32_16x16x32_bf16 v[212:215], v[128:131], v[112:115], v[212:215]\n"
    "ds_read_b128 v[140:143], v252 offset:17280\n"
    "v_add_f32 v245, v245, v207\n"
    "v_mfma_f32_16x16x32_bf16 a[128:131], v[160:163], v[224:227], a[128:131]\n"
    "v_exp_f32 v209, v209\n"
    "v_mfma_f32_16x16x32_bf16 a[160:163], v[160:163], v[228:231], a[160:163]\n"
    "ds_read_b128 v[136:139], v252 offset:17024\n"
    "v_cvt_pk_bf16_f32 v232, v208, v209\n"
    "v_mfma_f32_16x16x32_bf16 a[164:167], v[164:167], v[228:231], a[164:167]\n"
    "v_exp_f32 v210, v210\n"
    "v_mfma_f32_16x16x32_bf16 a[132:135], v[164:167], v[224:227], a[132:135]\n"
    "ds_read_b128 v[132:135], v252 offset:16768\n"
    "v_add_f32 v246, v246, v208\n"
    "v_mfma_f32_16x16x32_bf16 a[136:139], v[168:171], v[224:227], a[136:139]\n"
    "v_exp_f32 v211, v211\n"
    "v_mfma_f32_16x16x32_bf16 a[168:171], v[168:171], v[228:231], a[168:171]\n"
    "ds_read_b128 v[128:131], v252 offset:16512\n"
    "v_cvt_pk_bf16_f32 v233, v210, v211\n"
    "v_mfma_f32_16x16x32_bf16 a[172:175], v[172:175], v[228:231], a[172:175]\n"
    "v_exp_f32 v212, v212\n"
    "v_mfma_f32_16x16x32_bf16 a[140:143], v[172:175], v[224:227], a[140:143]\n"
    "v_add_f32 v246, v246, v209\n"
    "v_add_f32 v247, v247, v212\n"
    "v_mfma_f32_16x16x32_bf16 v[216:219], v[144:147], v[96:99], v[248:251]\n"
    "v_exp_f32 v213, v213\n"
    "v_mfma_f32_16x16x32_bf16 v[216:219], v[148:151], v[100:103], v[216:219]\n"
    "v_cvt_pk_bf16_f32 v236, v212, v213\n"
    "v_add_f32 v246, v246, v210\n"
    "v_mfma_f32_16x16x32_bf16 v[216:219], v[152:155], v[104:107], v[216:219]\n"
    "v_exp_f32 v214, v214\n"
    "v_mfma_f32_16x16x32_bf16 v[216:219], v[156:159], v[108:111], v[216:219]\n"
    "v_add_f32 v246, v246, v211\n"
    "v_add_f32 v247, v247, v213\n"
    "v_mfma_f32_16x16x32_bf16 v[220:223], v[156:159], v[124:127], v[248:251]\n"
    "v_exp_f32 v215, v215\n"
    "v_mfma_f32_16x16x32_bf16 v[220:223], v[152:155], v[120:123], v[220:223]\n"
    "v_cvt_pk_bf16_f32 v237, v214, v215\n"
    "v_add_f32 v247, v247, v214\n"
    "v_mfma_f32_16x16x32_bf16 v[220:223], v[148:151], v[116:119], v[220:223]\n"
    "v_exp_f32 v216, v216\n"
    "v_mfma_f32_16x16x32_bf16 v[220:223], v[144:147], v[112:115], v[220:223]\n"
    "ds_read_b128 v[156:159], v252 offset:21408\n"
    "v_add_f32 v247, v247, v215\n"
    "v_mfma_f32_16x16x32_bf16 a[144:147], v[176:179], v[224:227], a[144:147]\n"
    "v_exp_f32 v217, v217\n"
    "v_mfma_f32_16x16x32_bf16 a[176:179], v[176:179], v[228:231], a[176:179]\n"
    "ds_read_b128 v[152:155], v252 offset:21152\n"
    "v_cvt_pk_bf16_f32 v234, v216, v217\n"
    "v_mfma_f32_16x16x32_bf16 a[180:183], v[180:183], v[228:231], a[180:183]\n"
    "v_exp_f32 v218, v218\n"
    "v_mfma_f32_16x16x32_bf16 a[148:151], v[180:183], v[224:227], a[148:151]\n"
    "ds_read_b128 v[148:151], v252 offset:20896\n"
    "v_add_f32 v246, v246, v216\n"
    "v_mfma_f32_16x16x32_bf16 a[152:155], v[184:187], v[224:227], a[152:155]\n"
    "v_exp_f32 v219, v219\n"
    "v_mfma_f32_16x16x32_bf16 a[184:187], v[184:187], v[228:231], a[184:187]\n"
    "ds_read_b128 v[144:147], v252 offset:20640\n"
    "v_cvt_pk_bf16_f32 v235, v218, v219\n"
    "v_mfma_f32_16x16x32_bf16 a[188:191], v[188:191], v[228:231], a[188:191]\n"
    "v_exp_f32 v220, v220\n"
    "v_mfma_f32_16x16x32_bf16 a[156:159], v[188:191], v[224:227], a[156:159]\n"
    "s_waitcnt lgkmcnt(4)\n"
    "v_add_f32 v246, v246, v217\n"
    "v_add_f32 v247, v247, v220\n"
    "v_mfma_f32_16x16x32_bf16 v[192:195], v[128:131], v[0:3], v[248:251]\n"
    "v_exp_f32 v221, v221\n"
    "v_mfma_f32_16x16x32_bf16 v[192:195], v[132:135], v[4:7], v[192:195]\n"
    "v_cvt_pk_bf16_f32 v238, v220, v221\n"
    "v_add_f32 v246, v246, v218\n"
    "v_mfma_f32_16x16x32_bf16 v[192:195], v[136:139], v[8:11], v[192:195]\n"
    "v_exp_f32 v222, v222\n"
    "v_mfma_f32_16x16x32_bf16 v[192:195], v[140:143], v[12:15], v[192:195]\n"
    "v_add_f32 v246, v246, v219\n"
    "v_add_f32 v247, v247, v221\n"
    "v_mfma_f32_16x16x32_bf16 v[196:199], v[140:143], v[28:31], v[248:251]\n"
    "v_exp_f32 v223, v223\n"
    "v_mfma_f32_16x16x32_bf16 v[196:199], v[136:139], v[24:27], v[196:199]\n"
    "v_cvt_pk_bf16_f32 v239, v222, v223\n"
    "v_add_f32 v247, v247, v222\n"
    "v_mfma_f32_16x16x32_bf16 v[196:199], v[132:135], v[20:23], v[196:199]\n"
    "v_exp_f32 v192, v192\n"
    "v_mfma_f32_16x16x32_bf16 v[196:199], v[128:131], v[16:19], v[196:199]\n"
    "v_add_f32 v247, v247, v223\n"
    "v_add_f32 v240, v240, v192\n"
    "v_mfma_f32_16x16x32_bf16 a[192:195], v[160:163], v[232:235], a[192:195]\n"
    "v_exp_f32 v193, v193\n"
    "v_mfma_f32_16x16x32_bf16 a[224:227], v[160:163], v[236:239], a[224:227]\n"
    "ds_read_b64_tr_b16 v[160:161], v253 offset:16512\n"
    "v_cvt_pk_bf16_f32 v224, v192, v193\n"
    "v_mfma_f32_16x16x32_bf16 a[228:231], v[164:167], v[236:239], a[228:231]\n"
    "v_exp_f32 v194, v194\n"
    "v_mfma_f32_16x16x32_bf16 a[196:199], v[164:167], v[232:235], a[196:199]\n"
    "ds_read_b64_tr_b16 v[162:163], v253 offset:20640\n"
    "v_add_f32 v240, v240, v193\n"
    "v_mfma_f32_16x16x32_bf16 a[200:203], v[168:171], v[232:235], a[200:203]\n"
    "v_exp_f32 v195, v195\n"
    "v_mfma_f32_16x16x32_bf16 a[232:235], v[168:171], v[236:239], a[232:235]\n"
    "ds_read_b64_tr_b16 v[164:165], v253 offset:16520\n"
    "v_cvt_pk_bf16_f32 v225, v194, v195\n"
    "v_mfma_f32_16x16x32_bf16 a[236:239], v[172:175], v[236:239], a[236:239]\n"
    "v_exp_f32 v196, v196\n"
    "v_mfma_f32_16x16x32_bf16 a[204:207], v[172:175], v[232:235], a[204:207]\n"
    "s_waitcnt lgkmcnt(3)\n"
    "ds_read_b64_tr_b16 v[166:167], v253 offset:20648\n"
    "v_add_f32 v240, v240, v194\n"
    "v_mfma_f32_16x16x32_bf16 v[200:203], v[144:147], v[0:3], v[248:251]\n"
    "v_exp_f32 v197, v197\n"
    "v_mfma_f32_16x16x32_bf16 v[200:203], v[148:151], v[4:7], v[200:203]\n"
    "ds_read_b64_tr_b16 v[168:169], v253 offset:16768\n"
    "v_cvt_pk_bf16_f32 v228, v196, v197\n"
    "v_mfma_f32_16x16x32_bf16 v[200:203], v[152:155], v[8:11], v[200:203]\n"
    "v_exp_f32 v198, v198\n"
    "v_mfma_f32_16x16x32_bf16 v[200:203], v[156:159], v[12:15], v[200:203]\n"
    "ds_read_b64_tr_b16 v[170:171], v253 offset:20896\n"
    "v_add_f32 v240, v240, v195\n"
    "v_mfma_f32_16x16x32_bf16 v[204:207], v[156:159], v[28:31], v[248:251]\n"
    "v_exp_f32 v199, v199\n"
    "v_mfma_f32_16x16x32_bf16 v[204:207], v[152:155], v[24:27], v[204:207]\n"
    "ds_read_b64_tr_b16 v[172:173], v253 offset:16776\n"
    "v_cvt_pk_bf16_f32 v229, v198, v199\n"
    "v_mfma_f32_16x16x32_bf16 v[204:207], v[148:151], v[20:23], v[204:207]\n"
    "v_exp_f32 v200, v200\n"
    "v_mfma_f32_16x16x32_bf16 v[204:207], v[144:147], v[16:19], v[204:207]\n"
    "ds_read_b64_tr_b16 v[174:175], v253 offset:20904\n"
    "v_add_f32 v241, v241, v196\n"
    "v_mfma_f32_16x16x32_bf16 a[208:211], v[176:179], v[232:235], a[208:211]\n"
    "v_exp_f32 v201, v201\n"
    "v_mfma_f32_16x16x32_bf16 a[240:243], v[176:179], v[236:239], a[240:243]\n"
    "ds_read_b64_tr_b16 v[176:177], v253 offset:17024\n"
    "v_cvt_pk_bf16_f32 v226, v200, v201\n"
    "v_mfma_f32_16x16x32_bf16 a[244:247], v[180:183], v[236:239], a[244:247]\n"
    "v_exp_f32 v202, v202\n"
    "v_mfma_f32_16x16x32_bf16 a[212:215], v[180:183], v[232:235], a[212:215]\n"
    "ds_read_b64_tr_b16 v[178:179], v253 offset:21152\n"
    "v_add_f32 v241, v241, v197\n"
    "v_mfma_f32_16x16x32_bf16 a[216:219], v[184:187], v[232:235], a[216:219]\n"
    "v_exp_f32 v203, v203\n"
    "v_mfma_f32_16x16x32_bf16 a[248:251], v[184:187], v[236:239], a[248:251]\n"
    "ds_read_b64_tr_b16 v[180:181], v253 offset:17032\n"
    "v_cvt_pk_bf16_f32 v227, v202, v203\n"
    "v_mfma_f32_16x16x32_bf16 a[252:255], v[188:191], v[236:239], a[252:255]\n"
    "v_exp_f32 v204, v204\n"
    "v_mfma_f32_16x16x32_bf16 a[220:223], v[188:191], v[232:235], a[220:223]\n"
    "ds_read_b64_tr_b16 v[182:183], v253 offset:21160\n"
    "v_add_f32 v241, v241, v198\n"
    "v_mfma_f32_16x16x32_bf16 v[208:211], v[128:131], v[32:35], v[248:251]\n"
    "v_exp_f32 v205, v205\n"
    "v_mfma_f32_16x16x32_bf16 v[208:211], v[132:135], v[36:39], v[208:211]\n"
    "ds_read_b64_tr_b16 v[184:185], v253 offset:17280\n"
    "v_cvt_pk_bf16_f32 v230, v204, v205\n"
    "v_mfma_f32_16x16x32_bf16 v[208:211], v[136:139], v[40:43], v[208:211]\n"
    "v_exp_f32 v206, v206\n"
    "v_mfma_f32_16x16x32_bf16 v[208:211], v[140:143], v[44:47], v[208:211]\n"
    "ds_read_b64_tr_b16 v[186:187], v253 offset:21408\n"
    "v_add_f32 v241, v241, v199\n"
    "v_mfma_f32_16x16x32_bf16 v[212:215], v[140:143], v[60:63], v[248:251]\n"
    "v_exp_f32 v207, v207\n"
    "v_mfma_f32_16x16x32_bf16 v[212:215], v[136:139], v[56:59], v[212:215]\n"
    "ds_read_b64_tr_b16 v[188:189], v253 offset:17288\n"
    "v_cvt_pk_bf16_f32 v231, v206, v207\n"
    "v_mfma_f32_16x16x32_bf16 v[212:215], v[132:135], v[52:55], v[212:215]\n"
    "v_exp_f32 v208, v208\n"
    "v_mfma_f32_16x16x32_bf16 v[212:215], v[128:131], v[48:51], v[212:215]\n"
    "s_waitcnt lgkmcnt(13)\n"
    "ds_read_b64_tr_b16 v[190:191], v253 offset:21416\n"
    "v_add_f32 v240, v240, v200\n"
    "v_mfma_f32_16x16x32_bf16 a[0:3], v[160:163], v[224:227], a[0:3]\n"
    "v_exp_f32 v209, v209\n"
    "v_mfma_f32_16x16x32_bf16 a[32:35], v[160:163], v[228:231], a[32:35]\n"
    "s_waitcnt lgkmcnt(12)\n"
    "v_cvt_pk_bf16_f32 v232, v208, v209\n"
    "v_add_f32 v240, v240, v201\n"
    "v_mfma_f32_16x16x32_bf16 a[36:39], v[164:167], v[228:231], a[36:39]\n"
    "v_exp_f32 v210, v210\n"
    "v_mfma_f32_16x16x32_bf16 a[4:7], v[164:167], v[224:227], a[4:7]\n"
    "s_waitcnt lgkmcnt(10)\n"
    "v_add_f32 v240, v240, v202\n"
    "v_add_f32 v241, v241, v204\n"
    "v_mfma_f32_16x16x32_bf16 a[8:11], v[168:171], v[224:227], a[8:11]\n"
    "v_exp_f32 v211, v211\n"
    "v_mfma_f32_16x16x32_bf16 a[40:43], v[168:171], v[228:231], a[40:43]\n"
    "s_waitcnt lgkmcnt(8)\n"
    "v_cvt_pk_bf16_f32 v233, v210, v211\n"
    "v_add_f32 v240, v240, v203\n"
    "v_mfma_f32_16x16x32_bf16 a[44:47], v[172:175], v[228:231], a[44:47]\n"
    "s_add_u32 m0, s54, 0\n"
    "v_exp_f32 v212, v212\n"
    "v_mfma_f32_16x16x32_bf16 a[12:15], v[172:175], v[224:227], a[12:15]\n"
    "buffer_load_dwordx4 v254, s[40:43], s53 offen nt lds\n"
    "v_mfma_f32_16x16x32_bf16 v[216:219], v[144:147], v[32:35], v[248:251]\n"
    "v_exp_f32 v213, v213\n"
    "v_mfma_f32_16x16x32_bf16 v[216:219], v[148:151], v[36:39], v[216:219]\n"
    "v_cvt_pk_bf16_f32 v236, v212, v213\n"
    "v_add_f32 v241, v241, v205\n"
    "v_mfma_f32_16x16x32_bf16 v[216:219], v[152:155], v[40:43], v[216:219]\n"
    "v_exp_f32 v214, v214\n"
    "v_mfma_f32_16x16x32_bf16 v[216:219], v[156:159], v[44:47], v[216:219]\n"
    "v_add_f32 v241, v241, v206\n"
    "v_add_f32 v242, v242, v208\n"
    "v_mfma_f32_16x16x32_bf16 v[220:223], v[156:159], v[60:63], v[248:251]\n"
    "v_exp_f32 v215, v215\n"
    "v_mfma_f32_16x16x32_bf16 v[220:223], v[152:155], v[56:59], v[220:223]\n"
    "v_cvt_pk_bf16_f32 v237, v214, v215\n"
    "v_add_f32 v241, v241, v207\n"
    "v_mfma_f32_16x16x32_bf16 v[220:223], v[148:151], v[52:55], v[220:223]\n"
    "v_exp_f32 v216, v216\n"
    "v_mfma_f32_16x16x32_bf16 v[220:223], v[144:147], v[48:51], v[220:223]\n"
    "s_waitcnt lgkmcnt(6)\n"
    "v_add_f32 v242, v242, v209\n"
    "v_add_f32 v243, v243, v212\n"
    "v_mfma_f32_16x16x32_bf16 a[16:19], v[176:179], v[224:227], a[16:19]\n"
    "v_exp_f32 v217, v217\n"
    "v_mfma_f32_16x16x32_bf16 a[48:51], v[176:179], v[228:231], a[48:51]\n"
    "s_waitcnt lgkmcnt(4)\n"
    "v_cvt_pk_bf16_f32 v234, v216, v217\n"
    "v_add_f32 v242, v242, v210\n"
    "v_mfma_f32_16x16x32_bf16 a[52:55], v[180:183], v[228:231], a[52:55]\n"
    "v_exp_f32 v218, v218\n"
    "v_mfma_f32_16x16x32_bf16 a[20:23], v[180:183], v[224:227], a[20:23]\n"
    "s_waitcnt lgkmcnt(2)\n"
    "buffer_load_dwordx4 v254, s[40:43], s53 offen offset:1024 nt lds\n"
    "v_mfma_f32_16x16x32_bf16 a[24:27], v[184:187], v[224:227], a[24:27]\n"
    "v_exp_f32 v219, v219\n"
    "v_mfma_f32_16x16x32_bf16 a[56:59], v[184:187], v[228:231], a[56:59]\n"
    "s_waitcnt lgkmcnt(0)\n"
    "v_cvt_pk_bf16_f32 v235, v218, v219\n"
    "v_add_f32 v242, v242, v211\n"
    "v_mfma_f32_16x16x32_bf16 a[60:63], v[188:191], v[228:231], a[60:63]\n"
    "v_exp_f32 v220, v220\n"
    "v_mfma_f32_16x16x32_bf16 a[28:31], v[188:191], v[224:227], a[28:31]\n"
    "v_add_f32 v243, v243, v213\n"
    "v_add_f32 v242, v242, v216\n"
    "v_mfma_f32_16x16x32_bf16 v[192:195], v[128:131], v[64:67], v[248:251]\n"
    "v_exp_f32 v221, v221\n"
    "v_mfma_f32_16x16x32_bf16 v[192:195], v[132:135], v[68:71], v[192:195]\n"
    "v_cvt_pk_bf16_f32 v238, v220, v221\n"
    "v_add_f32 v243, v243, v214\n"
    "v_mfma_f32_16x16x32_bf16 v[192:195], v[136:139], v[72:75], v[192:195]\n"
    "v_exp_f32 v222, v222\n"
    "v_mfma_f32_16x16x32_bf16 v[192:195], v[140:143], v[76:79], v[192:195]\n"
    "v_add_f32 v243, v243, v215\n"
    "v_add_f32 v242, v242, v217\n"
    "v_mfma_f32_16x16x32_bf16 v[196:199], v[140:143], v[92:95], v[248:251]\n"
    "v_exp_f32 v223, v223\n"
    "v_mfma_f32_16x16x32_bf16 v[196:199], v[136:139], v[88:91], v[196:199]\n"
    "v_cvt_pk_bf16_f32 v239, v222, v223\n"
    "v_add_f32 v242, v242, v218\n"
    "v_mfma_f32_16x16x32_bf16 v[196:199], v[132:135], v[84:87], v[196:199]\n"
    "v_exp_f32 v192, v192\n"
    "v_mfma_f32_16x16x32_bf16 v[196:199], v[128:131], v[80:83], v[196:199]\n"
    "v_add_f32 v242, v242, v219\n"
    "v_add_f32 v243, v243, v220\n"
    "v_mfma_f32_16x16x32_bf16 a[64:67], v[160:163], v[232:235], a[64:67]\n"
    "v_exp_f32 v193, v193\n"
    "v_mfma_f32_16x16x32_bf16 a[96:99], v[160:163], v[236:239], a[96:99]\n"
    "v_cvt_pk_bf16_f32 v224, v192, v193\n"
    "v_add_f32 v243, v243, v221\n"
    "v_mfma_f32_16x16x32_bf16 a[100:103], v[164:167], v[236:239], a[100:103]\n"
    "s_add_u32 m0, s55, 57792\n"
    "v_exp_f32 v194, v194\n"
    "v_mfma_f32_16x16x32_bf16 a[68:71], v[164:167], v[232:235], a[68:71]\n"
    "buffer_load_dwordx4 v254, s[44:47], s53 offen nt lds\n"
    "v_mfma_f32_16x16x32_bf16 a[72:75], v[168:171], v[232:235], a[72:75]\n"
    "v_exp_f32 v195, v195\n"
    "v_mfma_f32_16x16x32_bf16 a[104:107], v[168:171], v[236:239], a[104:107]\n"
    "v_cvt_pk_bf16_f32 v225, v194, v195\n"
    "v_add_f32 v243, v243, v222\n"
    "v_mfma_f32_16x16x32_bf16 a[108:111], v[172:175], v[236:239], a[108:111]\n"
    "v_exp_f32 v196, v196\n"
    "v_mfma_f32_16x16x32_bf16 a[76:79], v[172:175], v[232:235], a[76:79]\n"
    "v_add_f32 v243, v243, v223\n"
    "v_add_f32 v244, v244, v192\n"
    "v_mfma_f32_16x16x32_bf16 v[200:203], v[144:147], v[64:67], v[248:251]\n"
    "v_exp_f32 v197, v197\n"
    "v_mfma_f32_16x16x32_bf16 v[200:203], v[148:151], v[68:71], v[200:203]\n"
    "v_cvt_pk_bf16_f32 v228, v196, v197\n"
    "v_add_f32 v244, v244, v193\n"
    "v_mfma_f32_16x16x32_bf16 v[200:203], v[152:155], v[72:75], v[200:203]\n"
    "v_exp_f32 v198, v198\n"
    "v_mfma_f32_16x16x32_bf16 v[200:203], v[156:159], v[76:79], v[200:203]\n"
    "v_add_f32 v244, v244, v194\n"
    "v_add_f32 v245, v245, v196\n"
    "v_mfma_f32_16x16x32_bf16 v[204:207], v[156:159], v[92:95], v[248:251]\n"
    "v_exp_f32 v199, v199\n"
    "v_mfma_f32_16x16x32_bf16 v[204:207], v[152:155], v[88:91], v[204:207]\n"
    "v_cvt_pk_bf16_f32 v229, v198, v199\n"
    "v_add_f32 v244, v244, v195\n"
    "v_mfma_f32_16x16x32_bf16 v[204:207], v[148:151], v[84:87], v[204:207]\n"
    "v_exp_f32 v200, v200\n"
    "v_mfma_f32_16x16x32_bf16 v[204:207], v[144:147], v[80:83], v[204:207]\n"
    "v_add_f32 v245, v245, v197\n"
    "v_add_f32 v244, v244, v200\n"
    "v_mfma_f32_16x16x32_bf16 a[80:83], v[176:179], v[232:235], a[80:83]\n"
    "v_exp_f32 v201, v201\n"
    "v_mfma_f32_16x16x32_bf16 a[112:115], v[176:179], v[236:239], a[112:115]\n"
    "v_cvt_pk_bf16_f32 v226, v200, v201\n"
    "v_add_f32 v245, v245, v198\n"
    "v_mfma_f32_16x16x32_bf16 a[116:119], v[180:183], v[236:239], a[116:119]\n"
    "s_add_u32 m0, s55, 57800\n"
    "v_exp_f32 v202, v202\n"
    "v_mfma_f32_16x16x32_bf16 a[84:87], v[180:183], v[232:235], a[84:87]\n"
    "buffer_load_dwordx4 v254, s[44:47], s53 offen offset:1024 nt lds\n"
    "v_mfma_f32_16x16x32_bf16 a[88:91], v[184:187], v[232:235], a[88:91]\n"
    "s_add_u32 s53, s53, 8192\n"
    "v_exp_f32 v203, v203\n"
    "v_mfma_f32_16x16x32_bf16 a[120:123], v[184:187], v[236:239], a[120:123]\n"
    "v_cvt_pk_bf16_f32 v227, v202, v203\n"
    "v_add_f32 v245, v245, v199\n"
    "v_mfma_f32_16x16x32_bf16 a[124:127], v[188:191], v[236:239], a[124:127]\n"
    "v_exp_f32 v204, v204\n"
    "v_mfma_f32_16x16x32_bf16 a[92:95], v[188:191], v[232:235], a[92:95]\n"
    "v_add_f32 v244, v244, v201\n"
    "v_add_f32 v245, v245, v204\n"
    "v_mfma_f32_16x16x32_bf16 v[208:211], v[128:131], v[96:99], v[248:251]\n"
    "v_exp_f32 v205, v205\n"
    "v_mfma_f32_16x16x32_bf16 v[208:211], v[132:135], v[100:103], v[208:211]\n"
    "v_cvt_pk_bf16_f32 v230, v204, v205\n"
    "v_add_f32 v244, v244, v202\n"
    "v_mfma_f32_16x16x32_bf16 v[208:211], v[136:139], v[104:107], v[208:211]\n"
    "s_waitcnt vmcnt(8)\n"
    "v_exp_f32 v206, v206\n"
    "v_mfma_f32_16x16x32_bf16 v[208:211], v[140:143], v[108:111], v[208:211]\n"
    "s_barrier\n"
    "v_add_f32 v244, v244, v203\n"
    "v_add_f32 v245, v245, v205\n"
    "v_mfma_f32_16x16x32_bf16 v[212:215], v[140:143], v[124:127], v[248:251]\n"
    "v_exp_f32 v207, v207\n"
    "v_mfma_f32_16x16x32_bf16 v[212:215], v[136:139], v[120:123], v[212:215]\n"
    "v_cvt_pk_bf16_f32 v231, v206, v207\n"
    "v_add_f32 v245, v245, v206\n"
    "v_mfma_f32_16x16x32_bf16 v[212:215], v[132:135], v[116:119], v[212:215]\n"
    "v_exp_f32 v208, v208\n"
    "v_mfma_f32_16x16x32_bf16 v[212:215], v[128:131], v[112:115], v[212:215]\n"
    "ds_read_b128 v[140:143], v252 offset:25536\n"
    "v_add_f32 v245, v245, v207\n"
    "v_mfma_f32_16x16x32_bf16 a[128:131], v[160:163], v[224:227], a[128:131]\n"
    "v_exp_f32 v209, v209\n"
    "v_mfma_f32_16x16x32_bf16 a[160:163], v[160:163], v[228:231], a[160:163]\n"
    "ds_read_b128 v[136:139], v252 offset:25280\n"
    "v_cvt_pk_bf16_f32 v232, v208, v209\n"
    "v_mfma_f32_16x16x32_bf16 a[164:167], v[164:167], v[228:231], a[164:167]\n"
    "v_exp_f32 v210, v210\n"
    "v_mfma_f32_16x16x32_bf16 a[132:135], v[164:167], v[224:227], a[132:135]\n"
    "ds_read_b128 v[132:135], v252 offset:25024\n"
    "v_add_f32 v246, v246, v208\n"
    "v_mfma_f32_16x16x32_bf16 a[136:139], v[168:171], v[224:227], a[136:139]\n"
    "v_exp_f32 v211, v211\n"
    "v_mfma_f32_16x16x32_bf16 a[168:171], v[168:171], v[228:231], a[168:171]\n"
    "ds_read_b128 v[128:131], v252 offset:24768\n"
    "v_cvt_pk_bf16_f32 v233, v210, v211\n"
    "v_mfma_f32_16x16x32_bf16 a[172:175], v[172:175], v[228:231], a[172:175]\n"
    "v_exp_f32 v212, v212\n"
    "v_mfma_f32_16x16x32_bf16 a[140:143], v[172:175], v[224:227], a[140:143]\n"
    "v_add_f32 v246, v246, v209\n"
    "v_add_f32 v247, v247, v212\n"
    "v_mfma_f32_16x16x32_bf16 v[216:219], v[144:147], v[96:99], v[248:251]\n"
    "v_exp_f32 v213, v213\n"
    "v_mfma_f32_16x16x32_bf16 v[216:219], v[148:151], v[100:103], v[216:219]\n"
    "v_cvt_pk_bf16_f32 v236, v212, v213\n"
    "v_add_f32 v246, v246, v210\n"
    "v_mfma_f32_16x16x32_bf16 v[216:219], v[152:155], v[104:107], v[216:219]\n"
    "v_exp_f32 v214, v214\n"
    "v_mfma_f32_16x16x32_bf16 v[216:219], v[156:159], v[108:111], v[216:219]\n"
    "v_add_f32 v246, v246, v211\n"
    "v_add_f32 v247, v247, v213\n"
    "v_mfma_f32_16x16x32_bf16 v[220:223], v[156:159], v[124:127], v[248:251]\n"
    "v_exp_f32 v215, v215\n"
    "v_mfma_f32_16x16x32_bf16 v[220:223], v[152:155], v[120:123], v[220:223]\n"
    "v_cvt_pk_bf16_f32 v237, v214, v215\n"
    "v_add_f32 v247, v247, v214\n"
    "v_mfma_f32_16x16x32_bf16 v[220:223], v[148:151], v[116:119], v[220:223]\n"
    "v_exp_f32 v216, v216\n"
    "v_mfma_f32_16x16x32_bf16 v[220:223], v[144:147], v[112:115], v[220:223]\n"
    "ds_read_b128 v[156:159], v252 offset:29664\n"
    "v_add_f32 v247, v247, v215\n"
    "v_mfma_f32_16x16x32_bf16 a[144:147], v[176:179], v[224:227], a[144:147]\n"
    "v_exp_f32 v217, v217\n"
    "v_mfma_f32_16x16x32_bf16 a[176:179], v[176:179], v[228:231], a[176:179]\n"
    "ds_read_b128 v[152:155], v252 offset:29408\n"
    "v_cvt_pk_bf16_f32 v234, v216, v217\n"
    "v_mfma_f32_16x16x32_bf16 a[180:183], v[180:183], v[228:231], a[180:183]\n"
    "v_exp_f32 v218, v218\n"
    "v_mfma_f32_16x16x32_bf16 a[148:151], v[180:183], v[224:227], a[148:151]\n"
    "ds_read_b128 v[148:151], v252 offset:29152\n"
    "v_add_f32 v246, v246, v216\n"
    "v_mfma_f32_16x16x32_bf16 a[152:155], v[184:187], v[224:227], a[152:155]\n"
    "v_exp_f32 v219, v219\n"
    "v_mfma_f32_16x16x32_bf16 a[184:187], v[184:187], v[228:231], a[184:187]\n"
    "ds_read_b128 v[144:147], v252 offset:28896\n"
    "v_cvt_pk_bf16_f32 v235, v218, v219\n"
    "v_mfma_f32_16x16x32_bf16 a[188:191], v[188:191], v[228:231], a[188:191]\n"
    "v_exp_f32 v220, v220\n"
    "v_mfma_f32_16x16x32_bf16 a[156:159], v[188:191], v[224:227], a[156:159]\n"
    "s_branch L_loop_%=\n"
    "L_exit_%=:\n"
    "s_waitcnt lgkmcnt(4)\n"
    "v_add_f32 v246, v246, v217\n"
    "v_add_f32 v247, v247, v220\n"
    "v_mfma_f32_16x16x32_bf16 v[192:195], v[128:131], v[32:35], v[248:251]\n"
    "v_exp_f32 v221, v221\n"
    "v_mfma_f32_16x16x32_bf16 v[192:195], v[132:135], v[36:39], v[192:195]\n"
    "v_cvt_pk_bf16_f32 v238, v220, v221\n"
    "v_add_f32 v246, v246, v218\n"
    "v_mfma_f32_16x16x32_bf16 v[192:195], v[136:139], v[40:43], v[192:195]\n"
    "v_exp_f32 v222, v222\n"
    "v_mfma_f32_16x16x32_bf16 v[192:195], v[140:143], v[44:47], v[192:195]\n"
    "v_add_f32 v246, v246, v219\n"
    "v_add_f32 v247, v247, v221\n"
    "v_mfma_f32_16x16x32_bf16 v[196:199], v[140:143], v[60:63], v[248:251]\n"
    "v_exp_f32 v223, v223\n"
    "v_mfma_f32_16x16x32_bf16 v[196:199], v[136:139], v[56:59], v[196:199]\n"
    "v_cvt_pk_bf16_f32 v239, v222, v223\n"
    "v_add_f32 v247, v247, v222\n"
    "v_mfma_f32_16x16x32_bf16 v[196:199], v[132:135], v[52:55], v[196:199]\n"
    "v_exp_f32 v192, v192\n"
    "v_mfma_f32_16x16x32_bf16 v[196:199], v[128:131], v[48:51], v[196:199]\n"
    "v_add_f32 v247, v247, v223\n"
    "v_add_f32 v242, v242, v192\n"
    "v_mfma_f32_16x16x32_bf16 a[192:195], v[160:163], v[232:235], a[192:195]\n"
    "v_exp_f32 v193, v193\n"
    "v_mfma_f32_16x16x32_bf16 a[224:227], v[160:163], v[236:239], a[224:227]\n"
    "ds_read_b64_tr_b16 v[160:161], v253 offset:33024\n"
    "ds_read_b64_tr_b16 v[162:163], v253 offset:37152\n"
    "v_cvt_pk_bf16_f32 v224, v192, v193\n"
    "v_mfma_f32_16x16x32_bf16 a[228:231], v[164:167], v[236:239], a[228:231]\n"
    "v_exp_f32 v194, v194\n"
    "v_mfma_f32_16x16x32_bf16 a[196:199], v[164:167], v[232:235], a[196:199]\n"
    "ds_read_b64_tr_b16 v[164:165], v253 offset:33032\n"
    "ds_read_b64_tr_b16 v[166:167], v253 offset:37160\n"
    "v_add_f32 v242, v242, v193\n"
    "v_mfma_f32_16x16x32_bf16 a[200:203], v[168:171], v[232:235], a[200:203]\n"
    "v_exp_f32 v195, v195\n"
    "v_mfma_f32_16x16x32_bf16 a[232:235], v[168:171], v[236:239], a[232:235]\n"
    "ds_read_b64_tr_b16 v[168:169], v253 offset:33280\n"
    "ds_read_b64_tr_b16 v[170:171], v253 offset:37408\n"
    "v_cvt_pk_bf16_f32 v225, v194, v195\n"
    "v_mfma_f32_16x16x32_bf16 a[236:239], v[172:175], v[236:239], a[236:239]\n"
    "v_exp_f32 v196, v196\n"
    "v_mfma_f32_16x16x32_bf16 a[204:207], v[172:175], v[232:235], a[204:207]\n"
    "s_waitcnt lgkmcnt(6)\n"
    "ds_read_b64_tr_b16 v[172:173], v253 offset:33288\n"
    "ds_read_b64_tr_b16 v[174:175], v253 offset:37416\n"
    "v_add_f32 v242, v242, v194\n"
    "v_mfma_f32_16x16x32_bf16 v[200:203], v[144:147], v[32:35], v[248:251]\n"
    "v_exp_f32 v197, v197\n"
    "v_mfma_f32_16x16x32_bf16 v[200:203], v[148:151], v[36:39], v[200:203]\n"
    "v_cvt_pk_bf16_f32 v228, v196, v197\n"
    "v_add_f32 v242, v242, v195\n"
    "v_mfma_f32_16x16x32_bf16 v[200:203], v[152:155], v[40:43], v[200:203]\n"
    "v_exp_f32 v198, v198\n"
    "v_mfma_f32_16x16x32_bf16 v[200:203], v[156:159], v[44:47], v[200:203]\n"
    "v_add_f32 v243, v243, v196\n"
    "v_add_f32 v243, v243, v197\n"
    "v_mfma_f32_16x16x32_bf16 v[204:207], v[156:159], v[60:63], v[248:251]\n"
    "v_exp_f32 v199, v199\n"
    "v_mfma_f32_16x16x32_bf16 v[204:207], v[152:155], v[56:59], v[204:207]\n"
    "v_cvt_pk_bf16_f32 v229, v198, v199\n"
    "v_add_f32 v243, v243, v198\n"
    "v_mfma_f32_16x16x32_bf16 v[204:207], v[148:151], v[52:55], v[204:207]\n"
    "v_exp_f32 v200, v200\n"
    "v_mfma_f32_16x16x32_bf16 v[204:207], v[144:147], v[48:51], v[204:207]\n"
    "v_add_f32 v243, v243, v199\n"
    "v_add_f32 v242, v242, v200\n"
    "v_mfma_f32_16x16x32_bf16 a[208:211], v[176:179], v[232:235], a[208:211]\n"
    "v_exp_f32 v201, v201\n"
    "v_mfma_f32_16x16x32_bf16 a[240:243], v[176:179], v[236:239], a[240:243]\n"
    "ds_read_b64_tr_b16 v[176:177], v253 offset:33536\n"
    "v_cvt_pk_bf16_f32 v226, v200, v201\n"
    "v_mfma_f32_16x16x32_bf16 a[244:247], v[180:183], v[236:239], a[244:247]\n"
    "v_exp_f32 v202, v202\n"
    "v_mfma_f32_16x16x32_bf16 a[212:215], v[180:183], v[232:235], a[212:215]\n"
    "ds_read_b64_tr_b16 v[178:179], v253 offset:37664\n"
    "ds_read_b64_tr_b16 v[180:181], v253 offset:33544\n"
    "v_add_f32 v242, v242, v201\n"
    "v_mfma_f32_16x16x32_bf16 a[216:219], v[184:187], v[232:235], a[216:219]\n"
    "v_exp_f32 v203, v203\n"
    "v_mfma_f32_16x16x32_bf16 a[248:251], v[184:187], v[236:239], a[248:251]\n"
    "ds_read_b64_tr_b16 v[182:183], v253 offset:37672\n"
    "v_cvt_pk_bf16_f32 v227, v202, v203\n"
    "v_mfma_f32_16x16x32_bf16 a[252:255], v[188:191], v[236:239], a[252:255]\n"
    "v_exp_f32 v204, v204\n"
    "v_mfma_f32_16x16x32_bf16 a[220:223], v[188:191], v[232:235], a[220:223]\n"
    "ds_read_b64_tr_b16 v[184:185], v253 offset:33792\n"
    "ds_read_b64_tr_b16 v[186:187], v253 offset:37920\n"
    "v_add_f32 v242, v242, v202\n"
    "v_mfma_f32_16x16x32_bf16 v[208:211], v[128:131], v[64:67], v[248:251]\n"
    "v_exp_f32 v205, v205\n"
    "v_mfma_f32_16x16x32_bf16 v[208:211], v[132:135], v[68:71], v[208:211]\n"
    "ds_read_b64_tr_b16 v[188:189], v253 offset:33800\n"
    "v_cvt_pk_bf16_f32 v230, v204, v205\n"
    "v_mfma_f32_16x16x32_bf16 v[208:211], v[136:139], v[72:75], v[208:211]\n"
    "v_exp_f32 v206, v206\n"
    "v_mfma_f32_16x16x32_bf16 v[208:211], v[140:143], v[76:79], v[208:211]\n"
    "ds_read_b64_tr_b16 v[190:191], v253 offset:37928\n"
    "v_add_f32 v242, v242, v203\n"
    "v_mfma_f32_16x16x32_bf16 v[212:215], v[140:143], v[92:95], v[248:251]\n"
    "v_exp_f32 v207, v207\n"
    "v_mfma_f32_16x16x32_bf16 v[212:215], v[136:139], v[88:91], v[212:215]\n"
    "v_cvt_pk_bf16_f32 v231, v206, v207\n"
    "v_add_f32 v243, v243, v204\n"
    "v_mfma_f32_16x16x32_bf16 v[212:215], v[132:135], v[84:87], v[212:215]\n"
    "v_exp_f32 v208, v208\n"
    "v_mfma_f32_16x16x32_bf16 v[212:215], v[128:131], v[80:83], v[212:215]\n"
    "s_waitcnt lgkmcnt(14)\n"
    "v_add_f32 v243, v243, v205\n"
    "v_add_f32 v244, v244, v208\n"
    "v_mfma_f32_16x16x32_bf16 a[64:67], v[160:163], v[224:227], a[64:67]\n"
    "v_exp_f32 v209, v209\n"
    "v_mfma_f32_16x16x32_bf16 a[96:99], v[160:163], v[228:231], a[96:99]\n"
    "s_waitcnt lgkmcnt(12)\n"
    "v_cvt_pk_bf16_f32 v232, v208, v209\n"
    "v_add_f32 v243, v243, v206\n"
    "v_mfma_f32_16x16x32_bf16 a[100:103], v[164:167], v[228:231], a[100:103]\n"
    "v_exp_f32 v210, v210\n"
    "v_mfma_f32_16x16x32_bf16 a[68:71], v[164:167], v[224:227], a[68:71]\n"
    "s_waitcnt lgkmcnt(10)\n"
    "v_add_f32 v243, v243, v207\n"
    "v_add_f32 v244, v244, v209\n"
    "v_mfma_f32_16x16x32_bf16 a[72:75], v[168:171], v[224:227], a[72:75]\n"
    "v_exp_f32 v211, v211\n"
    "v_mfma_f32_16x16x32_bf16 a[104:107], v[168:171], v[228:231], a[104:107]\n"
    "s_waitcnt lgkmcnt(8)\n"
    "v_cvt_pk_bf16_f32 v233, v210, v211\n"
    "v_add_f32 v244, v244, v210\n"
    "v_mfma_f32_16x16x32_bf16 a[108:111], v[172:175], v[228:231], a[108:111]\n"
    "v_exp_f32 v212, v212\n"
    "v_mfma_f32_16x16x32_bf16 a[76:79], v[172:175], v[224:227], a[76:79]\n"
    "v_add_f32 v244, v244, v211\n"
    "v_add_f32 v245, v245, v212\n"
    "v_mfma_f32_16x16x32_bf16 v[216:219], v[144:147], v[64:67], v[248:251]\n"
    "v_exp_f32 v213, v213\n"
    "v_mfma_f32_16x16x32_bf16 v[216:219], v[148:151], v[68:71], v[216:219]\n"
    "v_cvt_pk_bf16_f32 v236, v212, v213\n"
    "v_add_f32 v245, v245, v213\n"
    "v_mfma_f32_16x16x32_bf16 v[216:219], v[152:155], v[72:75], v[216:219]\n"
    "v_exp_f32 v214, v214\n"
    "v_mfma_f32_16x16x32_bf16 v[216:219], v[156:159], v[76:79], v[216:219]\n"
    "v_add_f32 v245, v245, v214\n"
    "v_mfma_f32_16x16x32_bf16 v[220:223], v[156:159], v[92:95], v[248:251]\n"
    "v_exp_f32 v215, v215\n"
    "v_mfma_f32_16x16x32_bf16 v[220:223], v[152:155], v[88:91], v[220:223]\n"
    "v_cvt_pk_bf16_f32 v237, v214, v215\n"
    "v_add_f32 v245, v245, v215\n"
    "v_mfma_f32_16x16x32_bf16 v[220:223], v[148:151], v[84:87], v[220:223]\n"
    "s_nop 0\n"
    "v_exp_f32 v216, v216\n"
    "v_mfma_f32_16x16x32_bf16 v[220:223], v[144:147], v[80:83], v[220:223]\n"
    "s_waitcnt lgkmcnt(6)\n"
    "v_add_f32 v244, v244, v216\n"
    "v_mfma_f32_16x16x32_bf16 a[80:83], v[176:179], v[224:227], a[80:83]\n"
    "v_exp_f32 v217, v217\n"
    "v_mfma_f32_16x16x32_bf16 a[112:115], v[176:179], v[228:231], a[112:115]\n"
    "s_waitcnt lgkmcnt(4)\n"
    "v_cvt_pk_bf16_f32 v234, v216, v217\n"
    "v_add_f32 v244, v244, v217\n"
    "v_mfma_f32_16x16x32_bf16 a[116:119], v[180:183], v[228:231], a[116:119]\n"
    "v_exp_f32 v218, v218\n"
    "v_mfma_f32_16x16x32_bf16 a[84:87], v[180:183], v[224:227], a[84:87]\n"
    "s_waitcnt lgkmcnt(2)\n"
    "v_add_f32 v244, v244, v218\n"
    "v_mfma_f32_16x16x32_bf16 a[88:91], v[184:187], v[224:227], a[88:91]\n"
    "v_exp_f32 v219, v219\n"
    "v_mfma_f32_16x16x32_bf16 a[120:123], v[184:187], v[228:231], a[120:123]\n"
    "s_waitcnt lgkmcnt(0)\n"
    "v_cvt_pk_bf16_f32 v235, v218, v219\n"
    "v_add_f32 v244, v244, v219\n"
    "v_mfma_f32_16x16x32_bf16 a[124:127], v[188:191], v[228:231], a[124:127]\n"
    "v_exp_f32 v220, v220\n"
    "v_mfma_f32_16x16x32_bf16 a[92:95], v[188:191], v[224:227], a[92:95]\n"
    "v_add_f32 v245, v245, v220\n"
    "v_mfma_f32_16x16x32_bf16 v[192:195], v[128:131], v[0:3], v[248:251]\n"
    "v_exp_f32 v221, v221\n"
    "v_mfma_f32_16x16x32_bf16 v[192:195], v[132:135], v[4:7], v[192:195]\n"
    "v_cvt_pk_bf16_f32 v238, v220, v221\n"
    "v_add_f32 v245, v245, v221\n"
    "v_mfma_f32_16x16x32_bf16 v[192:195], v[136:139], v[8:11], v[192:195]\n"
    "s_waitcnt vmcnt(4)\n"
    "v_exp_f32 v222, v222\n"
    "v_mfma_f32_16x16x32_bf16 v[192:195], v[140:143], v[12:15], v[192:195]\n"
    "s_barrier\n"
    "v_add_f32 v245, v245, v222\n"
    "v_mfma_f32_16x16x32_bf16 v[196:199], v[140:143], v[28:31], v[248:251]\n"
    "v_exp_f32 v223, v223\n"
    "v_mfma_f32_16x16x32_bf16 v[196:199], v[136:139], v[24:27], v[196:199]\n"
    "ds_read_b128 v[136:139], v252 offset:41792\n"
    "v_cvt_pk_bf16_f32 v239, v222, v223\n"
    "v_mfma_f32_16x16x32_bf16 v[196:199], v[132:135], v[20:23], v[196:199]\n"
    "v_exp_f32 v192, v192\n"
    "v_mfma_f32_16x16x32_bf16 v[196:199], v[128:131], v[16:19], v[196:199]\n"
    "ds_read_b128 v[128:131], v252 offset:41280\n"
    "ds_read_b128 v[132:135], v252 offset:41536\n"
    "v_add_f32 v245, v245, v223\n"
    "v_mfma_f32_16x16x32_bf16 a[128:131], v[160:163], v[232:235], a[128:131]\n"
    "v_exp_f32 v193, v193\n"
    "v_mfma_f32_16x16x32_bf16 a[160:163], v[160:163], v[236:239], a[160:163]\n"
    "ds_read_b128 v[140:143], v252 offset:42048\n"
    "v_cvt_pk_bf16_f32 v224, v192, v193\n"
    "v_mfma_f32_16x16x32_bf16 a[164:167], v[164:167], v[236:239], a[164:167]\n"
    "v_exp_f32 v194, v194\n"
    "v_mfma_f32_16x16x32_bf16 a[132:135], v[164:167], v[232:235], a[132:135]\n"
    "v_add_f32 v240, v240, v192\n"
    "v_add_f32 v240, v240, v193\n"
    "v_mfma_f32_16x16x32_bf16 a[136:139], v[168:171], v[232:235], a[136:139]\n"
    "v_exp_f32 v195, v195\n"
    "v_mfma_f32_16x16x32_bf16 a[168:171], v[168:171], v[236:239], a[168:171]\n"
    "v_cvt_pk_bf16_f32 v225, v194, v195\n"
    "v_add_f32 v240, v240, v194\n"
    "v_mfma_f32_16x16x32_bf16 a[172:175], v[172:175], v[236:239], a[172:175]\n"
    "v_exp_f32 v196, v196\n"
    "v_mfma_f32_16x16x32_bf16 a[140:143], v[172:175], v[232:235], a[140:143]\n"
    "v_add_f32 v240, v240, v195\n"
    "v_add_f32 v241, v241, v196\n"
    "v_mfma_f32_16x16x32_bf16 v[200:203], v[144:147], v[0:3], v[248:251]\n"
    "v_exp_f32 v197, v197\n"
    "v_mfma_f32_16x16x32_bf16 v[200:203], v[148:151], v[4:7], v[200:203]\n"
    "v_cvt_pk_bf16_f32 v228, v196, v197\n"
    "v_add_f32 v241, v241, v197\n"
    "v_mfma_f32_16x16x32_bf16 v[200:203], v[152:155], v[8:11], v[200:203]\n"
    "v_exp_f32 v198, v198\n"
    "v_mfma_f32_16x16x32_bf16 v[200:203], v[156:159], v[12:15], v[200:203]\n"
    "v_add_f32 v241, v241, v198\n"
    "v_mfma_f32_16x16x32_bf16 v[204:207], v[156:159], v[28:31], v[248:251]\n"
    "v_exp_f32 v199, v199\n"
    "v_mfma_f32_16x16x32_bf16 v[204:207], v[152:155], v[24:27], v[204:207]\n"
    "ds_read_b128 v[152:155], v252 offset:45920\n"
    "v_cvt_pk_bf16_f32 v229, v198, v199\n"
    "v_mfma_f32_16x16x32_bf16 v[204:207], v[148:151], v[20:23], v[204:207]\n"
    "s_nop 0\n"
    "v_exp_f32 v200, v200\n"
    "v_mfma_f32_16x16x32_bf16 v[204:207], v[144:147], v[16:19], v[204:207]\n"
    "ds_read_b128 v[144:147], v252 offset:45408\n"
    "ds_read_b128 v[148:151], v252 offset:45664\n"
    "v_add_f32 v241, v241, v199\n"
    "v_mfma_f32_16x16x32_bf16 a[144:147], v[176:179], v[232:235], a[144:147]\n"
    "v_exp_f32 v201, v201\n"
    "v_mfma_f32_16x16x32_bf16 a[176:179], v[176:179], v[236:239], a[176:179]\n"
    "ds_read_b128 v[156:159], v252 offset:46176\n"
    "v_cvt_pk_bf16_f32 v226, v200, v201\n"
    "v_mfma_f32_16x16x32_bf16 a[180:183], v[180:183], v[236:239], a[180:183]\n"
    "v_exp_f32 v202, v202\n"
    "v_mfma_f32_16x16x32_bf16 a[148:151], v[180:183], v[232:235], a[148:151]\n"
    "v_add_f32 v240, v240, v200\n"
    "v_add_f32 v240, v240, v201\n"
    "v_mfma_f32_16x16x32_bf16 a[152:155], v[184:187], v[232:235], a[152:155]\n"
    "v_exp_f32 v203, v203\n"
    "v_mfma_f32_16x16x32_bf16 a[184:187], v[184:187], v[236:239], a[184:187]\n"
    "v_cvt_pk_bf16_f32 v227, v202, v203\n"
    "v_add_f32 v240, v240, v202\n"
    "v_mfma_f32_16x16x32_bf16 a[188:191], v[188:191], v[236:239], a[188:191]\n"
    "v_exp_f32 v204, v204\n"
    "v_mfma_f32_16x16x32_bf16 a[156:159], v[188:191], v[232:235], a[156:159]\n"
    "s_waitcnt lgkmcnt(6)\n"
    "v_add_f32 v240, v240, v203\n"
    "v_add_f32 v241, v241, v204\n"
    "v_mfma_f32_16x16x32_bf16 v[208:211], v[128:131], v[0:3], v[248:251]\n"
    "s_waitcnt lgkmcnt(5)\n"
    "v_exp_f32 v205, v205\n"
    "v_mfma_f32_16x16x32_bf16 v[208:211], v[132:135], v[4:7], v[208:211]\n"
    "v_cvt_pk_bf16_f32 v230, v204, v205\n"
    "v_add_f32 v241, v241, v205\n"
    "v_mfma_f32_16x16x32_bf16 v[208:211], v[136:139], v[8:11], v[208:211]\n"
    "s_waitcnt lgkmcnt(4)\n"
    "v_exp_f32 v206, v206\n"
    "v_mfma_f32_16x16x32_bf16 v[208:211], v[140:143], v[12:15], v[208:211]\n"
    "v_add_f32 v241, v241, v206\n"
    "v_mfma_f32_16x16x32_bf16 v[212:215], v[140:143], v[28:31], v[248:251]\n"
    "v_exp_f32 v207, v207\n"
    "v_mfma_f32_16x16x32_bf16 v[212:215], v[136:139], v[24:27], v[212:215]\n"
    "ds_read_b128 v[136:139], v252 offset:512\n"
    "ds_read_b128 v[140:143], v252 offset:768\n"
    "v_cvt_pk_bf16_f32 v231, v206, v207\n"
    "v_mfma_f32_16x16x32_bf16 v[212:215], v[132:135], v[20:23], v[212:215]\n"
    "v_exp_f32 v208, v208\n"
    "v_mfma_f32_16x16x32_bf16 v[212:215], v[128:131], v[16:19], v[212:215]\n"
    "ds_read_b128 v[128:131], v252 offset:0\n"
    "ds_read_b128 v[132:135], v252 offset:256\n"
    "v_add_f32 v241, v241, v207\n"
    "v_mfma_f32_16x16x32_bf16 a[0:3], v[160:163], v[224:227], a[0:3]\n"
    "v_exp_f32 v209, v209\n"
    "v_mfma_f32_16x16x32_bf16 a[32:35], v[160:163], v[228:231], a[32:35]\n"
    "ds_read_b64_tr_b16 v[160:161], v253 offset:41280\n"
    "ds_read_b64_tr_b16 v[162:163], v253 offset:45408\n"
    "v_cvt_pk_bf16_f32 v232, v208, v209\n"
    "v_mfma_f32_16x16x32_bf16 a[36:39], v[164:167], v[228:231], a[36:39]\n"
    "v_exp_f32 v210, v210\n"
    "v_mfma_f32_16x16x32_bf16 a[4:7], v[164:167], v[224:227], a[4:7]\n"
    "ds_read_b64_tr_b16 v[164:165], v253 offset:41288\n"
    "ds_read_b64_tr_b16 v[166:167], v253 offset:45416\n"
    "v_add_f32 v240, v240, v208\n"
    "v_mfma_f32_16x16x32_bf16 a[8:11], v[168:171], v[224:227], a[8:11]\n"
    "v_exp_f32 v211, v211\n"
    "v_mfma_f32_16x16x32_bf16 a[40:43], v[168:171], v[228:231], a[40:43]\n"
    "ds_read_b64_tr_b16 v[168:169], v253 offset:41536\n"
    "ds_read_b64_tr_b16 v[170:171], v253 offset:45664\n"
    "v_cvt_pk_bf16_f32 v233, v210, v211\n"
    "v_mfma_f32_16x16x32_bf16 a[44:47], v[172:175], v[228:231], a[44:47]\n"
    "v_exp_f32 v212, v212\n"
    "v_mfma_f32_16x16x32_bf16 a[12:15], v[172:175], v[224:227], a[12:15]\n"
    "s_waitcnt lgkmcnt(12)\n"
    "ds_read_b64_tr_b16 v[172:173], v253 offset:41544\n"
    "ds_read_b64_tr_b16 v[174:175], v253 offset:45672\n"
    "v_add_f32 v240, v240, v209\n"
    "v_mfma_f32_16x16x32_bf16 v[216:219], v[144:147], v[0:3], v[248:251]\n"
    "s_waitcnt lgkmcnt(13)\n"
    "v_exp_f32 v213, v213\n"
    "v_mfma_f32_16x16x32_bf16 v[216:219], v[148:151], v[4:7], v[216:219]\n"
    "v_cvt_pk_bf16_f32 v236, v212, v213\n"
    "v_add_f32 v240, v240, v210\n"
    "v_add_f32 v241, v241, v212\n"
    "v_mfma_f32_16x16x32_bf16 v[216:219], v[152:155], v[8:11], v[216:219]\n"
    "s_waitcnt lgkmcnt(12)\n"
    "v_exp_f32 v214, v214\n"
    "v_mfma_f32_16x16x32_bf16 v[216:219], v[156:159], v[12:15], v[216:219]\n"
    "v_add_f32 v240, v240, v211\n"
    "v_add_f32 v241, v241, v213\n"
    "v_add_f32 v241, v241, v214\n"
    "v_mfma_f32_16x16x32_bf16 v[220:223], v[156:159], v[28:31], v[248:251]\n"
    "v_exp_f32 v215, v215\n"
    "v_mfma_f32_16x16x32_bf16 v[220:223], v[152:155], v[24:27], v[220:223]\n"
    "ds_read_b128 v[152:155], v252 offset:4640\n"
    "ds_read_b128 v[156:159], v252 offset:4896\n"
    "v_cvt_pk_bf16_f32 v237, v214, v215\n"
    "v_mfma_f32_16x16x32_bf16 v[220:223], v[148:151], v[20:23], v[220:223]\n"
    "v_exp_f32 v216, v216\n"
    "v_mfma_f32_16x16x32_bf16 v[220:223], v[144:147], v[16:19], v[220:223]\n"
    "ds_read_b128 v[144:147], v252 offset:4128\n"
    "ds_read_b128 v[148:151], v252 offset:4384\n"
    "v_add_f32 v241, v241, v215\n"
    "v_mfma_f32_16x16x32_bf16 a[16:19], v[176:179], v[224:227], a[16:19]\n"
    "v_exp_f32 v217, v217\n"
    "v_mfma_f32_16x16x32_bf16 a[48:51], v[176:179], v[228:231], a[48:51]\n"
    "ds_read_b64_tr_b16 v[176:177], v253 offset:41792\n"
    "ds_read_b64_tr_b16 v[178:179], v253 offset:45920\n"
    "v_cvt_pk_bf16_f32 v234, v216, v217\n"
    "v_mfma_f32_16x16x32_bf16 a[52:55], v[180:183], v[228:231], a[52:55]\n"
    "v_exp_f32 v218, v218\n"
    "v_mfma_f32_16x16x32_bf16 a[20:23], v[180:183], v[224:227], a[20:23]\n"
    "ds_read_b64_tr_b16 v[180:181], v253 offset:41800\n"
    "ds_read_b64_tr_b16 v[182:183], v253 offset:45928\n"
    "v_add_f32 v240, v240, v216\n"
    "v_mfma_f32_16x16x32_bf16 a[24:27], v[184:187], v[224:227], a[24:27]\n"
    "v_exp_f32 v219, v219\n"
    "v_mfma_f32_16x16x32_bf16 a[56:59], v[184:187], v[228:231], a[56:59]\n"
    "ds_read_b64_tr_b16 v[184:185], v253 offset:42048\n"
    "ds_read_b64_tr_b16 v[186:187], v253 offset:46176\n"
    "v_cvt_pk_bf16_f32 v235, v218, v219\n"
    "v_mfma_f32_16x16x32_bf16 a[60:63], v[188:191], v[228:231], a[60:63]\n"
    "v_exp_f32 v220, v220\n"
    "v_mfma_f32_16x16x32_bf16 a[28:31], v[188:191], v[224:227], a[28:31]\n"
    "s_waitcnt lgkmcnt(15)\n"
    "ds_read_b64_tr_b16 v[188:189], v253 offset:42056\n"
    "ds_read_b64_tr_b16 v[190:191], v253 offset:46184\n"
    "v_add_f32 v240, v240, v217\n"
    "v_mfma_f32_16x16x32_bf16 v[192:195], v[128:131], v[0:3], v[248:251]\n"
    "v_exp_f32 v221, v221\n"
    "v_mfma_f32_16x16x32_bf16 v[192:195], v[132:135], v[4:7], v[192:195]\n"
    "v_cvt_pk_bf16_f32 v238, v220, v221\n"
    "v_add_f32 v240, v240, v218\n"
    "v_add_f32 v241, v241, v220\n"
    "v_mfma_f32_16x16x32_bf16 v[192:195], v[136:139], v[8:11], v[192:195]\n"
    "s_waitcnt vmcnt(0)\n"
    "v_exp_f32 v222, v222\n"
    "v_mfma_f32_16x16x32_bf16 v[192:195], v[140:143], v[12:15], v[192:195]\n"
    "s_barrier\n"
    "v_add_f32 v240, v240, v219\n"
    "v_add_f32 v241, v241, v221\n"
    "v_add_f32 v241, v241, v222\n"
    "v_mfma_f32_16x16x32_bf16 v[196:199], v[140:143], v[28:31], v[248:251]\n"
    "v_exp_f32 v223, v223\n"
    "v_mfma_f32_16x16x32_bf16 v[196:199], v[136:139], v[24:27], v[196:199]\n"
    "ds_read_b128 v[136:139], v252 offset:8768\n"
    "ds_read_b128 v[140:143], v252 offset:9024\n"
    "v_cvt_pk_bf16_f32 v239, v222, v223\n"
    "v_mfma_f32_16x16x32_bf16 v[196:199], v[132:135], v[20:23], v[196:199]\n"
    "v_exp_f32 v192, v192\n"
    "v_mfma_f32_16x16x32_bf16 v[196:199], v[128:131], v[16:19], v[196:199]\n"
    "ds_read_b128 v[128:131], v252 offset:8256\n"
    "ds_read_b128 v[132:135], v252 offset:8512\n"
    "v_add_f32 v241, v241, v223\n"
    "v_mfma_f32_16x16x32_bf16 a[0:3], v[160:163], v[232:235], a[0:3]\n"
    "v_exp_f32 v193, v193\n"
    "v_mfma_f32_16x16x32_bf16 a[32:35], v[160:163], v[236:239], a[32:35]\n"
    "s_waitcnt lgkmcnt(15)\n"
    "ds_read_b64_tr_b16 v[160:161], v253 offset:0\n"
    "ds_read_b64_tr_b16 v[162:163], v253 offset:4128\n"
    "v_cvt_pk_bf16_f32 v224, v192, v193\n"
    "v_mfma_f32_16x16x32_bf16 a[36:39], v[164:167], v[236:239], a[36:39]\n"
    "v_exp_f32 v194, v194\n"
    "v_mfma_f32_16x16x32_bf16 a[4:7], v[164:167], v[232:235], a[4:7]\n"
    "ds_read_b64_tr_b16 v[164:165], v253 offset:8\n"
    "ds_read_b64_tr_b16 v[166:167], v253 offset:4136\n"
    "v_add_f32 v240, v240, v192\n"
    "v_mfma_f32_16x16x32_bf16 a[8:11], v[168:171], v[232:235], a[8:11]\n"
    "v_exp_f32 v195, v195\n"
    "v_mfma_f32_16x16x32_bf16 a[40:43], v[168:171], v[236:239], a[40:43]\n"
    "ds_read_b64_tr_b16 v[168:169], v253 offset:256\n"
    "ds_read_b64_tr_b16 v[170:171], v253 offset:4384\n"
    "v_cvt_pk_bf16_f32 v225, v194, v195\n"
    "v_mfma_f32_16x16x32_bf16 a[44:47], v[172:175], v[236:239], a[44:47]\n"
    "v_exp_f32 v196, v196\n"
    "v_mfma_f32_16x16x32_bf16 a[12:15], v[172:175], v[232:235], a[12:15]\n"
    "s_waitcnt lgkmcnt(15)\n"
    "ds_read_b64_tr_b16 v[172:173], v253 offset:264\n"
    "ds_read_b64_tr_b16 v[174:175], v253 offset:4392\n"
    "v_add_f32 v240, v240, v193\n"
    "v_mfma_f32_16x16x32_bf16 v[200:203], v[144:147], v[0:3], v[248:251]\n"
    "v_exp_f32 v197, v197\n"
    "v_mfma_f32_16x16x32_bf16 v[200:203], v[148:151], v[4:7], v[200:203]\n"
    "v_cvt_pk_bf16_f32 v228, v196, v197\n"
    "v_add_f32 v240, v240, v194\n"
    "v_add_f32 v241, v241, v196\n"
    "v_mfma_f32_16x16x32_bf16 v[200:203], v[152:155], v[8:11], v[200:203]\n"
    "v_exp_f32 v198, v198\n"
    "v_mfma_f32_16x16x32_bf16 v[200:203], v[156:159], v[12:15], v[200:203]\n"
    "v_add_f32 v240, v240, v195\n"
    "v_add_f32 v241, v241, v197\n"
    "v_add_f32 v241, v241, v198\n"
    "v_mfma_f32_16x16x32_bf16 v[204:207], v[156:159], v[28:31], v[248:251]\n"
    "v_exp_f32 v199, v199\n"
    "v_mfma_f32_16x16x32_bf16 v[204:207], v[152:155], v[24:27], v[204:207]\n"
    "ds_read_b128 v[152:155], v252 offset:12896\n"
    "ds_read_b128 v[156:159], v252 offset:13152\n"
    "v_cvt_pk_bf16_f32 v229, v198, v199\n"
    "v_mfma_f32_16x16x32_bf16 v[204:207], v[148:151], v[20:23], v[204:207]\n"
    "v_exp_f32 v200, v200\n"
    "v_mfma_f32_16x16x32_bf16 v[204:207], v[144:147], v[16:19], v[204:207]\n"
    "ds_read_b128 v[144:147], v252 offset:12384\n"
    "ds_read_b128 v[148:151], v252 offset:12640\n"
    "v_add_f32 v241, v241, v199\n"
    "v_mfma_f32_16x16x32_bf16 a[16:19], v[176:179], v[232:235], a[16:19]\n"
    "v_exp_f32 v201, v201\n"
    "v_mfma_f32_16x16x32_bf16 a[48:51], v[176:179], v[236:239], a[48:51]\n"
    "s_waitcnt lgkmcnt(15)\n"
    "ds_read_b64_tr_b16 v[176:177], v253 offset:512\n"
    "ds_read_b64_tr_b16 v[178:179], v253 offset:4640\n"
    "v_cvt_pk_bf16_f32 v226, v200, v201\n"
    "v_mfma_f32_16x16x32_bf16 a[52:55], v[180:183], v[236:239], a[52:55]\n"
    "v_exp_f32 v202, v202\n"
    "v_mfma_f32_16x16x32_bf16 a[20:23], v[180:183], v[232:235], a[20:23]\n"
    "ds_read_b64_tr_b16 v[180:181], v253 offset:520\n"
    "ds_read_b64_tr_b16 v[182:183], v253 offset:4648\n"
    "v_add_f32 v240, v240, v200\n"
    "v_mfma_f32_16x16x32_bf16 a[24:27], v[184:187], v[232:235], a[24:27]\n"
    "v_exp_f32 v203, v203\n"
    "v_mfma_f32_16x16x32_bf16 a[56:59], v[184:187], v[236:239], a[56:59]\n"
    "ds_read_b64_tr_b16 v[184:185], v253 offset:768\n"
    "ds_read_b64_tr_b16 v[186:187], v253 offset:4896\n"
    "v_cvt_pk_bf16_f32 v227, v202, v203\n"
    "v_mfma_f32_16x16x32_bf16 a[60:63], v[188:191], v[236:239], a[60:63]\n"
    "v_exp_f32 v204, v204\n"
    "v_mfma_f32_16x16x32_bf16 a[28:31], v[188:191], v[232:235], a[28:31]\n"
    "s_waitcnt lgkmcnt(15)\n"
    "ds_read_b64_tr_b16 v[188:189], v253 offset:776\n"
    "ds_read_b64_tr_b16 v[190:191], v253 offset:4904\n"
    "v_add_f32 v240, v240, v201\n"
    "v_mfma_f32_16x16x32_bf16 v[208:211], v[128:131], v[0:3], v[248:251]\n"
    "v_exp_f32 v205, v205\n"
    "v_mfma_f32_16x16x32_bf16 v[208:211], v[132:135], v[4:7], v[208:211]\n"
    "v_cvt_pk_bf16_f32 v230, v204, v205\n"
    "v_add_f32 v240, v240, v202\n"
    "v_mfma_f32_16x16x32_bf16 v[208:211], v[136:139], v[8:11], v[208:211]\n"
    "v_exp_f32 v206, v206\n"
    "v_mfma_f32_16x16x32_bf16 v[208:211], v[140:143], v[12:15], v[208:211]\n"
    "v_add_f32 v240, v240, v203\n"
    "v_add_f32 v241, v241, v204\n"
    "v_mfma_f32_16x16x32_bf16 v[212:215], v[140:143], v[28:31], v[248:251]\n"
    "v_exp_f32 v207, v207\n"
    "v_mfma_f32_16x16x32_bf16 v[212:215], v[136:139], v[24:27], v[212:215]\n"
    "v_cvt_pk_bf16_f32 v231, v206, v207\n"
    "v_add_f32 v241, v241, v205\n"
    "v_mfma_f32_16x16x32_bf16 v[212:215], v[132:135], v[20:23], v[212:215]\n"
    "v_exp_f32 v208, v208\n"
    "v_mfma_f32_16x16x32_bf16 v[212:215], v[128:131], v[16:19], v[212:215]\n"
    "v_add_f32 v241, v241, v206\n"
    "v_add_f32 v240, v240, v208\n"
    "v_mfma_f32_16x16x32_bf16 a[0:3], v[160:163], v[224:227], a[0:3]\n"
    "v_exp_f32 v209, v209\n"
    "v_mfma_f32_16x16x32_bf16 a[32:35], v[160:163], v[228:231], a[32:35]\n"
    "s_waitcnt lgkmcnt(15)\n"
    "ds_read_b64_tr_b16 v[160:161], v253 offset:8256\n"
    "ds_read_b64_tr_b16 v[162:163], v253 offset:12384\n"
    "v_cvt_pk_bf16_f32 v232, v208, v209\n"
    "v_mfma_f32_16x16x32_bf16 a[36:39], v[164:167], v[228:231], a[36:39]\n"
    "v_exp_f32 v210, v210\n"
    "v_mfma_f32_16x16x32_bf16 a[4:7], v[164:167], v[224:227], a[4:7]\n"
    "s_waitcnt lgkmcnt(15)\n"
    "ds_read_b64_tr_b16 v[164:165], v253 offset:8264\n"
    "ds_read_b64_tr_b16 v[166:167], v253 offset:12392\n"
    "v_add_f32 v241, v241, v207\n"
    "v_mfma_f32_16x16x32_bf16 a[8:11], v[168:171], v[224:227], a[8:11]\n"
    "v_exp_f32 v211, v211\n"
    "v_mfma_f32_16x16x32_bf16 a[40:43], v[168:171], v[228:231], a[40:43]\n"
    "s_waitcnt lgkmcnt(15)\n"
    "ds_read_b64_tr_b16 v[168:169], v253 offset:8512\n"
    "ds_read_b64_tr_b16 v[170:171], v253 offset:12640\n"
    "v_cvt_pk_bf16_f32 v233, v210, v211\n"
    "v_mfma_f32_16x16x32_bf16 a[44:47], v[172:175], v[228:231], a[44:47]\n"
    "v_exp_f32 v212, v212\n"
    "v_mfma_f32_16x16x32_bf16 a[12:15], v[172:175], v[224:227], a[12:15]\n"
    "s_waitcnt lgkmcnt(15)\n"
    "ds_read_b64_tr_b16 v[172:173], v253 offset:8520\n"
    "ds_read_b64_tr_b16 v[174:175], v253 offset:12648\n"
    "v_add_f32 v240, v240, v209\n"
    "v_mfma_f32_16x16x32_bf16 v[216:219], v[144:147], v[0:3], v[248:251]\n"
    "s_waitcnt lgkmcnt(15)\n"
    "v_exp_f32 v213, v213\n"
    "v_mfma_f32_16x16x32_bf16 v[216:219], v[148:151], v[4:7], v[216:219]\n"
    "v_cvt_pk_bf16_f32 v236, v212, v213\n"
    "v_add_f32 v240, v240, v210\n"
    "v_mfma_f32_16x16x32_bf16 v[216:219], v[152:155], v[8:11], v[216:219]\n"
    "v_exp_f32 v214, v214\n"
    "v_mfma_f32_16x16x32_bf16 v[216:219], v[156:159], v[12:15], v[216:219]\n"
    "v_add_f32 v240, v240, v211\n"
    "v_add_f32 v241, v241, v212\n"
    "v_mfma_f32_16x16x32_bf16 v[220:223], v[156:159], v[28:31], v[248:251]\n"
    "v_exp_f32 v215, v215\n"
    "v_mfma_f32_16x16x32_bf16 v[220:223], v[152:155], v[24:27], v[220:223]\n"
    "v_cvt_pk_bf16_f32 v237, v214, v215\n"
    "v_add_f32 v241, v241, v213\n"
    "v_mfma_f32_16x16x32_bf16 v[220:223], v[148:151], v[20:23], v[220:223]\n"
    "v_exp_f32 v216, v216\n"
    "v_mfma_f32_16x16x32_bf16 v[220:223], v[144:147], v[16:19], v[220:223]\n"
    "s_waitcnt lgkmcnt(14)\n"
    "v_add_f32 v241, v241, v214\n"
    "v_add_f32 v240, v240, v216\n"
    "v_mfma_f32_16x16x32_bf16 a[16:19], v[176:179], v[224:227], a[16:19]\n"
    "v_exp_f32 v217, v217\n"
    "v_mfma_f32_16x16x32_bf16 a[48:51], v[176:179], v[228:231], a[48:51]\n"
    "s_waitcnt lgkmcnt(12)\n"
    "ds_read_b64_tr_b16 v[176:177], v253 offset:8768\n"
    "v_cvt_pk_bf16_f32 v234, v216, v217\n"
    "v_mfma_f32_16x16x32_bf16 a[52:55], v[180:183], v[228:231], a[52:55]\n"
    "v_exp_f32 v218, v218\n"
    "v_mfma_f32_16x16x32_bf16 a[20:23], v[180:183], v[224:227], a[20:23]\n"
    "s_waitcnt lgkmcnt(11)\n"
    "ds_read_b64_tr_b16 v[178:179], v253 offset:12896\n"
    "ds_read_b64_tr_b16 v[180:181], v253 offset:8776\n"
    "v_add_f32 v241, v241, v215\n"
    "v_mfma_f32_16x16x32_bf16 a[24:27], v[184:187], v[224:227], a[24:27]\n"
    "v_exp_f32 v219, v219\n"
    "v_mfma_f32_16x16x32_bf16 a[56:59], v[184:187], v[228:231], a[56:59]\n"
    "s_waitcnt lgkmcnt(11)\n"
    "ds_read_b64_tr_b16 v[182:183], v253 offset:12904\n"
    "v_cvt_pk_bf16_f32 v235, v218, v219\n"
    "v_mfma_f32_16x16x32_bf16 a[60:63], v[188:191], v[228:231], a[60:63]\n"
    "v_exp_f32 v220, v220\n"
    "v_mfma_f32_16x16x32_bf16 a[28:31], v[188:191], v[224:227], a[28:31]\n"
    "ds_read_b64_tr_b16 v[184:185], v253 offset:9024\n"
    "ds_read_b64_tr_b16 v[186:187], v253 offset:13152\n"
    "v_add_f32 v240, v240, v217\n"
    "v_mfma_f32_16x16x32_bf16 v[192:195], v[128:131], v[32:35], v[248:251]\n"
    "v_exp_f32 v221, v221\n"
    "v_mfma_f32_16x16x32_bf16 v[192:195], v[132:135], v[36:39], v[192:195]\n"
    "ds_read_b64_tr_b16 v[188:189], v253 offset:9032\n"
    "v_cvt_pk_bf16_f32 v238, v220, v221\n"
    "v_mfma_f32_16x16x32_bf16 v[192:195], v[136:139], v[40:43], v[192:195]\n"
    "v_exp_f32 v222, v222\n"
    "v_mfma_f32_16x16x32_bf16 v[192:195], v[140:143], v[44:47], v[192:195]\n"
    "ds_read_b64_tr_b16 v[190:191], v253 offset:13160\n"
    "v_add_f32 v240, v240, v218\n"
    "v_mfma_f32_16x16x32_bf16 v[196:199], v[140:143], v[60:63], v[248:251]\n"
    "v_exp_f32 v223, v223\n"
    "v_mfma_f32_16x16x32_bf16 v[196:199], v[136:139], v[56:59], v[196:199]\n"
    "ds_read_b128 v[136:139], v252 offset:512\n"
    "v_cvt_pk_bf16_f32 v239, v222, v223\n"
    "v_mfma_f32_16x16x32_bf16 v[196:199], v[132:135], v[52:55], v[196:199]\n"
    "v_exp_f32 v192, v192\n"
    "v_mfma_f32_16x16x32_bf16 v[196:199], v[128:131], v[48:51], v[196:199]\n"
    "s_waitcnt lgkmcnt(15)\n"
    "ds_read_b128 v[128:131], v252 offset:0\n"
    "ds_read_b128 v[132:135], v252 offset:256\n"
    "v_add_f32 v240, v240, v219\n"
    "v_mfma_f32_16x16x32_bf16 a[0:3], v[160:163], v[232:235], a[0:3]\n"
    "v_exp_f32 v193, v193\n"
    "v_mfma_f32_16x16x32_bf16 a[32:35], v[160:163], v[236:239], a[32:35]\n"
    "s_waitcnt lgkmcnt(15)\n"
    "ds_read_b128 v[140:143], v252 offset:768\n"
    "v_add_f32 v241, v241, v220\n"
    "v_mfma_f32_16x16x32_bf16 a[36:39], v[164:167], v[236:239], a[36:39]\n"
    "v_exp_f32 v194, v194\n"
    "v_mfma_f32_16x16x32_bf16 a[4:7], v[164:167], v[232:235], a[4:7]\n"
    "s_waitcnt lgkmcnt(14)\n"
    "v_add_f32 v241, v241, v221\n"
    "v_cvt_pk_bf16_f32 v224, v192, v193\n"
    "v_mfma_f32_16x16x32_bf16 a[8:11], v[168:171], v[232:235], a[8:11]\n"
    "v_exp_f32 v195, v195\n"
    "v_mfma_f32_16x16x32_bf16 a[40:43], v[168:171], v[236:239], a[40:43]\n"
    "s_waitcnt lgkmcnt(12)\n"
    "v_add_f32 v241, v241, v222\n"
    "v_cvt_pk_bf16_f32 v225, v194, v195\n"
    "v_mfma_f32_16x16x32_bf16 a[44:47], v[172:175], v[236:239], a[44:47]\n"
    "v_exp_f32 v196, v196\n"
    "v_mfma_f32_16x16x32_bf16 a[12:15], v[172:175], v[232:235], a[12:15]\n"
    "v_add_f32 v241, v241, v223\n"
    "v_add_f32 v242, v242, v192\n"
    "v_mfma_f32_16x16x32_bf16 v[200:203], v[144:147], v[32:35], v[248:251]\n"
    "v_exp_f32 v197, v197\n"
    "v_mfma_f32_16x16x32_bf16 v[200:203], v[148:151], v[36:39], v[200:203]\n"
    "v_cvt_pk_bf16_f32 v228, v196, v197\n"
    "v_add_f32 v242, v242, v193\n"
    "v_mfma_f32_16x16x32_bf16 v[200:203], v[152:155], v[40:43], v[200:203]\n"
    "v_exp_f32 v198, v198\n"
    "v_mfma_f32_16x16x32_bf16 v[200:203], v[156:159], v[44:47], v[200:203]\n"
    "v_add_f32 v242, v242, v194\n"
    "v_add_f32 v243, v243, v196\n"
    "v_mfma_f32_16x16x32_bf16 v[204:207], v[156:159], v[60:63], v[248:251]\n"
    "v_exp_f32 v199, v199\n"
    "v_mfma_f32_16x16x32_bf16 v[204:207], v[152:155], v[56:59], v[204:207]\n"
    "ds_read_b128 v[152:155], v252 offset:4640\n"
    "v_cvt_pk_bf16_f32 v229, v198, v199\n"
    "v_mfma_f32_16x16x32_bf16 v[204:207], v[148:151], v[52:55], v[204:207]\n"
    "v_exp_f32 v200, v200\n"
    "v_mfma_f32_16x16x32_bf16 v[204:207], v[144:147], v[48:51], v[204:207]\n"
    "s_waitcnt lgkmcnt(11)\n"
    "ds_read_b128 v[144:147], v252 offset:4128\n"
    "ds_read_b128 v[148:151], v252 offset:4384\n"
    "v_add_f32 v242, v242, v195\n"
    "v_mfma_f32_16x16x32_bf16 a[16:19], v[176:179], v[232:235], a[16:19]\n"
    "v_exp_f32 v201, v201\n"
    "v_mfma_f32_16x16x32_bf16 a[48:51], v[176:179], v[236:239], a[48:51]\n"
    "s_waitcnt lgkmcnt(11)\n"
    "ds_read_b128 v[156:159], v252 offset:4896\n"
    "v_cvt_pk_bf16_f32 v226, v200, v201\n"
    "v_mfma_f32_16x16x32_bf16 a[52:55], v[180:183], v[236:239], a[52:55]\n"
    "v_exp_f32 v202, v202\n"
    "v_mfma_f32_16x16x32_bf16 a[20:23], v[180:183], v[232:235], a[20:23]\n"
    "s_waitcnt lgkmcnt(10)\n"
    "v_add_f32 v243, v243, v197\n"
    "v_add_f32 v242, v242, v200\n"
    "v_mfma_f32_16x16x32_bf16 a[24:27], v[184:187], v[232:235], a[24:27]\n"
    "v_exp_f32 v203, v203\n"
    "v_mfma_f32_16x16x32_bf16 a[56:59], v[184:187], v[236:239], a[56:59]\n"
    "s_waitcnt lgkmcnt(8)\n"
    "v_cvt_pk_bf16_f32 v227, v202, v203\n"
    "v_add_f32 v243, v243, v198\n"
    "v_mfma_f32_16x16x32_bf16 a[60:63], v[188:191], v[236:239], a[60:63]\n"
    "v_exp_f32 v204, v204\n"
    "v_mfma_f32_16x16x32_bf16 a[28:31], v[188:191], v[232:235], a[28:31]\n"
    "v_add_f32 v243, v243, v199\n"
    "v_add_f32 v242, v242, v201\n"
    "v_permlane16_swap_b32 v240, v241\n"
    "v_accvgpr_read_b32 v0, a0\n"
    "v_add_f32 v240, v240, v241\n"
    "v_mov_b32 v241, v240\n"
    "v_accvgpr_read_b32 v1, a1\n"
    "v_accvgpr_read_b32 v2, a2\n"
    "v_permlane32_swap_b32 v240, v241\n"
    "v_accvgpr_read_b32 v3, a3\n"
    "v_add_f32 v240, v240, v241\n"
    "v_cmp_nlt_f32 vcc, s62, v240\n"
    "v_cmp_ngt_f32 s[66:67], s63, v240\n"
    "v_rcp_f32 v240, v240\n"
    "v_accvgpr_read_b32 v4, a4\n"
    "s_or_b64 s[66:67], s[66:67], vcc\n"
    "v_mov_b32 v241, v240\n"
    "v_accvgpr_read_b32 v5, a5\n"
    "v_accvgpr_read_b32 v6, a6\n"
    "s_or_b64 s[60:61], s[60:61], s[66:67]\n"
    "v_permlane16_swap_b32 v240, v241\n"
    "v_accvgpr_read_b32 v7, a7\n"
    "v_pk_mul_f32 v[0:1], v[0:1], v[240:241] op_sel_hi:[1,0]\n"
    "v_pk_mul_f32 v[2:3], v[2:3], v[240:241] op_sel_hi:[1,0]\n"
    "v_pk_mul_f32 v[4:5], v[4:5], v[240:241] op_sel_hi:[1,0]\n"
    "v_pk_mul_f32 v[6:7], v[6:7], v[240:241] op_sel_hi:[1,0]\n"
    "v_cvt_pk_bf16_f32 v0, v0, v1\n"
    "v_cvt_pk_bf16_f32 v1, v2, v3\n"
    "v_cvt_pk_bf16_f32 v2, v4, v5\n"
    "v_cvt_pk_bf16_f32 v3, v6, v7\n"
    "v_accvgpr_read_b32 v4, a8\n"
    "v_accvgpr_read_b32 v5, a9\n"
    "v_accvgpr_read_b32 v6, a10\n"
    "v_accvgpr_read_b32 v7, a11\n"
    "v_accvgpr_read_b32 v8, a12\n"
    "v_accvgpr_read_b32 v9, a13\n"
    "v_accvgpr_read_b32 v10, a14\n"
    "v_accvgpr_read_b32 v11, a15\n"
    "v_pk_mul_f32 v[4:5], v[4:5], v[240:241] op_sel_hi:[1,0]\n"
    "v_pk_mul_f32 v[6:7], v[6:7], v[240:241] op_sel_hi:[1,0]\n"
    "v_pk_mul_f32 v[8:9], v[8:9], v[240:241] op_sel_hi:[1,0]\n"
    "v_pk_mul_f32 v[10:11], v[10:11], v[240:241] op_sel_hi:[1,0]\n"
    "v_cvt_pk_bf16_f32 v4, v4, v5\n"
    "v_cvt_pk_bf16_f32 v5, v6, v7\n"
    "v_cvt_pk_bf16_f32 v6, v8, v9\n"
    "v_cvt_pk_bf16_f32 v7, v10, v11\n"
    "v_accvgpr_read_b32 v8, a16\n"
    "v_accvgpr_read_b32 v9, a17\n"
    "v_accvgpr_read_b32 v10, a18\n"
    "v_accvgpr_read_b32 v11, a19\n"
    "v_accvgpr_read_b32 v12, a20\n"
    "v_accvgpr_read_b32 v13, a21\n"
    "v_accvgpr_read_b32 v14, a22\n"
    "v_accvgpr_read_b32 v15, a23\n"
    "v_pk_mul_f32 v[8:9], v[8:9], v[240:241] op_sel_hi:[1,0]\n"
    "v_pk_mul_f32 v[10:11], v[10:11], v[240:241] op_sel_hi:[1,0]\n"
    "v_pk_mul_f32 v[12:13], v[12:13], v[240:241] op_sel_hi:[1,0]\n"
    "v_pk_mul_f32 v[14:15], v[14:15], v[240:241] op_sel_hi:[1,0]\n"
    "v_cvt_pk_bf16_f32 v8, v8, v9\n"
    "v_cvt_pk_bf16_f32 v9, v10, v11\n"
    "v_cvt_pk_bf16_f32 v10, v12, v13\n"
    "v_cvt_pk_bf16_f32 v11, v14, v15\n"
    "v_accvgpr_read_b32 v12, a24\n"
    "v_accvgpr_read_b32 v13, a25\n"
    "v_accvgpr_read_b32 v14, a26\n"
    "v_accvgpr_read_b32 v15, a27\n"
    "v_accvgpr_read_b32 v16, a28\n"
    "v_accvgpr_read_b32 v17, a29\n"
    "v_accvgpr_read_b32 v18, a30\n"
    "v_accvgpr_read_b32 v19, a31\n"
    "v_pk_mul_f32 v[12:13], v[12:13], v[240:241] op_sel_hi:[1,0]\n"
    "v_pk_mul_f32 v[14:15], v[14:15], v[240:241] op_sel_hi:[1,0]\n"
    "v_pk_mul_f32 v[16:17], v[16:17], v[240:241] op_sel_hi:[1,0]\n"
    "v_pk_mul_f32 v[18:19], v[18:19], v[240:241] op_sel_hi:[1,0]\n"
    "v_cvt_pk_bf16_f32 v12, v12, v13\n"
    "v_cvt_pk_bf16_f32 v13, v14, v15\n"
    "v_cvt_pk_bf16_f32 v14, v16, v17\n"
    "v_cvt_pk_bf16_f32 v15, v18, v19\n"
    "v_accvgpr_read_b32 v16, a32\n"
    "v_accvgpr_read_b32 v17, a33\n"
    "v_accvgpr_read_b32 v18, a34\n"
    "v_accvgpr_read_b32 v19, a35\n"
    "v_accvgpr_read_b32 v20, a36\n"
    "v_accvgpr_read_b32 v21, a37\n"
    "v_accvgpr_read_b32 v22, a38\n"
    "v_accvgpr_read_b32 v23, a39\n"
    "v_pk_mul_f32 v[16:17], v[16:17], v[240:241] op_sel:[0,1] op_sel_hi:[1,1]\n"
    "v_pk_mul_f32 v[18:19], v[18:19], v[240:241] op_sel:[0,1] op_sel_hi:[1,1]\n"
    "v_pk_mul_f32 v[20:21], v[20:21], v[240:241] op_sel:[0,1] op_sel_hi:[1,1]\n"
    "v_pk_mul_f32 v[22:23], v[22:23], v[240:241] op_sel:[0,1] op_sel_hi:[1,1]\n"
    "v_cvt_pk_bf16_f32 v16, v16, v17\n"
    "v_cvt_pk_bf16_f32 v17, v18, v19\n"
    "v_cvt_pk_bf16_f32 v18, v20, v21\n"
    "v_cvt_pk_bf16_f32 v19, v22, v23\n"
    "v_accvgpr_read_b32 v20, a40\n"
    "v_accvgpr_read_b32 v21, a41\n"
    "v_accvgpr_read_b32 v22, a42\n"
    "v_accvgpr_read_b32 v23, a43\n"
    "v_accvgpr_read_b32 v24, a44\n"
    "v_accvgpr_read_b32 v25, a45\n"
    "v_accvgpr_read_b32 v26, a46\n"
    "v_accvgpr_read_b32 v27, a47\n"
    "v_pk_mul_f32 v[20:21], v[20:21], v[240:241] op_sel:[0,1] op_sel_hi:[1,1]\n"
    "v_pk_mul_f32 v[22:23], v[22:23], v[240:241] op_sel:[0,1] op_sel_hi:[1,1]\n"
    "v_pk_mul_f32 v[24:25], v[24:25], v[240:241] op_sel:[0,1] op_sel_hi:[1,1]\n"
    "v_pk_mul_f32 v[26:27], v[26:27], v[240:241] op_sel:[0,1] op_sel_hi:[1,1]\n"
    "v_cvt_pk_bf16_f32 v20, v20, v21\n"
    "v_cvt_pk_bf16_f32 v21, v22, v23\n"
    "v_cvt_pk_bf16_f32 v22, v24, v25\n"
    "v_cvt_pk_bf16_f32 v23, v26, v27\n"
    "v_accvgpr_read_b32 v24, a48\n"
    "v_accvgpr_read_b32 v25, a49\n"
    "v_accvgpr_read_b32 v26, a50\n"
    "v_accvgpr_read_b32 v27, a51\n"
    "v_accvgpr_read_b32 v28, a52\n"
    "v_accvgpr_read_b32 v29, a53\n"
    "v_accvgpr_read_b32 v30, a54\n"
    "v_accvgpr_read_b32 v31, a55\n"
    "v_pk_mul_f32 v[24:25], v[24:25], v[240:241] op_sel:[0,1] op_sel_hi:[1,1]\n"
    "v_pk_mul_f32 v[26:27], v[26:27], v[240:241] op_sel:[0,1] op_sel_hi:[1,1]\n"
    "v_pk_mul_f32 v[28:29], v[28:29], v[240:241] op_sel:[0,1] op_sel_hi:[1,1]\n"
    "v_pk_mul_f32 v[30:31], v[30:31], v[240:241] op_sel:[0,1] op_sel_hi:[1,1]\n"
    "v_cvt_pk_bf16_f32 v24, v24, v25\n"
    "v_cvt_pk_bf16_f32 v25, v26, v27\n"
    "v_cvt_pk_bf16_f32 v26, v28, v29\n"
    "v_cvt_pk_bf16_f32 v27, v30, v31\n"
    "v_accvgpr_read_b32 v28, a56\n"
    "v_accvgpr_read_b32 v29, a57\n"
    "v_accvgpr_read_b32 v30, a58\n"
    "v_accvgpr_read_b32 v31, a59\n"
    "v_pk_mul_f32 v[28:29], v[28:29], v[240:241] op_sel:[0,1] op_sel_hi:[1,1]\n"
    "v_pk_mul_f32 v[30:31], v[30:31], v[240:241] op_sel:[0,1] op_sel_hi:[1,1]\n"
    "v_cvt_pk_bf16_f32 v28, v28, v29\n"
    "v_cvt_pk_bf16_f32 v29, v30, v31\n"
    "v_accvgpr_read_b32 v30, a60\n"
    "v_accvgpr_read_b32 v31, a61\n"
    "v_accvgpr_read_b32 v240, a62\n"
    "v_mul_f32 v30, v30, v241\n"
    "v_mul_f32 v31, v31, v241\n"
    "v_mul_f32 v240, v240, v241\n"
    "v_cvt_pk_bf16_f32 v30, v30, v31\n"
    "v_accvgpr_read_b32 v31, a63\n"
    "v_mul_f32 v31, v31, v241\n"
    "v_cvt_pk_bf16_f32 v31, v240, v31\n"
    "s_waitcnt lgkmcnt(6)\n"
    "v_mfma_f32_16x16x32_bf16 v[208:211], v[128:131], v[32:35], v[248:251]\n"
    "s_waitcnt lgkmcnt(5)\n"
    "v_exp_f32 v205, v205\n"
    "v_mfma_f32_16x16x32_bf16 v[208:211], v[132:135], v[36:39], v[208:211]\n"
    "buffer_store_dwordx4 v[0:3], v255, s[48:51], s86 offen sc0 sc1 nt\n"
    "v_cvt_pk_bf16_f32 v230, v204, v205\n"
    "v_add_f32 v242, v242, v202\n"
    "v_add_f32 v243, v243, v204\n"
    "v_mfma_f32_16x16x32_bf16 v[208:211], v[136:139], v[40:43], v[208:211]\n"
    "s_waitcnt lgkmcnt(4)\n"
    "v_exp_f32 v206, v206\n"
    "v_mfma_f32_16x16x32_bf16 v[208:211], v[140:143], v[44:47], v[208:211]\n"
    "v_add_f32 v242, v242, v203\n"
    "v_add_f32 v243, v243, v205\n"
    "v_add_f32 v243, v243, v206\n"
    "v_mfma_f32_16x16x32_bf16 v[212:215], v[140:143], v[60:63], v[248:251]\n"
    "v_exp_f32 v207, v207\n"
    "v_mfma_f32_16x16x32_bf16 v[212:215], v[136:139], v[56:59], v[212:215]\n"
    "ds_read_b128 v[136:139], v252 offset:41792\n"
    "ds_read_b128 v[140:143], v252 offset:42048\n"
    "v_cvt_pk_bf16_f32 v231, v206, v207\n"
    "v_mfma_f32_16x16x32_bf16 v[212:215], v[132:135], v[52:55], v[212:215]\n"
    "v_exp_f32 v208, v208\n"
    "v_mfma_f32_16x16x32_bf16 v[212:215], v[128:131], v[48:51], v[212:215]\n"
    "ds_read_b128 v[128:131], v252 offset:41280\n"
    "ds_read_b128 v[132:135], v252 offset:41536\n"
    "v_add_f32 v243, v243, v207\n"
    "v_mfma_f32_16x16x32_bf16 a[64:67], v[160:163], v[224:227], a[64:67]\n"
    "v_exp_f32 v209, v209\n"
    "v_mfma_f32_16x16x32_bf16 a[96:99], v[160:163], v[228:231], a[96:99]\n"
    "ds_read_b64_tr_b16 v[160:161], v253 offset:0\n"
    "ds_read_b64_tr_b16 v[162:163], v253 offset:4128\n"
    "v_cvt_pk_bf16_f32 v232, v208, v209\n"
    "v_mfma_f32_16x16x32_bf16 a[100:103], v[164:167], v[228:231], a[100:103]\n"
    "v_exp_f32 v210, v210\n"
    "v_mfma_f32_16x16x32_bf16 a[68:71], v[164:167], v[224:227], a[68:71]\n"
    "ds_read_b64_tr_b16 v[164:165], v253 offset:8\n"
    "ds_read_b64_tr_b16 v[166:167], v253 offset:4136\n"
    "v_add_f32 v242, v242, v208\n"
    "v_mfma_f32_16x16x32_bf16 a[72:75], v[168:171], v[224:227], a[72:75]\n"
    "v_exp_f32 v211, v211\n"
    "v_mfma_f32_16x16x32_bf16 a[104:107], v[168:171], v[228:231], a[104:107]\n"
    "buffer_store_dwordx4 v[4:7], v255, s[48:51], s86 offen offset:64 sc0 sc1 nt\n"
    "ds_read_b64_tr_b16 v[168:169], v253 offset:256\n"
    "ds_read_b64_tr_b16 v[170:171], v253 offset:4384\n"
    "v_cvt_pk_bf16_f32 v233, v210, v211\n"
    "v_mfma_f32_16x16x32_bf16 a[108:111], v[172:175], v[228:231], a[108:111]\n"
    "v_exp_f32 v212, v212\n"
    "v_mfma_f32_16x16x32_bf16 a[76:79], v[172:175], v[224:227], a[76:79]\n"
    "s_waitcnt lgkmcnt(12)\n"
    "ds_read_b64_tr_b16 v[172:173], v253 offset:264\n"
    "ds_read_b64_tr_b16 v[174:175], v253 offset:4392\n"
    "v_add_f32 v242, v242, v209\n"
    "v_mfma_f32_16x16x32_bf16 v[216:219], v[144:147], v[32:35], v[248:251]\n"
    "s_waitcnt lgkmcnt(13)\n"
    "v_exp_f32 v213, v213\n"
    "v_mfma_f32_16x16x32_bf16 v[216:219], v[148:151], v[36:39], v[216:219]\n"
    "v_cvt_pk_bf16_f32 v236, v212, v213\n"
    "v_add_f32 v242, v242, v210\n"
    "v_add_f32 v243, v243, v212\n"
    "v_mfma_f32_16x16x32_bf16 v[216:219], v[152:155], v[40:43], v[216:219]\n"
    "s_waitcnt lgkmcnt(12)\n"
    "v_exp_f32 v214, v214\n"
    "v_mfma_f32_16x16x32_bf16 v[216:219], v[156:159], v[44:47], v[216:219]\n"
    "v_add_f32 v242, v242, v211\n"
    "v_add_f32 v243, v243, v213\n"
    "v_add_f32 v243, v243, v214\n"
    "v_mfma_f32_16x16x32_bf16 v[220:223], v[156:159], v[60:63], v[248:251]\n"
    "v_exp_f32 v215, v215\n"
    "v_mfma_f32_16x16x32_bf16 v[220:223], v[152:155], v[56:59], v[220:223]\n"
    "ds_read_b128 v[152:155], v252 offset:45920\n"
    "ds_read_b128 v[156:159], v252 offset:46176\n"
    "v_cvt_pk_bf16_f32 v237, v214, v215\n"
    "v_mfma_f32_16x16x32_bf16 v[220:223], v[148:151], v[52:55], v[220:223]\n"
    "v_exp_f32 v216, v216\n"
    "v_mfma_f32_16x16x32_bf16 v[220:223], v[144:147], v[48:51], v[220:223]\n"
    "ds_read_b128 v[144:147], v252 offset:45408\n"
    "ds_read_b128 v[148:151], v252 offset:45664\n"
    "v_add_f32 v243, v243, v215\n"
    "v_mfma_f32_16x16x32_bf16 a[80:83], v[176:179], v[224:227], a[80:83]\n"
    "v_exp_f32 v217, v217\n"
    "v_mfma_f32_16x16x32_bf16 a[112:115], v[176:179], v[228:231], a[112:115]\n"
    "buffer_store_dwordx4 v[8:11], v255, s[48:51], s86 offen offset:128 sc0 sc1 nt\n"
    "ds_read_b64_tr_b16 v[176:177], v253 offset:512\n"
    "ds_read_b64_tr_b16 v[178:179], v253 offset:4640\n"
    "v_cvt_pk_bf16_f32 v234, v216, v217\n"
    "v_mfma_f32_16x16x32_bf16 a[116:119], v[180:183], v[228:231], a[116:119]\n"
    "v_exp_f32 v218, v218\n"
    "v_mfma_f32_16x16x32_bf16 a[84:87], v[180:183], v[224:227], a[84:87]\n"
    "ds_read_b64_tr_b16 v[180:181], v253 offset:520\n"
    "ds_read_b64_tr_b16 v[182:183], v253 offset:4648\n"
    "v_add_f32 v242, v242, v216\n"
    "v_mfma_f32_16x16x32_bf16 a[88:91], v[184:187], v[224:227], a[88:91]\n"
    "v_exp_f32 v219, v219\n"
    "v_mfma_f32_16x16x32_bf16 a[120:123], v[184:187], v[228:231], a[120:123]\n"
    "ds_read_b64_tr_b16 v[184:185], v253 offset:768\n"
    "ds_read_b64_tr_b16 v[186:187], v253 offset:4896\n"
    "v_cvt_pk_bf16_f32 v235, v218, v219\n"
    "v_mfma_f32_16x16x32_bf16 a[124:127], v[188:191], v[228:231], a[124:127]\n"
    "v_exp_f32 v220, v220\n"
    "v_mfma_f32_16x16x32_bf16 a[92:95], v[188:191], v[224:227], a[92:95]\n"
    "s_waitcnt lgkmcnt(15)\n"
    "ds_read_b64_tr_b16 v[188:189], v253 offset:776\n"
    "ds_read_b64_tr_b16 v[190:191], v253 offset:4904\n"
    "v_add_f32 v242, v242, v217\n"
    "v_mfma_f32_16x16x32_bf16 v[192:195], v[128:131], v[32:35], v[248:251]\n"
    "v_exp_f32 v221, v221\n"
    "v_mfma_f32_16x16x32_bf16 v[192:195], v[132:135], v[36:39], v[192:195]\n"
    "v_cvt_pk_bf16_f32 v238, v220, v221\n"
    "v_add_f32 v242, v242, v218\n"
    "v_mfma_f32_16x16x32_bf16 v[192:195], v[136:139], v[40:43], v[192:195]\n"
    "v_exp_f32 v222, v222\n"
    "v_mfma_f32_16x16x32_bf16 v[192:195], v[140:143], v[44:47], v[192:195]\n"
    "v_add_f32 v242, v242, v219\n"
    "v_add_f32 v243, v243, v220\n"
    "v_mfma_f32_16x16x32_bf16 v[196:199], v[140:143], v[60:63], v[248:251]\n"
    "v_exp_f32 v223, v223\n"
    "v_mfma_f32_16x16x32_bf16 v[196:199], v[136:139], v[56:59], v[196:199]\n"
    "buffer_store_dwordx4 v[12:15], v255, s[48:51], s86 offen offset:192 sc0 sc1 nt\n"
    "v_cvt_pk_bf16_f32 v239, v222, v223\n"
    "v_add_f32 v243, v243, v221\n"
    "v_mfma_f32_16x16x32_bf16 v[196:199], v[132:135], v[52:55], v[196:199]\n"
    "v_exp_f32 v192, v192\n"
    "v_mfma_f32_16x16x32_bf16 v[196:199], v[128:131], v[48:51], v[196:199]\n"
    "v_add_f32 v243, v243, v222\n"
    "v_add_f32 v242, v242, v192\n"
    "v_mfma_f32_16x16x32_bf16 a[64:67], v[160:163], v[232:235], a[64:67]\n"
    "v_exp_f32 v193, v193\n"
    "v_mfma_f32_16x16x32_bf16 a[96:99], v[160:163], v[236:239], a[96:99]\n"
    "s_waitcnt lgkmcnt(15)\n"
    "ds_read_b64_tr_b16 v[160:161], v253 offset:41280\n"
    "ds_read_b64_tr_b16 v[162:163], v253 offset:45408\n"
    "v_cvt_pk_bf16_f32 v224, v192, v193\n"
    "v_mfma_f32_16x16x32_bf16 a[100:103], v[164:167], v[236:239], a[100:103]\n"
    "v_exp_f32 v194, v194\n"
    "v_mfma_f32_16x16x32_bf16 a[68:71], v[164:167], v[232:235], a[68:71]\n"
    "s_waitcnt lgkmcnt(15)\n"
    "ds_read_b64_tr_b16 v[164:165], v253 offset:41288\n"
    "ds_read_b64_tr_b16 v[166:167], v253 offset:45416\n"
    "v_add_f32 v243, v243, v223\n"
    "v_mfma_f32_16x16x32_bf16 a[72:75], v[168:171], v[232:235], a[72:75]\n"
    "v_exp_f32 v195, v195\n"
    "v_mfma_f32_16x16x32_bf16 a[104:107], v[168:171], v[236:239], a[104:107]\n"
    "s_waitcnt lgkmcnt(15)\n"
    "ds_read_b64_tr_b16 v[168:169], v253 offset:41536\n"
    "ds_read_b64_tr_b16 v[170:171], v253 offset:45664\n"
    "v_cvt_pk_bf16_f32 v225, v194, v195\n"
    "v_mfma_f32_16x16x32_bf16 a[108:111], v[172:175], v[236:239], a[108:111]\n"
    "v_exp_f32 v196, v196\n"
    "v_mfma_f32_16x16x32_bf16 a[76:79], v[172:175], v[232:235], a[76:79]\n"
    "s_waitcnt lgkmcnt(15)\n"
    "ds_read_b64_tr_b16 v[172:173], v253 offset:41544\n"
    "ds_read_b64_tr_b16 v[174:175], v253 offset:45672\n"
    "v_add_f32 v242, v242, v193\n"
    "v_mfma_f32_16x16x32_bf16 v[200:203], v[144:147], v[32:35], v[248:251]\n"
    "s_waitcnt lgkmcnt(15)\n"
    "v_exp_f32 v197, v197\n"
    "v_mfma_f32_16x16x32_bf16 v[200:203], v[148:151], v[36:39], v[200:203]\n"
    "buffer_store_dwordx4 v[16:19], v255, s[48:51], s87 offen sc0 sc1 nt\n"
    "v_cvt_pk_bf16_f32 v228, v196, v197\n"
    "v_add_f32 v242, v242, v194\n"
    "v_mfma_f32_16x16x32_bf16 v[200:203], v[152:155], v[40:43], v[200:203]\n"
    "v_exp_f32 v198, v198\n"
    "v_mfma_f32_16x16x32_bf16 v[200:203], v[156:159], v[44:47], v[200:203]\n"
    "v_add_f32 v242, v242, v195\n"
    "v_add_f32 v243, v243, v196\n"
    "v_mfma_f32_16x16x32_bf16 v[204:207], v[156:159], v[60:63], v[248:251]\n"
    "v_exp_f32 v199, v199\n"
    "v_mfma_f32_16x16x32_bf16 v[204:207], v[152:155], v[56:59], v[204:207]\n"
    "v_cvt_pk_bf16_f32 v229, v198, v199\n"
    "v_add_f32 v243, v243, v197\n"
    "v_mfma_f32_16x16x32_bf16 v[204:207], v[148:151], v[52:55], v[204:207]\n"
    "v_exp_f32 v200, v200\n"
    "v_mfma_f32_16x16x32_bf16 v[204:207], v[144:147], v[48:51], v[204:207]\n"
    "s_waitcnt lgkmcnt(14)\n"
    "v_add_f32 v243, v243, v198\n"
    "v_add_f32 v242, v242, v200\n"
    "v_mfma_f32_16x16x32_bf16 a[80:83], v[176:179], v[232:235], a[80:83]\n"
    "v_exp_f32 v201, v201\n"
    "v_mfma_f32_16x16x32_bf16 a[112:115], v[176:179], v[236:239], a[112:115]\n"
    "s_waitcnt lgkmcnt(12)\n"
    "ds_read_b64_tr_b16 v[176:177], v253 offset:41792\n"
    "v_cvt_pk_bf16_f32 v226, v200, v201\n"
    "v_mfma_f32_16x16x32_bf16 a[116:119], v[180:183], v[236:239], a[116:119]\n"
    "v_exp_f32 v202, v202\n"
    "v_mfma_f32_16x16x32_bf16 a[84:87], v[180:183], v[232:235], a[84:87]\n"
    "s_waitcnt lgkmcnt(11)\n"
    "ds_read_b64_tr_b16 v[178:179], v253 offset:45920\n"
    "ds_read_b64_tr_b16 v[180:181], v253 offset:41800\n"
    "v_add_f32 v243, v243, v199\n"
    "v_mfma_f32_16x16x32_bf16 a[88:91], v[184:187], v[232:235], a[88:91]\n"
    "v_exp_f32 v203, v203\n"
    "v_mfma_f32_16x16x32_bf16 a[120:123], v[184:187], v[236:239], a[120:123]\n"
    "s_waitcnt lgkmcnt(11)\n"
    "buffer_store_dwordx4 v[20:23], v255, s[48:51], s87 offen offset:64 sc0 sc1 nt\n"
    "ds_read_b64_tr_b16 v[182:183], v253 offset:45928\n"
    "v_cvt_pk_bf16_f32 v227, v202, v203\n"
    "v_mfma_f32_16x16x32_bf16 a[124:127], v[188:191], v[236:239], a[124:127]\n"
    "v_exp_f32 v204, v204\n"
    "v_mfma_f32_16x16x32_bf16 a[92:95], v[188:191], v[232:235], a[92:95]\n"
    "ds_read_b64_tr_b16 v[184:185], v253 offset:42048\n"
    "ds_read_b64_tr_b16 v[186:187], v253 offset:46176\n"
    "v_add_f32 v242, v242, v201\n"
    "v_mfma_f32_16x16x32_bf16 v[208:211], v[128:131], v[64:67], v[248:251]\n"
    "v_exp_f32 v205, v205\n"
    "v_mfma_f32_16x16x32_bf16 v[208:211], v[132:135], v[68:71], v[208:211]\n"
    "ds_read_b64_tr_b16 v[188:189], v253 offset:42056\n"
    "v_cvt_pk_bf16_f32 v230, v204, v205\n"
    "v_mfma_f32_16x16x32_bf16 v[208:211], v[136:139], v[72:75], v[208:211]\n"
    "v_exp_f32 v206, v206\n"
    "v_mfma_f32_16x16x32_bf16 v[208:211], v[140:143], v[76:79], v[208:211]\n"
    "ds_read_b64_tr_b16 v[190:191], v253 offset:46184\n"
    "v_add_f32 v242, v242, v202\n"
    "v_mfma_f32_16x16x32_bf16 v[212:215], v[140:143], v[92:95], v[248:251]\n"
    "v_exp_f32 v207, v207\n"
    "v_mfma_f32_16x16x32_bf16 v[212:215], v[136:139], v[88:91], v[212:215]\n"
    "ds_read_b128 v[136:139], v252 offset:512\n"
    "v_cvt_pk_bf16_f32 v231, v206, v207\n"
    "v_mfma_f32_16x16x32_bf16 v[212:215], v[132:135], v[84:87], v[212:215]\n"
    "v_exp_f32 v208, v208\n"
    "v_mfma_f32_16x16x32_bf16 v[212:215], v[128:131], v[80:83], v[212:215]\n"
    "s_waitcnt lgkmcnt(15)\n"
    "ds_read_b128 v[128:131], v252 offset:0\n"
    "ds_read_b128 v[132:135], v252 offset:256\n"
    "v_add_f32 v242, v242, v203\n"
    "v_mfma_f32_16x16x32_bf16 a[64:67], v[160:163], v[224:227], a[64:67]\n"
    "v_exp_f32 v209, v209\n"
    "v_mfma_f32_16x16x32_bf16 a[96:99], v[160:163], v[228:231], a[96:99]\n"
    "s_waitcnt lgkmcnt(15)\n"
    "buffer_store_dwordx4 v[24:27], v255, s[48:51], s87 offen offset:128 sc0 sc1 nt\n"
    "ds_read_b128 v[140:143], v252 offset:768\n"
    "v_add_f32 v243, v243, v204\n"
    "v_mfma_f32_16x16x32_bf16 a[100:103], v[164:167], v[228:231], a[100:103]\n"
    "v_exp_f32 v210, v210\n"
    "v_mfma_f32_16x16x32_bf16 a[68:71], v[164:167], v[224:227], a[68:71]\n"
    "s_waitcnt lgkmcnt(14)\n"
    "v_add_f32 v243, v243, v205\n"
    "v_cvt_pk_bf16_f32 v232, v208, v209\n"
    "v_mfma_f32_16x16x32_bf16 a[72:75], v[168:171], v[224:227], a[72:75]\n"
    "v_exp_f32 v211, v211\n"
    "v_mfma_f32_16x16x32_bf16 a[104:107], v[168:171], v[228:231], a[104:107]\n"
    "s_waitcnt lgkmcnt(12)\n"
    "v_add_f32 v243, v243, v206\n"
    "v_cvt_pk_bf16_f32 v233, v210, v211\n"
    "v_mfma_f32_16x16x32_bf16 a[108:111], v[172:175], v[228:231], a[108:111]\n"
    "v_exp_f32 v212, v212\n"
    "v_mfma_f32_16x16x32_bf16 a[76:79], v[172:175], v[224:227], a[76:79]\n"
    "v_add_f32 v243, v243, v207\n"
    "v_add_f32 v244, v244, v208\n"
    "v_mfma_f32_16x16x32_bf16 v[216:219], v[144:147], v[64:67], v[248:251]\n"
    "v_exp_f32 v213, v213\n"
    "v_mfma_f32_16x16x32_bf16 v[216:219], v[148:151], v[68:71], v[216:219]\n"
    "v_cvt_pk_bf16_f32 v236, v212, v213\n"
    "v_add_f32 v244, v244, v209\n"
    "v_mfma_f32_16x16x32_bf16 v[216:219], v[152:155], v[72:75], v[216:219]\n"
    "v_exp_f32 v214, v214\n"
    "v_mfma_f32_16x16x32_bf16 v[216:219], v[156:159], v[76:79], v[216:219]\n"
    "v_add_f32 v244, v244, v210\n"
    "v_add_f32 v245, v245, v212\n"
    "v_mfma_f32_16x16x32_bf16 v[220:223], v[156:159], v[92:95], v[248:251]\n"
    "v_exp_f32 v215, v215\n"
    "v_mfma_f32_16x16x32_bf16 v[220:223], v[152:155], v[88:91], v[220:223]\n"
    "buffer_store_dwordx4 v[28:31], v255, s[48:51], s87 offen offset:192 sc0 sc1 nt\n"
    "ds_read_b128 v[152:155], v252 offset:4640\n"
    "v_cvt_pk_bf16_f32 v237, v214, v215\n"
    "v_mfma_f32_16x16x32_bf16 v[220:223], v[148:151], v[84:87], v[220:223]\n"
    "v_exp_f32 v216, v216\n"
    "v_mfma_f32_16x16x32_bf16 v[220:223], v[144:147], v[80:83], v[220:223]\n"
    "s_waitcnt lgkmcnt(11)\n"
    "ds_read_b128 v[144:147], v252 offset:4128\n"
    "ds_read_b128 v[148:151], v252 offset:4384\n"
    "v_add_f32 v244, v244, v211\n"
    "v_mfma_f32_16x16x32_bf16 a[80:83], v[176:179], v[224:227], a[80:83]\n"
    "v_exp_f32 v217, v217\n"
    "v_mfma_f32_16x16x32_bf16 a[112:115], v[176:179], v[228:231], a[112:115]\n"
    "s_waitcnt lgkmcnt(11)\n"
    "ds_read_b128 v[156:159], v252 offset:4896\n"
    "v_cvt_pk_bf16_f32 v234, v216, v217\n"
    "v_mfma_f32_16x16x32_bf16 a[116:119], v[180:183], v[228:231], a[116:119]\n"
    "v_exp_f32 v218, v218\n"
    "v_mfma_f32_16x16x32_bf16 a[84:87], v[180:183], v[224:227], a[84:87]\n"
    "s_waitcnt lgkmcnt(10)\n"
    "v_add_f32 v245, v245, v213\n"
    "v_add_f32 v244, v244, v216\n"
    "v_mfma_f32_16x16x32_bf16 a[88:91], v[184:187], v[224:227], a[88:91]\n"
    "v_exp_f32 v219, v219\n"
    "v_mfma_f32_16x16x32_bf16 a[120:123], v[184:187], v[228:231], a[120:123]\n"
    "s_waitcnt lgkmcnt(8)\n"
    "v_cvt_pk_bf16_f32 v235, v218, v219\n"
    "v_add_f32 v245, v245, v214\n"
    "v_mfma_f32_16x16x32_bf16 a[124:127], v[188:191], v[228:231], a[124:127]\n"
    "v_exp_f32 v220, v220\n"
    "v_mfma_f32_16x16x32_bf16 a[92:95], v[188:191], v[224:227], a[92:95]\n"
    "v_add_f32 v245, v245, v215\n"
    "v_add_f32 v244, v244, v217\n"
    "v_permlane16_swap_b32 v242, v243\n"
    "v_accvgpr_read_b32 v32, a64\n"
    "v_add_f32 v242, v242, v243\n"
    "v_mov_b32 v243, v242\n"
    "v_accvgpr_read_b32 v33, a65\n"
    "v_accvgpr_read_b32 v34, a66\n"
    "v_permlane32_swap_b32 v242, v243\n"
    "v_accvgpr_read_b32 v35, a67\n"
    "v_add_f32 v242, v242, v243\n"
    "v_cmp_nlt_f32 vcc, s62, v242\n"
    "v_cmp_ngt_f32 s[66:67], s63, v242\n"
    "v_rcp_f32 v242, v242\n"
    "v_accvgpr_read_b32 v36, a68\n"
    "s_or_b64 s[66:67], s[66:67], vcc\n"
    "v_mov_b32 v243, v242\n"
    "v_accvgpr_read_b32 v37, a69\n"
    "v_accvgpr_read_b32 v38, a70\n"
    "s_or_b64 s[60:61], s[60:61], s[66:67]\n"
    "v_permlane16_swap_b32 v242, v243\n"
    "v_accvgpr_read_b32 v39, a71\n"
    "v_pk_mul_f32 v[32:33], v[32:33], v[242:243] op_sel_hi:[1,0]\n"
    "v_pk_mul_f32 v[34:35], v[34:35], v[242:243] op_sel_hi:[1,0]\n"
    "v_pk_mul_f32 v[36:37], v[36:37], v[242:243] op_sel_hi:[1,0]\n"
    "v_pk_mul_f32 v[38:39], v[38:39], v[242:243] op_sel_hi:[1,0]\n"
    "v_cvt_pk_bf16_f32 v32, v32, v33\n"
    "v_cvt_pk_bf16_f32 v33, v34, v35\n"
    "v_cvt_pk_bf16_f32 v34, v36, v37\n"
    "v_cvt_pk_bf16_f32 v35, v38, v39\n"
    "v_accvgpr_read_b32 v36, a72\n"
    "v_accvgpr_read_b32 v37, a73\n"
    "v_accvgpr_read_b32 v38, a74\n"
    "v_accvgpr_read_b32 v39, a75\n"
    "v_accvgpr_read_b32 v40, a76\n"
    "v_accvgpr_read_b32 v41, a77\n"
    "v_accvgpr_read_b32 v42, a78\n"
    "v_accvgpr_read_b32 v43, a79\n"
    "v_pk_mul_f32 v[36:37], v[36:37], v[242:243] op_sel_hi:[1,0]\n"
    "v_pk_mul_f32 v[38:39], v[38:39], v[242:243] op_sel_hi:[1,0]\n"
    "v_pk_mul_f32 v[40:41], v[40:41], v[242:243] op_sel_hi:[1,0]\n"
    "v_pk_mul_f32 v[42:43], v[42:43], v[242:243] op_sel_hi:[1,0]\n"
    "v_cvt_pk_bf16_f32 v36, v36, v37\n"
    "v_cvt_pk_bf16_f32 v37, v38, v39\n"
    "v_cvt_pk_bf16_f32 v38, v40, v41\n"
    "v_cvt_pk_bf16_f32 v39, v42, v43\n"
    "v_accvgpr_read_b32 v40, a80\n"
    "v_accvgpr_read_b32 v41, a81\n"
    "v_accvgpr_read_b32 v42, a82\n"
    "v_accvgpr_read_b32 v43, a83\n"
    "v_accvgpr_read_b32 v44, a84\n"
    "v_accvgpr_read_b32 v45, a85\n"
    "v_accvgpr_read_b32 v46, a86\n"
    "v_accvgpr_read_b32 v47, a87\n"
    "v_pk_mul_f32 v[40:41], v[40:41], v[242:243] op_sel_hi:[1,0]\n"
    "v_pk_mul_f32 v[42:43], v[42:43], v[242:243] op_sel_hi:[1,0]\n"
    "v_pk_mul_f32 v[44:45], v[44:45], v[242:243] op_sel_hi:[1,0]\n"
    "v_pk_mul_f32 v[46:47], v[46:47], v[242:243] op_sel_hi:[1,0]\n"
    "v_cvt_pk_bf16_f32 v40, v40, v41\n"
    "v_cvt_pk_bf16_f32 v41, v42, v43\n"
    "v_cvt_pk_bf16_f32 v42, v44, v45\n"
    "v_cvt_pk_bf16_f32 v43, v46, v47\n"
    "v_accvgpr_read_b32 v44, a88\n"
    "v_accvgpr_read_b32 v45, a89\n"
    "v_accvgpr_read_b32 v46, a90\n"
    "v_accvgpr_read_b32 v47, a91\n"
    "v_accvgpr_read_b32 v48, a92\n"
    "v_accvgpr_read_b32 v49, a93\n"
    "v_accvgpr_read_b32 v50, a94\n"
    "v_accvgpr_read_b32 v51, a95\n"
    "v_pk_mul_f32 v[44:45], v[44:45], v[242:243] op_sel_hi:[1,0]\n"
    "v_pk_mul_f32 v[46:47], v[46:47], v[242:243] op_sel_hi:[1,0]\n"
    "v_pk_mul_f32 v[48:49], v[48:49], v[242:243] op_sel_hi:[1,0]\n"
    "v_pk_mul_f32 v[50:51], v[50:51], v[242:243] op_sel_hi:[1,0]\n"
    "v_cvt_pk_bf16_f32 v44, v44, v45\n"
    "v_cvt_pk_bf16_f32 v45, v46, v47\n"
    "v_cvt_pk_bf16_f32 v46, v48, v49\n"
    "v_cvt_pk_bf16_f32 v47, v50, v51\n"
    "v_accvgpr_read_b32 v48, a96\n"
    "v_accvgpr_read_b32 v49, a97\n"
    "v_accvgpr_read_b32 v50, a98\n"
    "v_accvgpr_read_b32 v51, a99\n"
    "v_accvgpr_read_b32 v52, a100\n"
    "v_accvgpr_read_b32 v53, a101\n"
    "v_accvgpr_read_b32 v54, a102\n"
    "v_accvgpr_read_b32 v55, a103\n"
    "v_pk_mul_f32 v[48:49], v[48:49], v[242:243] op_sel:[0,1] op_sel_hi:[1,1]\n"
    "v_pk_mul_f32 v[50:51], v[50:51], v[242:243] op_sel:[0,1] op_sel_hi:[1,1]\n"
    "v_pk_mul_f32 v[52:53], v[52:53], v[242:243] op_sel:[0,1] op_sel_hi:[1,1]\n"
    "v_pk_mul_f32 v[54:55], v[54:55], v[242:243] op_sel:[0,1] op_sel_hi:[1,1]\n"
    "v_cvt_pk_bf16_f32 v48, v48, v49\n"
    "v_cvt_pk_bf16_f32 v49, v50, v51\n"
    "v_cvt_pk_bf16_f32 v50, v52, v53\n"
    "v_cvt_pk_bf16_f32 v51, v54, v55\n"
    "v_accvgpr_read_b32 v52, a104\n"
    "v_accvgpr_read_b32 v53, a105\n"
    "v_accvgpr_read_b32 v54, a106\n"
    "v_accvgpr_read_b32 v55, a107\n"
    "v_accvgpr_read_b32 v56, a108\n"
    "v_accvgpr_read_b32 v57, a109\n"
    "v_accvgpr_read_b32 v58, a110\n"
    "v_accvgpr_read_b32 v59, a111\n"
    "v_pk_mul_f32 v[52:53], v[52:53], v[242:243] op_sel:[0,1] op_sel_hi:[1,1]\n"
    "v_pk_mul_f32 v[54:55], v[54:55], v[242:243] op_sel:[0,1] op_sel_hi:[1,1]\n"
    "v_pk_mul_f32 v[56:57], v[56:57], v[242:243] op_sel:[0,1] op_sel_hi:[1,1]\n"
    "v_pk_mul_f32 v[58:59], v[58:59], v[242:243] op_sel:[0,1] op_sel_hi:[1,1]\n"
    "v_cvt_pk_bf16_f32 v52, v52, v53\n"
    "v_cvt_pk_bf16_f32 v53, v54, v55\n"
    "v_cvt_pk_bf16_f32 v54, v56, v57\n"
    "v_cvt_pk_bf16_f32 v55, v58, v59\n"
    "v_accvgpr_read_b32 v56, a112\n"
    "v_accvgpr_read_b32 v57, a113\n"
    "v_accvgpr_read_b32 v58, a114\n"
    "v_accvgpr_read_b32 v59, a115\n"
    "v_accvgpr_read_b32 v60, a116\n"
    "v_accvgpr_read_b32 v61, a117\n"
    "v_accvgpr_read_b32 v62, a118\n"
    "v_accvgpr_read_b32 v63, a119\n"
    "v_pk_mul_f32 v[56:57], v[56:57], v[242:243] op_sel:[0,1] op_sel_hi:[1,1]\n"
    "v_pk_mul_f32 v[58:59], v[58:59], v[242:243] op_sel:[0,1] op_sel_hi:[1,1]\n"
    "v_pk_mul_f32 v[60:61], v[60:61], v[242:243] op_sel:[0,1] op_sel_hi:[1,1]\n"
    "v_pk_mul_f32 v[62:63], v[62:63], v[242:243] op_sel:[0,1] op_sel_hi:[1,1]\n"
    "v_cvt_pk_bf16_f32 v56, v56, v57\n"
    "v_cvt_pk_bf16_f32 v57, v58, v59\n"
    "v_cvt_pk_bf16_f32 v58, v60, v61\n"
    "v_cvt_pk_bf16_f32 v59, v62, v63\n"
    "v_accvgpr_read_b32 v60, a120\n"
    "v_accvgpr_read_b32 v61, a121\n"
    "v_accvgpr_read_b32 v62, a122\n"
    "v_accvgpr_read_b32 v63, a123\n"
    "v_pk_mul_f32 v[60:61], v[60:61], v[242:243] op_sel:[0,1] op_sel_hi:[1,1]\n"
    "v_pk_mul_f32 v[62:63], v[62:63], v[242:243] op_sel:[0,1] op_sel_hi:[1,1]\n"
    "v_cvt_pk_bf16_f32 v60, v60, v61\n"
    "v_cvt_pk_bf16_f32 v61, v62, v63\n"
    "v_accvgpr_read_b32 v62, a124\n"
    "v_accvgpr_read_b32 v63, a125\n"
    "v_accvgpr_read_b32 v242, a126\n"
    "v_mul_f32 v62, v62, v243\n"
    "v_mul_f32 v63, v63, v243\n"
    "v_mul_f32 v242, v242, v243\n"
    "v_cvt_pk_bf16_f32 v62, v62, v63\n"
    "v_accvgpr_read_b32 v63, a127\n"
    "v_mul_f32 v63, v63, v243\n"
    "v_cvt_pk_bf16_f32 v63, v242, v63\n"
    "s_waitcnt lgkmcnt(6)\n"
    "v_mfma_f32_16x16x32_bf16 v[192:195], v[128:131], v[64:67], v[248:251]\n"
    "s_waitcnt lgkmcnt(5)\n"
    "v_exp_f32 v221, v221\n"
    "v_mfma_f32_16x16x32_bf16 v[192:195], v[132:135], v[68:71], v[192:195]\n"
    "buffer_store_dwordx4 v[32:35], v255, s[48:51], s88 offen sc0 sc1 nt\n"
    "v_cvt_pk_bf16_f32 v238, v220, v221\n"
    "v_add_f32 v244, v244, v218\n"
    "v_add_f32 v245, v245, v220\n"
    "v_mfma_f32_16x16x32_bf16 v[192:195], v[136:139], v[72:75], v[192:195]\n"
    "s_waitcnt lgkmcnt(4)\n"
    "v_exp_f32 v222, v222\n"
    "v_mfma_f32_16x16x32_bf16 v[192:195], v[140:143], v[76:79], v[192:195]\n"
    "v_add_f32 v244, v244, v219\n"
    "v_add_f32 v245, v245, v221\n"
    "v_add_f32 v245, v245, v222\n"
    "v_mfma_f32_16x16x32_bf16 v[196:199], v[140:143], v[92:95], v[248:251]\n"
    "v_exp_f32 v223, v223\n"
    "v_mfma_f32_16x16x32_bf16 v[196:199], v[136:139], v[88:91], v[196:199]\n"
    "ds_read_b128 v[136:139], v252 offset:8768\n"
    "ds_read_b128 v[140:143], v252 offset:9024\n"
    "v_cvt_pk_bf16_f32 v239, v222, v223\n"
    "v_mfma_f32_16x16x32_bf16 v[196:199], v[132:135], v[84:87], v[196:199]\n"
    "v_exp_f32 v192, v192\n"
    "v_mfma_f32_16x16x32_bf16 v[196:199], v[128:131], v[80:83], v[196:199]\n"
    "ds_read_b128 v[128:131], v252 offset:8256\n"
    "ds_read_b128 v[132:135], v252 offset:8512\n"
    "v_add_f32 v245, v245, v223\n"
    "v_mfma_f32_16x16x32_bf16 a[128:131], v[160:163], v[232:235], a[128:131]\n"
    "v_exp_f32 v193, v193\n"
    "v_mfma_f32_16x16x32_bf16 a[160:163], v[160:163], v[236:239], a[160:163]\n"
    "ds_read_b64_tr_b16 v[160:161], v253 offset:0\n"
    "ds_read_b64_tr_b16 v[162:163], v253 offset:4128\n"
    "v_cvt_pk_bf16_f32 v224, v192, v193\n"
    "v_mfma_f32_16x16x32_bf16 a[164:167], v[164:167], v[236:239], a[164:167]\n"
    "v_exp_f32 v194, v194\n"
    "v_mfma_f32_16x16x32_bf16 a[132:135], v[164:167], v[232:235], a[132:135]\n"
    "ds_read_b64_tr_b16 v[164:165], v253 offset:8\n"
    "ds_read_b64_tr_b16 v[166:167], v253 offset:4136\n"
    "v_add_f32 v244, v244, v192\n"
    "v_mfma_f32_16x16x32_bf16 a[136:139], v[168:171], v[232:235], a[136:139]\n"
    "v_exp_f32 v195, v195\n"
    "v_mfma_f32_16x16x32_bf16 a[168:171], v[168:171], v[236:239], a[168:171]\n"
    "buffer_store_dwordx4 v[36:39], v255, s[48:51], s88 offen offset:64 sc0 sc1 nt\n"
    "ds_read_b64_tr_b16 v[168:169], v253 offset:256\n"
    "ds_read_b64_tr_b16 v[170:171], v253 offset:4384\n"
    "v_cvt_pk_bf16_f32 v225, v194, v195\n"
    "v_mfma_f32_16x16x32_bf16 a[172:175], v[172:175], v[236:239], a[172:175]\n"
    "v_exp_f32 v196, v196\n"
    "v_mfma_f32_16x16x32_bf16 a[140:143], v[172:175], v[232:235], a[140:143]\n"
    "s_waitcnt lgkmcnt(12)\n"
    "ds_read_b64_tr_b16 v[172:173], v253 offset:264\n"
    "ds_read_b64_tr_b16 v[174:175], v253 offset:4392\n"
    "v_add_f32 v244, v244, v193\n"
    "v_mfma_f32_16x16x32_bf16 v[200:203], v[144:147], v[64:67], v[248:251]\n"
    "s_waitcnt lgkmcnt(13)\n"
    "v_exp_f32 v197, v197\n"
    "v_mfma_f32_16x16x32_bf16 v[200:203], v[148:151], v[68:71], v[200:203]\n"
    "v_cvt_pk_bf16_f32 v228, v196, v197\n"
    "v_add_f32 v244, v244, v194\n"
    "v_add_f32 v245, v245, v196\n"
    "v_mfma_f32_16x16x32_bf16 v[200:203], v[152:155], v[72:75], v[200:203]\n"
    "s_waitcnt lgkmcnt(12)\n"
    "v_exp_f32 v198, v198\n"
    "v_mfma_f32_16x16x32_bf16 v[200:203], v[156:159], v[76:79], v[200:203]\n"
    "v_add_f32 v244, v244, v195\n"
    "v_add_f32 v245, v245, v197\n"
    "v_add_f32 v245, v245, v198\n"
    "v_mfma_f32_16x16x32_bf16 v[204:207], v[156:159], v[92:95], v[248:251]\n"
    "v_exp_f32 v199, v199\n"
    "v_mfma_f32_16x16x32_bf16 v[204:207], v[152:155], v[88:91], v[204:207]\n"
    "ds_read_b128 v[152:155], v252 offset:12896\n"
    "ds_read_b128 v[156:159], v252 offset:13152\n"
    "v_cvt_pk_bf16_f32 v229, v198, v199\n"
    "v_mfma_f32_16x16x32_bf16 v[204:207], v[148:151], v[84:87], v[204:207]\n"
    "v_exp_f32 v200, v200\n"
    "v_mfma_f32_16x16x32_bf16 v[204:207], v[144:147], v[80:83], v[204:207]\n"
    "ds_read_b128 v[144:147], v252 offset:12384\n"
    "ds_read_b128 v[148:151], v252 offset:12640\n"
    "v_add_f32 v245, v245, v199\n"
    "v_mfma_f32_16x16x32_bf16 a[144:147], v[176:179], v[232:235], a[144:147]\n"
    "v_exp_f32 v201, v201\n"
    "v_mfma_f32_16x16x32_bf16 a[176:179], v[176:179], v[236:239], a[176:179]\n"
    "buffer_store_dwordx4 v[40:43], v255, s[48:51], s88 offen offset:128 sc0 sc1 nt\n"
    "ds_read_b64_tr_b16 v[176:177], v253 offset:512\n"
    "ds_read_b64_tr_b16 v[178:179], v253 offset:4640\n"
    "v_cvt_pk_bf16_f32 v226, v200, v201\n"
    "v_mfma_f32_16x16x32_bf16 a[180:183], v[180:183], v[236:239], a[180:183]\n"
    "v_exp_f32 v202, v202\n"
    "v_mfma_f32_16x16x32_bf16 a[148:151], v[180:183], v[232:235], a[148:151]\n"
    "ds_read_b64_tr_b16 v[180:181], v253 offset:520\n"
    "ds_read_b64_tr_b16 v[182:183], v253 offset:4648\n"
    "v_add_f32 v244, v244, v200\n"
    "v_mfma_f32_16x16x32_bf16 a[152:155], v[184:187], v[232:235], a[152:155]\n"
    "v_exp_f32 v203, v203\n"
    "v_mfma_f32_16x16x32_bf16 a[184:187], v[184:187], v[236:239], a[184:187]\n"
    "ds_read_b64_tr_b16 v[184:185], v253 offset:768\n"
    "ds_read_b64_tr_b16 v[186:187], v253 offset:4896\n"
    "v_cvt_pk_bf16_f32 v227, v202, v203\n"
    "v_mfma_f32_16x16x32_bf16 a[188:191], v[188:191], v[236:239], a[188:191]\n"
    "v_exp_f32 v204, v204\n"
    "v_mfma_f32_16x16x32_bf16 a[156:159], v[188:191], v[232:235], a[156:159]\n"
    "s_waitcnt lgkmcnt(15)\n"
    "ds_read_b64_tr_b16 v[188:189], v253 offset:776\n"
    "ds_read_b64_tr_b16 v[190:191], v253 offset:4904\n"
    "v_add_f32 v244, v244, v201\n"
    "v_mfma_f32_16x16x32_bf16 v[208:211], v[128:131], v[64:67], v[248:251]\n"
    "v_exp_f32 v205, v205\n"
    "v_mfma_f32_16x16x32_bf16 v[208:211], v[132:135], v[68:71], v[208:211]\n"
    "v_cvt_pk_bf16_f32 v230, v204, v205\n"
    "v_add_f32 v244, v244, v202\n"
    "v_mfma_f32_16x16x32_bf16 v[208:211], v[136:139], v[72:75], v[208:211]\n"
    "v_exp_f32 v206, v206\n"
    "v_mfma_f32_16x16x32_bf16 v[208:211], v[140:143], v[76:79], v[208:211]\n"
    "v_add_f32 v244, v244, v203\n"
    "v_add_f32 v245, v245, v204\n"
    "v_mfma_f32_16x16x32_bf16 v[212:215], v[140:143], v[92:95], v[248:251]\n"
    "v_exp_f32 v207, v207\n"
    "v_mfma_f32_16x16x32_bf16 v[212:215], v[136:139], v[88:91], v[212:215]\n"
    "buffer_store_dwordx4 v[44:47], v255, s[48:51], s88 offen offset:192 sc0 sc1 nt\n"
    "v_cvt_pk_bf16_f32 v231, v206, v207\n"
    "v_add_f32 v245, v245, v205\n"
    "v_mfma_f32_16x16x32_bf16 v[212:215], v[132:135], v[84:87], v[212:215]\n"
    "v_exp_f32 v208, v208\n"
    "v_mfma_f32_16x16x32_bf16 v[212:215], v[128:131], v[80:83], v[212:215]\n"
    "v_add_f32 v245, v245, v206\n"
    "v_add_f32 v244, v244, v208\n"
    "v_mfma_f32_16x16x32_bf16 a[128:131], v[160:163], v[224:227], a[128:131]\n"
    "v_exp_f32 v209, v209\n"
    "v_mfma_f32_16x16x32_bf16 a[160:163], v[160:163], v[228:231], a[160:163]\n"
    "s_waitcnt lgkmcnt(15)\n"
    "ds_read_b64_tr_b16 v[160:161], v253 offset:8256\n"
    "ds_read_b64_tr_b16 v[162:163], v253 offset:12384\n"
    "v_cvt_pk_bf16_f32 v232, v208, v209\n"
    "v_mfma_f32_16x16x32_bf16 a[164:167], v[164:167], v[228:231], a[164:167]\n"
    "v_exp_f32 v210, v210\n"
    "v_mfma_f32_16x16x32_bf16 a[132:135], v[164:167], v[224:227], a[132:135]\n"
    "s_waitcnt lgkmcnt(15)\n"
    "ds_read_b64_tr_b16 v[164:165], v253 offset:8264\n"
    "ds_read_b64_tr_b16 v[166:167], v253 offset:12392\n"
    "v_add_f32 v245, v245, v207\n"
    "v_mfma_f32_16x16x32_bf16 a[136:139], v[168:171], v[224:227], a[136:139]\n"
    "v_exp_f32 v211, v211\n"
    "v_mfma_f32_16x16x32_bf16 a[168:171], v[168:171], v[228:231], a[168:171]\n"
    "s_waitcnt lgkmcnt(15)\n"
    "ds_read_b64_tr_b16 v[168:169], v253 offset:8512\n"
    "ds_read_b64_tr_b16 v[170:171], v253 offset:12640\n"
    "v_cvt_pk_bf16_f32 v233, v210, v211\n"
    "v_mfma_f32_16x16x32_bf16 a[172:175], v[172:175], v[228:231], a[172:175]\n"
    "v_exp_f32 v212, v212\n"
    "v_mfma_f32_16x16x32_bf16 a[140:143], v[172:175], v[224:227], a[140:143]\n"
    "s_waitcnt lgkmcnt(15)\n"
    "ds_read_b64_tr_b16 v[172:173], v253 offset:8520\n"
    "ds_read_b64_tr_b16 v[174:175], v253 offset:12648\n"
    "v_add_f32 v244, v244, v209\n"
    "v_mfma_f32_16x16x32_bf16 v[216:219], v[144:147], v[64:67], v[248:251]\n"
    "s_waitcnt lgkmcnt(15)\n"
    "v_exp_f32 v213, v213\n"
    "v_mfma_f32_16x16x32_bf16 v[216:219], v[148:151], v[68:71], v[216:219]\n"
    "buffer_store_dwordx4 v[48:51], v255, s[48:51], s89 offen sc0 sc1 nt\n"
    "v_cvt_pk_bf16_f32 v236, v212, v213\n"
    "v_add_f32 v244, v244, v210\n"
    "v_mfma_f32_16x16x32_bf16 v[216:219], v[152:155], v[72:75], v[216:219]\n"
    "v_exp_f32 v214, v214\n"
    "v_mfma_f32_16x16x32_bf16 v[216:219], v[156:159], v[76:79], v[216:219]\n"
    "v_add_f32 v244, v244, v211\n"
    "v_add_f32 v245, v245, v212\n"
    "v_mfma_f32_16x16x32_bf16 v[220:223], v[156:159], v[92:95], v[248:251]\n"
    "v_exp_f32 v215, v215\n"
    "v_mfma_f32_16x16x32_bf16 v[220:223], v[152:155], v[88:91], v[220:223]\n"
    "v_cvt_pk_bf16_f32 v237, v214, v215\n"
    "v_add_f32 v245, v245, v213\n"
    "v_mfma_f32_16x16x32_bf16 v[220:223], v[148:151], v[84:87], v[220:223]\n"
    "v_exp_f32 v216, v216\n"
    "v_mfma_f32_16x16x32_bf16 v[220:223], v[144:147], v[80:83], v[220:223]\n"
    "s_waitcnt lgkmcnt(14)\n"
    "v_add_f32 v245, v245, v214\n"
    "v_add_f32 v244, v244, v216\n"
    "v_mfma_f32_16x16x32_bf16 a[144:147], v[176:179], v[224:227], a[144:147]\n"
    "v_exp_f32 v217, v217\n"
    "v_mfma_f32_16x16x32_bf16 a[176:179], v[176:179], v[228:231], a[176:179]\n"
    "s_waitcnt lgkmcnt(12)\n"
    "ds_read_b64_tr_b16 v[176:177], v253 offset:8768\n"
    "v_cvt_pk_bf16_f32 v234, v216, v217\n"
    "v_mfma_f32_16x16x32_bf16 a[180:183], v[180:183], v[228:231], a[180:183]\n"
    "v_exp_f32 v218, v218\n"
    "v_mfma_f32_16x16x32_bf16 a[148:151], v[180:183], v[224:227], a[148:151]\n"
    "s_waitcnt lgkmcnt(11)\n"
    "ds_read_b64_tr_b16 v[178:179], v253 offset:12896\n"
    "ds_read_b64_tr_b16 v[180:181], v253 offset:8776\n"
    "v_add_f32 v245, v245, v215\n"
    "v_mfma_f32_16x16x32_bf16 a[152:155], v[184:187], v[224:227], a[152:155]\n"
    "v_exp_f32 v219, v219\n"
    "v_mfma_f32_16x16x32_bf16 a[184:187], v[184:187], v[228:231], a[184:187]\n"
    "s_waitcnt lgkmcnt(11)\n"
    "buffer_store_dwordx4 v[52:55], v255, s[48:51], s89 offen offset:64 sc0 sc1 nt\n"
    "ds_read_b64_tr_b16 v[182:183], v253 offset:12904\n"
    "v_cvt_pk_bf16_f32 v235, v218, v219\n"
    "v_mfma_f32_16x16x32_bf16 a[188:191], v[188:191], v[228:231], a[188:191]\n"
    "v_exp_f32 v220, v220\n"
    "v_mfma_f32_16x16x32_bf16 a[156:159], v[188:191], v[224:227], a[156:159]\n"
    "ds_read_b64_tr_b16 v[184:185], v253 offset:9024\n"
    "ds_read_b64_tr_b16 v[186:187], v253 offset:13152\n"
    "v_add_f32 v244, v244, v217\n"
    "v_mfma_f32_16x16x32_bf16 v[192:195], v[128:131], v[96:99], v[248:251]\n"
    "v_exp_f32 v221, v221\n"
    "v_mfma_f32_16x16x32_bf16 v[192:195], v[132:135], v[100:103], v[192:195]\n"
    "ds_read_b64_tr_b16 v[188:189], v253 offset:9032\n"
    "v_cvt_pk_bf16_f32 v238, v220, v221\n"
    "v_mfma_f32_16x16x32_bf16 v[192:195], v[136:139], v[104:107], v[192:195]\n"
    "v_exp_f32 v222, v222\n"
    "v_mfma_f32_16x16x32_bf16 v[192:195], v[140:143], v[108:111], v[192:195]\n"
    "ds_read_b64_tr_b16 v[190:191], v253 offset:13160\n"
    "v_add_f32 v244, v244, v218\n"
    "v_mfma_f32_16x16x32_bf16 v[196:199], v[140:143], v[124:127], v[248:251]\n"
    "v_exp_f32 v223, v223\n"
    "v_mfma_f32_16x16x32_bf16 v[196:199], v[136:139], v[120:123], v[196:199]\n"
    "ds_read_b128 v[136:139], v252 offset:512\n"
    "v_cvt_pk_bf16_f32 v239, v222, v223\n"
    "v_mfma_f32_16x16x32_bf16 v[196:199], v[132:135], v[116:119], v[196:199]\n"
    "v_exp_f32 v192, v192\n"
    "v_mfma_f32_16x16x32_bf16 v[196:199], v[128:131], v[112:115], v[196:199]\n"
    "s_waitcnt lgkmcnt(15)\n"
    "ds_read_b128 v[128:131], v252 offset:0\n"
    "ds_read_b128 v[132:135], v252 offset:256\n"
    "v_add_f32 v244, v244, v219\n"
    "v_mfma_f32_16x16x32_bf16 a[128:131], v[160:163], v[232:235], a[128:131]\n"
    "v_exp_f32 v193, v193\n"
    "v_mfma_f32_16x16x32_bf16 a[160:163], v[160:163], v[236:239], a[160:163]\n"
    "s_waitcnt lgkmcnt(15)\n"
    "buffer_store_dwordx4 v[56:59], v255, s[48:51], s89 offen offset:128 sc0 sc1 nt\n"
    "ds_read_b128 v[140:143], v252 offset:768\n"
    "v_add_f32 v245, v245, v220\n"
    "v_mfma_f32_16x16x32_bf16 a[164:167], v[164:167], v[236:239], a[164:167]\n"
    "v_exp_f32 v194, v194\n"
    "v_mfma_f32_16x16x32_bf16 a[132:135], v[164:167], v[232:235], a[132:135]\n"
    "s_waitcnt lgkmcnt(14)\n"
    "v_add_f32 v245, v245, v221\n"
    "v_cvt_pk_bf16_f32 v224, v192, v193\n"
    "v_mfma_f32_16x16x32_bf16 a[136:139], v[168:171], v[232:235], a[136:139]\n"
    "v_exp_f32 v195, v195\n"
    "v_mfma_f32_16x16x32_bf16 a[168:171], v[168:171], v[236:239], a[168:171]\n"
    "s_waitcnt lgkmcnt(12)\n"
    "v_add_f32 v245, v245, v222\n"
    "v_cvt_pk_bf16_f32 v225, v194, v195\n"
    "v_mfma_f32_16x16x32_bf16 a[172:175], v[172:175], v[236:239], a[172:175]\n"
    "v_exp_f32 v196, v196\n"
    "v_mfma_f32_16x16x32_bf16 a[140:143], v[172:175], v[232:235], a[140:143]\n"
    "v_add_f32 v245, v245, v223\n"
    "v_add_f32 v246, v246, v192\n"
    "v_mfma_f32_16x16x32_bf16 v[200:203], v[144:147], v[96:99], v[248:251]\n"
    "v_exp_f32 v197, v197\n"
    "v_mfma_f32_16x16x32_bf16 v[200:203], v[148:151], v[100:103], v[200:203]\n"
    "v_cvt_pk_bf16_f32 v228, v196, v197\n"
    "v_add_f32 v246, v246, v193\n"
    "v_mfma_f32_16x16x32_bf16 v[200:203], v[152:155], v[104:107], v[200:203]\n"
    "v_exp_f32 v198, v198\n"
    "v_mfma_f32_16x16x32_bf16 v[200:203], v[156:159], v[108:111], v[200:203]\n"
    "v_add_f32 v246, v246, v194\n"
    "v_add_f32 v247, v247, v196\n"
    "v_mfma_f32_16x16x32_bf16 v[204:207], v[156:159], v[124:127], v[248:251]\n"
    "v_exp_f32 v199, v199\n"
    "v_mfma_f32_16x16x32_bf16 v[204:207], v[152:155], v[120:123], v[204:207]\n"
    "buffer_store_dwordx4 v[60:63], v255, s[48:51], s89 offen offset:192 sc0 sc1 nt\n"
    "ds_read_b128 v[152:155], v252 offset:4640\n"
    "v_cvt_pk_bf16_f32 v229, v198, v199\n"
    "v_mfma_f32_16x16x32_bf16 v[204:207], v[148:151], v[116:119], v[204:207]\n"
    "v_exp_f32 v200, v200\n"
    "v_mfma_f32_16x16x32_bf16 v[204:207], v[144:147], v[112:115], v[204:207]\n"
    "s_waitcnt lgkmcnt(11)\n"
    "ds_read_b128 v[144:147], v252 offset:4128\n"
    "ds_read_b128 v[148:151], v252 offset:4384\n"
    "v_add_f32 v246, v246, v195\n"
    "v_mfma_f32_16x16x32_bf16 a[144:147], v[176:179], v[232:235], a[144:147]\n"
    "v_exp_f32 v201, v201\n"
    "v_mfma_f32_16x16x32_bf16 a[176:179], v[176:179], v[236:239], a[176:179]\n"
    "s_waitcnt lgkmcnt(11)\n"
    "ds_read_b128 v[156:159], v252 offset:4896\n"
    "v_cvt_pk_bf16_f32 v226, v200, v201\n"
    "v_mfma_f32_16x16x32_bf16 a[180:183], v[180:183], v[236:239], a[180:183]\n"
    "v_exp_f32 v202, v202\n"
    "v_mfma_f32_16x16x32_bf16 a[148:151], v[180:183], v[232:235], a[148:151]\n"
    "s_waitcnt lgkmcnt(10)\n"
    "v_add_f32 v247, v247, v197\n"
    "v_add_f32 v246, v246, v200\n"
    "v_mfma_f32_16x16x32_bf16 a[152:155], v[184:187], v[232:235], a[152:155]\n"
    "v_exp_f32 v203, v203\n"
    "v_mfma_f32_16x16x32_bf16 a[184:187], v[184:187], v[236:239], a[184:187]\n"
    "s_waitcnt lgkmcnt(8)\n"
    "v_cvt_pk_bf16_f32 v227, v202, v203\n"
    "v_add_f32 v247, v247, v198\n"
    "v_mfma_f32_16x16x32_bf16 a[188:191], v[188:191], v[236:239], a[188:191]\n"
    "v_exp_f32 v204, v204\n"
    "v_mfma_f32_16x16x32_bf16 a[156:159], v[188:191], v[232:235], a[156:159]\n"
    "v_add_f32 v247, v247, v199\n"
    "v_add_f32 v246, v246, v201\n"
    "v_permlane16_swap_b32 v244, v245\n"
    "v_accvgpr_read_b32 v64, a128\n"
    "v_add_f32 v244, v244, v245\n"
    "v_mov_b32 v245, v244\n"
    "v_accvgpr_read_b32 v65, a129\n"
    "v_accvgpr_read_b32 v66, a130\n"
    "v_permlane32_swap_b32 v244, v245\n"
    "v_accvgpr_read_b32 v67, a131\n"
    "v_add_f32 v244, v244, v245\n"
    "v_cmp_nlt_f32 vcc, s62, v244\n"
    "v_cmp_ngt_f32 s[66:67], s63, v244\n"
    "v_rcp_f32 v244, v244\n"
    "v_accvgpr_read_b32 v68, a132\n"
    "s_or_b64 s[66:67], s[66:67], vcc\n"
    "v_mov_b32 v245, v244\n"
    "v_accvgpr_read_b32 v69, a133\n"
    "v_accvgpr_read_b32 v70, a134\n"
    "s_or_b64 s[60:61], s[60:61], s[66:67]\n"
    "v_permlane16_swap_b32 v244, v245\n"
    "v_accvgpr_read_b32 v71, a135\n"
    "v_pk_mul_f32 v[64:65], v[64:65], v[244:245] op_sel_hi:[1,0]\n"
    "v_pk_mul_f32 v[66:67], v[66:67], v[244:245] op_sel_hi:[1,0]\n"
    "v_pk_mul_f32 v[68:69], v[68:69], v[244:245] op_sel_hi:[1,0]\n"
    "v_pk_mul_f32 v[70:71], v[70:71], v[244:245] op_sel_hi:[1,0]\n"
    "v_cvt_pk_bf16_f32 v64, v64, v65\n"
    "v_cvt_pk_bf16_f32 v65, v66, v67\n"
    "v_cvt_pk_bf16_f32 v66, v68, v69\n"
    "v_cvt_pk_bf16_f32 v67, v70, v71\n"
    "v_accvgpr_read_b32 v68, a136\n"
    "v_accvgpr_read_b32 v69, a137\n"
    "v_accvgpr_read_b32 v70, a138\n"
    "v_accvgpr_read_b32 v71, a139\n"
    "v_accvgpr_read_b32 v72, a140\n"
    "v_accvgpr_read_b32 v73, a141\n"
    "v_accvgpr_read_b32 v74, a142\n"
    "v_accvgpr_read_b32 v75, a143\n"
    "v_pk_mul_f32 v[68:69], v[68:69], v[244:245] op_sel_hi:[1,0]\n"
    "v_pk_mul_f32 v[70:71], v[70:71], v[244:245] op_sel_hi:[1,0]\n"
    "v_pk_mul_f32 v[72:73], v[72:73], v[244:245] op_sel_hi:[1,0]\n"
    "v_pk_mul_f32 v[74:75], v[74:75], v[244:245] op_sel_hi:[1,0]\n"
    "v_cvt_pk_bf16_f32 v68, v68, v69\n"
    "v_cvt_pk_bf16_f32 v69, v70, v71\n"
    "v_cvt_pk_bf16_f32 v70, v72, v73\n"
    "v_cvt_pk_bf16_f32 v71, v74, v75\n"
    "v_accvgpr_read_b32 v72, a144\n"
    "v_accvgpr_read_b32 v73, a145\n"
    "v_accvgpr_read_b32 v74, a146\n"
    "v_accvgpr_read_b32 v75, a147\n"
    "v_accvgpr_read_b32 v76, a148\n"
    "v_accvgpr_read_b32 v77, a149\n"
    "v_accvgpr_read_b32 v78, a150\n"
    "v_accvgpr_read_b32 v79, a151\n"
    "v_pk_mul_f32 v[72:73], v[72:73], v[244:245] op_sel_hi:[1,0]\n"
    "v_pk_mul_f32 v[74:75], v[74:75], v[244:245] op_sel_hi:[1,0]\n"
    "v_pk_mul_f32 v[76:77], v[76:77], v[244:245] op_sel_hi:[1,0]\n"
    "v_pk_mul_f32 v[78:79], v[78:79], v[244:245] op_sel_hi:[1,0]\n"
    "v_cvt_pk_bf16_f32 v72, v72, v73\n"
    "v_cvt_pk_bf16_f32 v73, v74, v75\n"
    "v_cvt_pk_bf16_f32 v74, v76, v77\n"
    "v_cvt_pk_bf16_f32 v75, v78, v79\n"
    "v_accvgpr_read_b32 v76, a152\n"
    "v_accvgpr_read_b32 v77, a153\n"
    "v_accvgpr_read_b32 v78, a154\n"
    "v_accvgpr_read_b32 v79, a155\n"
    "v_accvgpr_read_b32 v80, a156\n"
    "v_accvgpr_read_b32 v81, a157\n"
    "v_accvgpr_read_b32 v82, a158\n"
    "v_accvgpr_read_b32 v83, a159\n"
    "v_pk_mul_f32 v[76:77], v[76:77], v[244:245] op_sel_hi:[1,0]\n"
    "v_pk_mul_f32 v[78:79], v[78:79], v[244:245] op_sel_hi:[1,0]\n"
    "v_pk_mul_f32 v[80:81], v[80:81], v[244:245] op_sel_hi:[1,0]\n"
    "v_pk_mul_f32 v[82:83], v[82:83], v[244:245] op_sel_hi:[1,0]\n"
    "v_cvt_pk_bf16_f32 v76, v76, v77\n"
    "v_cvt_pk_bf16_f32 v77, v78, v79\n"
    "v_cvt_pk_bf16_f32 v78, v80, v81\n"
    "v_cvt_pk_bf16_f32 v79, v82, v83\n"
    "v_accvgpr_read_b32 v80, a160\n"
    "v_accvgpr_read_b32 v81, a161\n"
    "v_accvgpr_read_b32 v82, a162\n"
    "v_accvgpr_read_b32 v83, a163\n"
    "v_accvgpr_read_b32 v84, a164\n"
    "v_accvgpr_read_b32 v85, a165\n"
    "v_accvgpr_read_b32 v86, a166\n"
    "v_accvgpr_read_b32 v87, a167\n"
    "v_pk_mul_f32 v[80:81], v[80:81], v[244:245] op_sel:[0,1] op_sel_hi:[1,1]\n"
    "v_pk_mul_f32 v[82:83], v[82:83], v[244:245] op_sel:[0,1] op_sel_hi:[1,1]\n"
    "v_pk_mul_f32 v[84:85], v[84:85], v[244:245] op_sel:[0,1] op_sel_hi:[1,1]\n"
    "v_pk_mul_f32 v[86:87], v[86:87], v[244:245] op_sel:[0,1] op_sel_hi:[1,1]\n"
    "v_cvt_pk_bf16_f32 v80, v80, v81\n"
    "v_cvt_pk_bf16_f32 v81, v82, v83\n"
    "v_cvt_pk_bf16_f32 v82, v84, v85\n"
    "v_cvt_pk_bf16_f32 v83, v86, v87\n"
    "v_accvgpr_read_b32 v84, a168\n"
    "v_accvgpr_read_b32 v85, a169\n"
    "v_accvgpr_read_b32 v86, a170\n"
    "v_accvgpr_read_b32 v87, a171\n"
    "v_accvgpr_read_b32 v88, a172\n"
    "v_accvgpr_read_b32 v89, a173\n"
    "v_accvgpr_read_b32 v90, a174\n"
    "v_accvgpr_read_b32 v91, a175\n"
    "v_pk_mul_f32 v[84:85], v[84:85], v[244:245] op_sel:[0,1] op_sel_hi:[1,1]\n"
    "v_pk_mul_f32 v[86:87], v[86:87], v[244:245] op_sel:[0,1] op_sel_hi:[1,1]\n"
    "v_pk_mul_f32 v[88:89], v[88:89], v[244:245] op_sel:[0,1] op_sel_hi:[1,1]\n"
    "v_pk_mul_f32 v[90:91], v[90:91], v[244:245] op_sel:[0,1] op_sel_hi:[1,1]\n"
    "v_cvt_pk_bf16_f32 v84, v84, v85\n"
    "v_cvt_pk_bf16_f32 v85, v86, v87\n"
    "v_cvt_pk_bf16_f32 v86, v88, v89\n"
    "v_cvt_pk_bf16_f32 v87, v90, v91\n"
    "v_accvgpr_read_b32 v88, a176\n"
    "v_accvgpr_read_b32 v89, a177\n"
    "v_accvgpr_read_b32 v90, a178\n"
    "v_accvgpr_read_b32 v91, a179\n"
    "v_accvgpr_read_b32 v92, a180\n"
    "v_accvgpr_read_b32 v93, a181\n"
    "v_accvgpr_read_b32 v94, a182\n"
    "v_accvgpr_read_b32 v95, a183\n"
    "v_pk_mul_f32 v[88:89], v[88:89], v[244:245] op_sel:[0,1] op_sel_hi:[1,1]\n"
    "v_pk_mul_f32 v[90:91], v[90:91], v[244:245] op_sel:[0,1] op_sel_hi:[1,1]\n"
    "v_pk_mul_f32 v[92:93], v[92:93], v[244:245] op_sel:[0,1] op_sel_hi:[1,1]\n"
    "v_pk_mul_f32 v[94:95], v[94:95], v[244:245] op_sel:[0,1] op_sel_hi:[1,1]\n"
    "v_cvt_pk_bf16_f32 v88, v88, v89\n"
    "v_cvt_pk_bf16_f32 v89, v90, v91\n"
    "v_cvt_pk_bf16_f32 v90, v92, v93\n"
    "v_cvt_pk_bf16_f32 v91, v94, v95\n"
    "v_accvgpr_read_b32 v92, a184\n"
    "v_accvgpr_read_b32 v93, a185\n"
    "v_accvgpr_read_b32 v94, a186\n"
    "v_accvgpr_read_b32 v95, a187\n"
    "v_pk_mul_f32 v[92:93], v[92:93], v[244:245] op_sel:[0,1] op_sel_hi:[1,1]\n"
    "v_pk_mul_f32 v[94:95], v[94:95], v[244:245] op_sel:[0,1] op_sel_hi:[1,1]\n"
    "v_cvt_pk_bf16_f32 v92, v92, v93\n"
    "v_cvt_pk_bf16_f32 v93, v94, v95\n"
    "v_accvgpr_read_b32 v94, a188\n"
    "v_accvgpr_read_b32 v95, a189\n"
    "v_accvgpr_read_b32 v244, a190\n"
    "v_mul_f32 v94, v94, v245\n"
    "v_mul_f32 v95, v95, v245\n"
    "v_mul_f32 v244, v244, v245\n"
    "v_cvt_pk_bf16_f32 v94, v94, v95\n"
    "v_accvgpr_read_b32 v95, a191\n"
    "v_mul_f32 v95, v95, v245\n"
    "v_cvt_pk_bf16_f32 v95, v244, v95\n"
    "s_waitcnt lgkmcnt(6)\n"
    "v_mfma_f32_16x16x32_bf16 v[208:211], v[128:131], v[96:99], v[248:251]\n"
    "s_waitcnt lgkmcnt(5)\n"
    "v_exp_f32 v205, v205\n"
    "v_mfma_f32_16x16x32_bf16 v[208:211], v[132:135], v[100:103], v[208:211]\n"
    "buffer_store_dwordx4 v[64:67], v255, s[48:51], s90 offen sc0 sc1 nt\n"
    "v_cvt_pk_bf16_f32 v230, v204, v205\n"
    "v_add_f32 v246, v246, v202\n"
    "v_add_f32 v247, v247, v204\n"
    "v_mfma_f32_16x16x32_bf16 v[208:211], v[136:139], v[104:107], v[208:211]\n"
    "s_waitcnt lgkmcnt(4)\n"
    "v_exp_f32 v206, v206\n"
    "v_mfma_f32_16x16x32_bf16 v[208:211], v[140:143], v[108:111], v[208:211]\n"
    "v_add_f32 v246, v246, v203\n"
    "v_add_f32 v247, v247, v205\n"
    "v_add_f32 v247, v247, v206\n"
    "v_mfma_f32_16x16x32_bf16 v[212:215], v[140:143], v[124:127], v[248:251]\n"
    "v_exp_f32 v207, v207\n"
    "v_mfma_f32_16x16x32_bf16 v[212:215], v[136:139], v[120:123], v[212:215]\n"
    "ds_read_b128 v[136:139], v252 offset:41792\n"
    "ds_read_b128 v[140:143], v252 offset:42048\n"
    "v_cvt_pk_bf16_f32 v231, v206, v207\n"
    "v_mfma_f32_16x16x32_bf16 v[212:215], v[132:135], v[116:119], v[212:215]\n"
    "v_exp_f32 v208, v208\n"
    "v_mfma_f32_16x16x32_bf16 v[212:215], v[128:131], v[112:115], v[212:215]\n"
    "ds_read_b128 v[128:131], v252 offset:41280\n"
    "ds_read_b128 v[132:135], v252 offset:41536\n"
    "v_add_f32 v247, v247, v207\n"
    "v_mfma_f32_16x16x32_bf16 a[192:195], v[160:163], v[224:227], a[192:195]\n"
    "v_exp_f32 v209, v209\n"
    "v_mfma_f32_16x16x32_bf16 a[224:227], v[160:163], v[228:231], a[224:227]\n"
    "ds_read_b64_tr_b16 v[160:161], v253 offset:0\n"
    "ds_read_b64_tr_b16 v[162:163], v253 offset:4128\n"
    "v_cvt_pk_bf16_f32 v232, v208, v209\n"
    "v_mfma_f32_16x16x32_bf16 a[228:231], v[164:167], v[228:231], a[228:231]\n"
    "v_exp_f32 v210, v210\n"
    "v_mfma_f32_16x16x32_bf16 a[196:199], v[164:167], v[224:227], a[196:199]\n"
    "ds_read_b64_tr_b16 v[164:165], v253 offset:8\n"
    "ds_read_b64_tr_b16 v[166:167], v253 offset:4136\n"
    "v_add_f32 v246, v246, v208\n"
    "v_mfma_f32_16x16x32_bf16 a[200:203], v[168:171], v[224:227], a[200:203]\n"
    "v_exp_f32 v211, v211\n"
    "v_mfma_f32_16x16x32_bf16 a[232:235], v[168:171], v[228:231], a[232:235]\n"
    "buffer_store_dwordx4 v[68:71], v255, s[48:51], s90 offen offset:64 sc0 sc1 nt\n"
    "ds_read_b64_tr_b16 v[168:169], v253 offset:256\n"
    "ds_read_b64_tr_b16 v[170:171], v253 offset:4384\n"
    "v_cvt_pk_bf16_f32 v233, v210, v211\n"
    "v_mfma_f32_16x16x32_bf16 a[236:239], v[172:175], v[228:231], a[236:239]\n"
    "v_exp_f32 v212, v212\n"
    "v_mfma_f32_16x16x32_bf16 a[204:207], v[172:175], v[224:227], a[204:207]\n"
    "s_waitcnt lgkmcnt(12)\n"
    "ds_read_b64_tr_b16 v[172:173], v253 offset:264\n"
    "ds_read_b64_tr_b16 v[174:175], v253 offset:4392\n"
    "v_add_f32 v246, v246, v209\n"
    "v_mfma_f32_16x16x32_bf16 v[216:219], v[144:147], v[96:99], v[248:251]\n"
    "s_waitcnt lgkmcnt(13)\n"
    "v_exp_f32 v213, v213\n"
    "v_mfma_f32_16x16x32_bf16 v[216:219], v[148:151], v[100:103], v[216:219]\n"
    "v_cvt_pk_bf16_f32 v236, v212, v213\n"
    "v_add_f32 v246, v246, v210\n"
    "v_add_f32 v247, v247, v212\n"
    "v_mfma_f32_16x16x32_bf16 v[216:219], v[152:155], v[104:107], v[216:219]\n"
    "s_waitcnt lgkmcnt(12)\n"
    "v_exp_f32 v214, v214\n"
    "v_mfma_f32_16x16x32_bf16 v[216:219], v[156:159], v[108:111], v[216:219]\n"
    "v_add_f32 v246, v246, v211\n"
    "v_add_f32 v247, v247, v213\n"
    "v_add_f32 v247, v247, v214\n"
    "v_mfma_f32_16x16x32_bf16 v[220:223], v[156:159], v[124:127], v[248:251]\n"
    "v_exp_f32 v215, v215\n"
    "v_mfma_f32_16x16x32_bf16 v[220:223], v[152:155], v[120:123], v[220:223]\n"
    "ds_read_b128 v[152:155], v252 offset:45920\n"
    "ds_read_b128 v[156:159], v252 offset:46176\n"
    "v_cvt_pk_bf16_f32 v237, v214, v215\n"
    "v_mfma_f32_16x16x32_bf16 v[220:223], v[148:151], v[116:119], v[220:223]\n"
    "v_exp_f32 v216, v216\n"
    "v_mfma_f32_16x16x32_bf16 v[220:223], v[144:147], v[112:115], v[220:223]\n"
    "ds_read_b128 v[144:147], v252 offset:45408\n"
    "ds_read_b128 v[148:151], v252 offset:45664\n"
    "v_add_f32 v247, v247, v215\n"
    "v_mfma_f32_16x16x32_bf16 a[208:211], v[176:179], v[224:227], a[208:211]\n"
    "v_exp_f32 v217, v217\n"
    "v_mfma_f32_16x16x32_bf16 a[240:243], v[176:179], v[228:231], a[240:243]\n"
    "buffer_store_dwordx4 v[72:75], v255, s[48:51], s90 offen offset:128 sc0 sc1 nt\n"
    "ds_read_b64_tr_b16 v[176:177], v253 offset:512\n"
    "ds_read_b64_tr_b16 v[178:179], v253 offset:4640\n"
    "v_cvt_pk_bf16_f32 v234, v216, v217\n"
    "v_mfma_f32_16x16x32_bf16 a[244:247], v[180:183], v[228:231], a[244:247]\n"
    "v_exp_f32 v218, v218\n"
    "v_mfma_f32_16x16x32_bf16 a[212:215], v[180:183], v[224:227], a[212:215]\n"
    "ds_read_b64_tr_b16 v[180:181], v253 offset:520\n"
    "ds_read_b64_tr_b16 v[182:183], v253 offset:4648\n"
    "v_add_f32 v246, v246, v216\n"
    "v_mfma_f32_16x16x32_bf16 a[216:219], v[184:187], v[224:227], a[216:219]\n"
    "v_exp_f32 v219, v219\n"
    "v_mfma_f32_16x16x32_bf16 a[248:251], v[184:187], v[228:231], a[248:251]\n"
    "ds_read_b64_tr_b16 v[184:185], v253 offset:768\n"
    "ds_read_b64_tr_b16 v[186:187], v253 offset:4896\n"
    "v_cvt_pk_bf16_f32 v235, v218, v219\n"
    "v_mfma_f32_16x16x32_bf16 a[252:255], v[188:191], v[228:231], a[252:255]\n"
    "v_exp_f32 v220, v220\n"
    "v_mfma_f32_16x16x32_bf16 a[220:223], v[188:191], v[224:227], a[220:223]\n"
    "s_waitcnt lgkmcnt(15)\n"
    "ds_read_b64_tr_b16 v[188:189], v253 offset:776\n"
    "ds_read_b64_tr_b16 v[190:191], v253 offset:4904\n"
    "v_add_f32 v246, v246, v217\n"
    "v_mfma_f32_16x16x32_bf16 v[192:195], v[128:131], v[96:99], v[248:251]\n"
    "v_exp_f32 v221, v221\n"
    "v_mfma_f32_16x16x32_bf16 v[192:195], v[132:135], v[100:103], v[192:195]\n"
    "v_cvt_pk_bf16_f32 v238, v220, v221\n"
    "v_add_f32 v246, v246, v218\n"
    "v_add_f32 v247, v247, v220\n"
    "v_mfma_f32_16x16x32_bf16 v[192:195], v[136:139], v[104:107], v[192:195]\n"
    "v_exp_f32 v222, v222\n"
    "v_mfma_f32_16x16x32_bf16 v[192:195], v[140:143], v[108:111], v[192:195]\n"
    "v_add_f32 v246, v246, v219\n"
    "v_add_f32 v247, v247, v221\n"
    "v_add_f32 v247, v247, v222\n"
    "v_mfma_f32_16x16x32_bf16 v[196:199], v[140:143], v[124:127], v[248:251]\n"
    "v_exp_f32 v223, v223\n"
    "v_mfma_f32_16x16x32_bf16 v[196:199], v[136:139], v[120:123], v[196:199]\n"
    "buffer_store_dwordx4 v[76:79], v255, s[48:51], s90 offen offset:192 sc0 sc1 nt\n"
    "ds_read_b128 v[136:139], v252 offset:33536\n"
    "ds_read_b128 v[140:143], v252 offset:33792\n"
    "v_cvt_pk_bf16_f32 v239, v222, v223\n"
    "v_mfma_f32_16x16x32_bf16 v[196:199], v[132:135], v[116:119], v[196:199]\n"
    "v_exp_f32 v192, v192\n"
    "v_mfma_f32_16x16x32_bf16 v[196:199], v[128:131], v[112:115], v[196:199]\n"
    "ds_read_b128 v[128:131], v252 offset:33024\n"
    "ds_read_b128 v[132:135], v252 offset:33280\n"
    "v_add_f32 v247, v247, v223\n"
    "v_mfma_f32_16x16x32_bf16 a[192:195], v[160:163], v[232:235], a[192:195]\n"
    "v_exp_f32 v193, v193\n"
    "v_mfma_f32_16x16x32_bf16 a[224:227], v[160:163], v[236:239], a[224:227]\n"
    "s_waitcnt lgkmcnt(15)\n"
    "ds_read_b64_tr_b16 v[160:161], v253 offset:41280\n"
    "ds_read_b64_tr_b16 v[162:163], v253 offset:45408\n"
    "v_cvt_pk_bf16_f32 v224, v192, v193\n"
    "v_mfma_f32_16x16x32_bf16 a[228:231], v[164:167], v[236:239], a[228:231]\n"
    "v_exp_f32 v194, v194\n"
    "v_mfma_f32_16x16x32_bf16 a[196:199], v[164:167], v[232:235], a[196:199]\n"
    "ds_read_b64_tr_b16 v[164:165], v253 offset:41288\n"
    "ds_read_b64_tr_b16 v[166:167], v253 offset:45416\n"
    "v_add_f32 v246, v246, v192\n"
    "v_mfma_f32_16x16x32_bf16 a[200:203], v[168:171], v[232:235], a[200:203]\n"
    "v_exp_f32 v195, v195\n"
    "v_mfma_f32_16x16x32_bf16 a[232:235], v[168:171], v[236:239], a[232:235]\n"
    "ds_read_b64_tr_b16 v[168:169], v253 offset:41536\n"
    "ds_read_b64_tr_b16 v[170:171], v253 offset:45664\n"
    "v_cvt_pk_bf16_f32 v225, v194, v195\n"
    "v_mfma_f32_16x16x32_bf16 a[236:239], v[172:175], v[236:239], a[236:239]\n"
    "v_exp_f32 v196, v196\n"
    "v_mfma_f32_16x16x32_bf16 a[204:207], v[172:175], v[232:235], a[204:207]\n"
    "s_waitcnt lgkmcnt(15)\n"
    "ds_read_b64_tr_b16 v[172:173], v253 offset:41544\n"
    "ds_read_b64_tr_b16 v[174:175], v253 offset:45672\n"
    "v_add_f32 v246, v246, v193\n"
    "v_mfma_f32_16x16x32_bf16 v[200:203], v[144:147], v[96:99], v[248:251]\n"
    "v_exp_f32 v197, v197\n"
    "v_mfma_f32_16x16x32_bf16 v[200:203], v[148:151], v[100:103], v[200:203]\n"
    "buffer_store_dwordx4 v[80:83], v255, s[48:51], s91 offen sc0 sc1 nt\n"
    "v_cvt_pk_bf16_f32 v228, v196, v197\n"
    "v_add_f32 v246, v246, v194\n"
    "v_add_f32 v247, v247, v196\n"
    "v_mfma_f32_16x16x32_bf16 v[200:203], v[152:155], v[104:107], v[200:203]\n"
    "v_exp_f32 v198, v198\n"
    "v_mfma_f32_16x16x32_bf16 v[200:203], v[156:159], v[108:111], v[200:203]\n"
    "v_add_f32 v246, v246, v195\n"
    "v_add_f32 v247, v247, v197\n"
    "v_add_f32 v247, v247, v198\n"
    "v_mfma_f32_16x16x32_bf16 v[204:207], v[156:159], v[124:127], v[248:251]\n"
    "v_exp_f32 v199, v199\n"
    "v_mfma_f32_16x16x32_bf16 v[204:207], v[152:155], v[120:123], v[204:207]\n"
    "ds_read_b128 v[152:155], v252 offset:37664\n"
    "ds_read_b128 v[156:159], v252 offset:37920\n"
    "v_cvt_pk_bf16_f32 v229, v198, v199\n"
    "v_mfma_f32_16x16x32_bf16 v[204:207], v[148:151], v[116:119], v[204:207]\n"
    "v_exp_f32 v200, v200\n"
    "v_mfma_f32_16x16x32_bf16 v[204:207], v[144:147], v[112:115], v[204:207]\n"
    "ds_read_b128 v[144:147], v252 offset:37152\n"
    "ds_read_b128 v[148:151], v252 offset:37408\n"
    "v_add_f32 v247, v247, v199\n"
    "v_mfma_f32_16x16x32_bf16 a[208:211], v[176:179], v[232:235], a[208:211]\n"
    "v_exp_f32 v201, v201\n"
    "v_mfma_f32_16x16x32_bf16 a[240:243], v[176:179], v[236:239], a[240:243]\n"
    "s_waitcnt lgkmcnt(15)\n"
    "ds_read_b64_tr_b16 v[176:177], v253 offset:41792\n"
    "ds_read_b64_tr_b16 v[178:179], v253 offset:45920\n"
    "v_cvt_pk_bf16_f32 v226, v200, v201\n"
    "v_mfma_f32_16x16x32_bf16 a[244:247], v[180:183], v[236:239], a[244:247]\n"
    "v_exp_f32 v202, v202\n"
    "v_mfma_f32_16x16x32_bf16 a[212:215], v[180:183], v[232:235], a[212:215]\n"
    "ds_read_b64_tr_b16 v[180:181], v253 offset:41800\n"
    "ds_read_b64_tr_b16 v[182:183], v253 offset:45928\n"
    "v_add_f32 v246, v246, v200\n"
    "v_mfma_f32_16x16x32_bf16 a[216:219], v[184:187], v[232:235], a[216:219]\n"
    "v_exp_f32 v203, v203\n"
    "v_mfma_f32_16x16x32_bf16 a[248:251], v[184:187], v[236:239], a[248:251]\n"
    "buffer_store_dwordx4 v[84:87], v255, s[48:51], s91 offen offset:64 sc0 sc1 nt\n"
    "ds_read_b64_tr_b16 v[184:185], v253 offset:42048\n"
    "ds_read_b64_tr_b16 v[186:187], v253 offset:46176\n"
    "v_cvt_pk_bf16_f32 v227, v202, v203\n"
    "v_mfma_f32_16x16x32_bf16 a[252:255], v[188:191], v[236:239], a[252:255]\n"
    "v_exp_f32 v204, v204\n"
    "v_mfma_f32_16x16x32_bf16 a[220:223], v[188:191], v[232:235], a[220:223]\n"
    "s_waitcnt lgkmcnt(15)\n"
    "ds_read_b64_tr_b16 v[188:189], v253 offset:42056\n"
    "ds_read_b64_tr_b16 v[190:191], v253 offset:46184\n"
    "v_add_f32 v246, v246, v201\n"
    "v_mfma_f32_16x16x32_bf16 v[208:211], v[128:131], v[96:99], v[248:251]\n"
    "v_exp_f32 v205, v205\n"
    "v_mfma_f32_16x16x32_bf16 v[208:211], v[132:135], v[100:103], v[208:211]\n"
    "v_cvt_pk_bf16_f32 v230, v204, v205\n"
    "v_add_f32 v246, v246, v202\n"
    "v_mfma_f32_16x16x32_bf16 v[208:211], v[136:139], v[104:107], v[208:211]\n"
    "v_exp_f32 v206, v206\n"
    "v_mfma_f32_16x16x32_bf16 v[208:211], v[140:143], v[108:111], v[208:211]\n"
    "v_add_f32 v246, v246, v203\n"
    "v_add_f32 v247, v247, v204\n"
    "v_mfma_f32_16x16x32_bf16 v[212:215], v[140:143], v[124:127], v[248:251]\n"
    "v_exp_f32 v207, v207\n"
    "v_mfma_f32_16x16x32_bf16 v[212:215], v[136:139], v[120:123], v[212:215]\n"
    "v_cvt_pk_bf16_f32 v231, v206, v207\n"
    "v_add_f32 v247, v247, v205\n"
    "v_mfma_f32_16x16x32_bf16 v[212:215], v[132:135], v[116:119], v[212:215]\n"
    "v_exp_f32 v208, v208\n"
    "v_mfma_f32_16x16x32_bf16 v[212:215], v[128:131], v[112:115], v[212:215]\n"
    "v_add_f32 v247, v247, v206\n"
    "v_add_f32 v246, v246, v208\n"
    "v_mfma_f32_16x16x32_bf16 a[192:195], v[160:163], v[224:227], a[192:195]\n"
    "v_exp_f32 v209, v209\n"
    "v_mfma_f32_16x16x32_bf16 a[224:227], v[160:163], v[228:231], a[224:227]\n"
    "s_waitcnt lgkmcnt(15)\n"
    "buffer_store_dwordx4 v[88:91], v255, s[48:51], s91 offen offset:128 sc0 sc1 nt\n"
    "ds_read_b64_tr_b16 v[160:161], v253 offset:33024\n"
    "ds_read_b64_tr_b16 v[162:163], v253 offset:37152\n"
    "v_cvt_pk_bf16_f32 v232, v208, v209\n"
    "v_mfma_f32_16x16x32_bf16 a[228:231], v[164:167], v[228:231], a[228:231]\n"
    "v_exp_f32 v210, v210\n"
    "v_mfma_f32_16x16x32_bf16 a[196:199], v[164:167], v[224:227], a[196:199]\n"
    "s_waitcnt lgkmcnt(15)\n"
    "ds_read_b64_tr_b16 v[164:165], v253 offset:33032\n"
    "ds_read_b64_tr_b16 v[166:167], v253 offset:37160\n"
    "v_add_f32 v247, v247, v207\n"
    "v_mfma_f32_16x16x32_bf16 a[200:203], v[168:171], v[224:227], a[200:203]\n"
    "v_exp_f32 v211, v211\n"
    "v_mfma_f32_16x16x32_bf16 a[232:235], v[168:171], v[228:231], a[232:235]\n"
    "s_waitcnt lgkmcnt(15)\n"
    "ds_read_b64_tr_b16 v[168:169], v253 offset:33280\n"
    "ds_read_b64_tr_b16 v[170:171], v253 offset:37408\n"
    "v_cvt_pk_bf16_f32 v233, v210, v211\n"
    "v_mfma_f32_16x16x32_bf16 a[236:239], v[172:175], v[228:231], a[236:239]\n"
    "v_exp_f32 v212, v212\n"
    "v_mfma_f32_16x16x32_bf16 a[204:207], v[172:175], v[224:227], a[204:207]\n"
    "s_waitcnt lgkmcnt(15)\n"
    "ds_read_b64_tr_b16 v[172:173], v253 offset:33288\n"
    "ds_read_b64_tr_b16 v[174:175], v253 offset:37416\n"
    "v_add_f32 v246, v246, v209\n"
    "v_mfma_f32_16x16x32_bf16 v[216:219], v[144:147], v[96:99], v[248:251]\n"
    "s_waitcnt lgkmcnt(15)\n"
    "v_exp_f32 v213, v213\n"
    "v_mfma_f32_16x16x32_bf16 v[216:219], v[148:151], v[100:103], v[216:219]\n"
    "v_cvt_pk_bf16_f32 v236, v212, v213\n"
    "v_add_f32 v246, v246, v210\n"
    "v_mfma_f32_16x16x32_bf16 v[216:219], v[152:155], v[104:107], v[216:219]\n"
    "v_exp_f32 v214, v214\n"
    "v_mfma_f32_16x16x32_bf16 v[216:219], v[156:159], v[108:111], v[216:219]\n"
    "v_add_f32 v246, v246, v211\n"
    "v_add_f32 v247, v247, v212\n"
    "v_mfma_f32_16x16x32_bf16 v[220:223], v[156:159], v[124:127], v[248:251]\n"
    "v_exp_f32 v215, v215\n"
    "v_mfma_f32_16x16x32_bf16 v[220:223], v[152:155], v[120:123], v[220:223]\n"
    "buffer_store_dwordx4 v[92:95], v255, s[48:51], s91 offen offset:192 sc0 sc1 nt\n"
    "v_cvt_pk_bf16_f32 v237, v214, v215\n"
    "v_add_f32 v247, v247, v213\n"
    "v_mfma_f32_16x16x32_bf16 v[220:223], v[148:151], v[116:119], v[220:223]\n"
    "v_exp_f32 v216, v216\n"
    "v_mfma_f32_16x16x32_bf16 v[220:223], v[144:147], v[112:115], v[220:223]\n"
    "s_waitcnt lgkmcnt(14)\n"
    "v_add_f32 v247, v247, v214\n"
    "v_add_f32 v246, v246, v216\n"
    "v_mfma_f32_16x16x32_bf16 a[208:211], v[176:179], v[224:227], a[208:211]\n"
    "v_exp_f32 v217, v217\n"
    "v_mfma_f32_16x16x32_bf16 a[240:243], v[176:179], v[228:231], a[240:243]\n"
    "s_waitcnt lgkmcnt(12)\n"
    "ds_read_b64_tr_b16 v[176:177], v253 offset:33536\n"
    "v_cvt_pk_bf16_f32 v234, v216, v217\n"
    "v_mfma_f32_16x16x32_bf16 a[244:247], v[180:183], v[228:231], a[244:247]\n"
    "v_exp_f32 v218, v218\n"
    "v_mfma_f32_16x16x32_bf16 a[212:215], v[180:183], v[224:227], a[212:215]\n"
    "s_waitcnt lgkmcnt(11)\n"
    "ds_read_b64_tr_b16 v[178:179], v253 offset:37664\n"
    "ds_read_b64_tr_b16 v[180:181], v253 offset:33544\n"
    "v_add_f32 v247, v247, v215\n"
    "v_mfma_f32_16x16x32_bf16 a[216:219], v[184:187], v[224:227], a[216:219]\n"
    "v_exp_f32 v219, v219\n"
    "v_mfma_f32_16x16x32_bf16 a[248:251], v[184:187], v[228:231], a[248:251]\n"
    "s_waitcnt lgkmcnt(11)\n"
    "ds_read_b64_tr_b16 v[182:183], v253 offset:37672\n"
    "v_cvt_pk_bf16_f32 v235, v218, v219\n"
    "v_mfma_f32_16x16x32_bf16 a[252:255], v[188:191], v[228:231], a[252:255]\n"
    "v_exp_f32 v220, v220\n"
    "v_mfma_f32_16x16x32_bf16 a[220:223], v[188:191], v[224:227], a[220:223]\n"
    "ds_read_b64_tr_b16 v[184:185], v253 offset:33792\n"
    "ds_read_b64_tr_b16 v[186:187], v253 offset:37920\n"
    "v_add_f32 v246, v246, v217\n"
    "v_exp_f32 v221, v221\n"
    "ds_read_b64_tr_b16 v[188:189], v253 offset:33800\n"
    "ds_read_b64_tr_b16 v[190:191], v253 offset:37928\n"
    "v_cvt_pk_bf16_f32 v238, v220, v221\n"
    "v_add_f32 v246, v246, v218\n"
    "v_exp_f32 v222, v222\n"
    "v_add_f32 v246, v246, v219\n"
    "v_add_f32 v247, v247, v220\n"
    "v_add_f32 v247, v247, v221\n"
    "v_add_f32 v247, v247, v222\n"
    "v_exp_f32 v223, v223\n"
    "s_nop 0\n"
    "v_cvt_pk_bf16_f32 v239, v222, v223\n"
    "v_add_f32 v247, v247, v223\n"
    "s_waitcnt lgkmcnt(14)\n"
    "v_mfma_f32_16x16x32_bf16 a[192:195], v[160:163], v[232:235], a[192:195]\n"
    "v_permlane16_swap_b32 v246, v247\n"
    "v_add_f32 v246, v246, v247\n"
    "v_mov_b32 v247, v246\n"
    "s_nop 1\n"
    "v_permlane32_swap_b32 v246, v247\n"
    "v_add_f32 v246, v246, v247\n"
    "v_cmp_nlt_f32 vcc, s62, v246\n"
    "v_mfma_f32_16x16x32_bf16 a[224:227], v[160:163], v[236:239], a[224:227]\n"
    "s_waitcnt lgkmcnt(12)\n"
    "v_cmp_ngt_f32 s[66:67], s63, v246\n"
    "v_rcp_f32 v246, v246\n"
    "s_or_b64 s[66:67], s[66:67], vcc\n"
    "v_mov_b32 v247, v246\n"
    "s_cmp_eq_u32 s52, 3\n"
    "s_cselect_b32 s62, 0xffff, -1\n"
    "v_mfma_f32_16x16x32_bf16 a[228:231], v[164:167], v[236:239], a[228:231]\n"
    "s_and_b32 s66, s66, s62\n"
    "s_and_b32 s67, s67, s62\n"
    "s_or_b64 s[60:61], s[60:61], s[66:67]\n"
    "v_permlane16_swap_b32 v246, v247\n"
    "v_mfma_f32_16x16x32_bf16 a[196:199], v[164:167], v[232:235], a[196:199]\n"
    "s_waitcnt lgkmcnt(10)\n"
    "v_mfma_f32_16x16x32_bf16 a[200:203], v[168:171], v[232:235], a[200:203]\n"
    "v_mfma_f32_16x16x32_bf16 a[232:235], v[168:171], v[236:239], a[232:235]\n"
    "s_waitcnt lgkmcnt(8)\n"
    "v_mfma_f32_16x16x32_bf16 a[236:239], v[172:175], v[236:239], a[236:239]\n"
    "v_mfma_f32_16x16x32_bf16 a[204:207], v[172:175], v[232:235], a[204:207]\n"
    "s_waitcnt lgkmcnt(6)\n"
    "v_mfma_f32_16x16x32_bf16 a[208:211], v[176:179], v[232:235], a[208:211]\n"
    "v_accvgpr_read_b32 v96, a192\n"
    "v_accvgpr_read_b32 v97, a193\n"
    "v_accvgpr_read_b32 v98, a194\n"
    "v_mfma_f32_16x16x32_bf16 a[240:243], v[176:179], v[236:239], a[240:243]\n"
    "s_waitcnt lgkmcnt(4)\n"
    "v_accvgpr_read_b32 v99, a195\n"
    "v_accvgpr_read_b32 v100, a196\n"
    "v_accvgpr_read_b32 v101, a197\n"
    "v_mfma_f32_16x16x32_bf16 a[244:247], v[180:183], v[236:239], a[244:247]\n"
    "v_accvgpr_read_b32 v102, a198\n"
    "v_accvgpr_read_b32 v103, a199\n"
    "v_mul_f32 v96, v96, v246\n"
    "v_mfma_f32_16x16x32_bf16 a[212:215], v[180:183], v[232:235], a[212:215]\n"
    "s_waitcnt lgkmcnt(2)\n"
    "v_mul_f32 v97, v97, v246\n"
    "v_mul_f32 v98, v98, v246\n"
    "v_mul_f32 v99, v99, v246\n"
    "v_mfma_f32_16x16x32_bf16 a[216:219], v[184:187], v[232:235], a[216:219]\n"
    "v_mul_f32 v100, v100, v246\n"
    "v_mul_f32 v101, v101, v246\n"
    "v_mul_f32 v102, v102, v246\n"
    "v_mfma_f32_16x16x32_bf16 a[248:251], v[184:187], v[236:239], a[248:251]\n"
    "s_waitcnt lgkmcnt(0)\n"
    "v_mul_f32 v103, v103, v246\n"
    "v_cvt_pk_bf16_f32 v96, v96, v97\n"
    "v_cvt_pk_bf16_f32 v97, v98, v99\n"
    "v_mfma_f32_16x16x32_bf16 a[252:255], v[188:191], v[236:239], a[252:255]\n"
    "v_cvt_pk_bf16_f32 v98, v100, v101\n"
    "v_cvt_pk_bf16_f32 v99, v102, v103\n"
    "buffer_store_dwordx4 v[96:99], v255, s[48:51], s92 offen sc0 sc1 nt\n"
    "v_mfma_f32_16x16x32_bf16 a[220:223], v[188:191], v[232:235], a[220:223]\n"
    "v_accvgpr_read_b32 v104, a224\n"
    "v_accvgpr_read_b32 v105, a225\n"
    "v_accvgpr_read_b32 v106, a226\n"
    "v_accvgpr_read_b32 v107, a227\n"
    "v_accvgpr_read_b32 v108, a228\n"
    "v_accvgpr_read_b32 v109, a229\n"
    "v_accvgpr_read_b32 v110, a230\n"
    "v_accvgpr_read_b32 v111, a231\n"
    "v_mul_f32 v104, v104, v247\n"
    "v_mul_f32 v105, v105, v247\n"
    "v_mul_f32 v106, v106, v247\n"
    "v_mul_f32 v107, v107, v247\n"
    "v_mul_f32 v108, v108, v247\n"
    "v_mul_f32 v109, v109, v247\n"
    "v_mul_f32 v110, v110, v247\n"
    "v_mul_f32 v111, v111, v247\n"
    "v_cvt_pk_bf16_f32 v104, v104, v105\n"
    "v_cvt_pk_bf16_f32 v105, v106, v107\n"
    "v_cvt_pk_bf16_f32 v106, v108, v109\n"
    "v_cvt_pk_bf16_f32 v107, v110, v111\n"
    "buffer_store_dwordx4 v[104:107], v255, s[48:51], s93 offen sc0 sc1 nt\n"
    "v_accvgpr_read_b32 v112, a200\n"
    "v_accvgpr_read_b32 v113, a201\n"
    "v_accvgpr_read_b32 v114, a202\n"
    "v_accvgpr_read_b32 v115, a203\n"
    "v_accvgpr_read_b32 v116, a204\n"
    "v_accvgpr_read_b32 v117, a205\n"
    "v_accvgpr_read_b32 v118, a206\n"
    "v_accvgpr_read_b32 v119, a207\n"
    "v_mul_f32 v112, v112, v246\n"
    "v_mul_f32 v113, v113, v246\n"
    "v_mul_f32 v114, v114, v246\n"
    "v_mul_f32 v115, v115, v246\n"
    "v_mul_f32 v116, v116, v246\n"
    "v_mul_f32 v117, v117, v246\n"
    "v_mul_f32 v118, v118, v246\n"
    "v_mul_f32 v119, v119, v246\n"
    "v_cvt_pk_bf16_f32 v112, v112, v113\n"
    "v_cvt_pk_bf16_f32 v113, v114, v115\n"
    "v_cvt_pk_bf16_f32 v114, v116, v117\n"
    "v_cvt_pk_bf16_f32 v115, v118, v119\n"
    "buffer_store_dwordx4 v[112:115], v255, s[48:51], s92 offen offset:64 sc0 sc1 nt\n"
    "v_accvgpr_read_b32 v120, a232\n"
    "v_accvgpr_read_b32 v121, a233\n"
    "v_accvgpr_read_b32 v122, a234\n"
    "v_accvgpr_read_b32 v123, a235\n"
    "v_accvgpr_read_b32 v124, a236\n"
    "v_accvgpr_read_b32 v125, a237\n"
    "v_accvgpr_read_b32 v126, a238\n"
    "v_accvgpr_read_b32 v127, a239\n"
    "v_mul_f32 v120, v120, v247\n"
    "v_mul_f32 v121, v121, v247\n"
    "v_mul_f32 v122, v122, v247\n"
    "v_mul_f32 v123, v123, v247\n"
    "v_mul_f32 v124, v124, v247\n"
    "v_mul_f32 v125, v125, v247\n"
    "v_mul_f32 v126, v126, v247\n"
    "v_mul_f32 v127, v127, v247\n"
    "v_cvt_pk_bf16_f32 v120, v120, v121\n"
    "v_cvt_pk_bf16_f32 v121, v122, v123\n"
    "v_cvt_pk_bf16_f32 v122, v124, v125\n"
    "v_cvt_pk_bf16_f32 v123, v126, v127\n"
    "buffer_store_dwordx4 v[120:123], v255, s[48:51], s93 offen offset:64 sc0 sc1 nt\n"
    "v_accvgpr_read_b32 v96, a208\n"
    "v_accvgpr_read_b32 v97, a209\n"
    "v_accvgpr_read_b32 v98, a210\n"
    "v_accvgpr_read_b32 v99, a211\n"
    "v_accvgpr_read_b32 v100, a212\n"
    "v_accvgpr_read_b32 v101, a213\n"
    "v_accvgpr_read_b32 v102, a214\n"
    "v_accvgpr_read_b32 v103, a215\n"
    "v_mul_f32 v96, v96, v246\n"
    "v_mul_f32 v97, v97, v246\n"
    "v_mul_f32 v98, v98, v246\n"
    "v_mul_f32 v99, v99, v246\n"
    "v_mul_f32 v100, v100, v246\n"
    "v_mul_f32 v101, v101, v246\n"
    "v_mul_f32 v102, v102, v246\n"
    "v_mul_f32 v103, v103, v246\n"
    "v_cvt_pk_bf16_f32 v96, v96, v97\n"
    "v_cvt_pk_bf16_f32 v97, v98, v99\n"
    "v_cvt_pk_bf16_f32 v98, v100, v101\n"
    "v_cvt_pk_bf16_f32 v99, v102, v103\n"
    "buffer_store_dwordx4 v[96:99], v255, s[48:51], s92 offen offset:128 sc0 sc1 nt\n"
    "v_accvgpr_read_b32 v104, a240\n"
    "v_accvgpr_read_b32 v105, a241\n"
    "v_accvgpr_read_b32 v106, a242\n"
    "v_accvgpr_read_b32 v107, a243\n"
    "v_accvgpr_read_b32 v108, a244\n"
    "v_accvgpr_read_b32 v109, a245\n"
    "v_accvgpr_read_b32 v110, a246\n"
    "v_accvgpr_read_b32 v111, a247\n"
    "v_mul_f32 v104, v104, v247\n"
    "v_mul_f32 v105, v105, v247\n"
    "v_mul_f32 v106, v106, v247\n"
    "v_mul_f32 v107, v107, v247\n"
    "v_mul_f32 v108, v108, v247\n"
    "v_mul_f32 v109, v109, v247\n"
    "v_mul_f32 v110, v110, v247\n"
    "v_mul_f32 v111, v111, v247\n"
    "v_cvt_pk_bf16_f32 v104, v104, v105\n"
    "v_cvt_pk_bf16_f32 v105, v106, v107\n"
    "v_cvt_pk_bf16_f32 v106, v108, v109\n"
    "v_cvt_pk_bf16_f32 v107, v110, v111\n"
    "buffer_store_dwordx4 v[104:107], v255, s[48:51], s93 offen offset:128 sc0 sc1 nt\n"
    "v_accvgpr_read_b32 v112, a216\n"
    "v_accvgpr_read_b32 v113, a217\n"
    "v_accvgpr_read_b32 v114, a218\n"
    "v_accvgpr_read_b32 v115, a219\n"
    "v_accvgpr_read_b32 v116, a220\n"
    "v_accvgpr_read_b32 v117, a221\n"
    "v_accvgpr_read_b32 v118, a222\n"
    "v_accvgpr_read_b32 v119, a223\n"
    "v_mul_f32 v112, v112, v246\n"
    "v_mul_f32 v113, v113, v246\n"
    "v_mul_f32 v114, v114, v246\n"
    "v_mul_f32 v115, v115, v246\n"
    "v_mul_f32 v116, v116, v246\n"
    "v_mul_f32 v117, v117, v246\n"
    "v_mul_f32 v118, v118, v246\n"
    "v_mul_f32 v119, v119, v246\n"
    "v_cvt_pk_bf16_f32 v112, v112, v113\n"
    "v_cvt_pk_bf16_f32 v113, v114, v115\n"
    "v_cvt_pk_bf16_f32 v114, v116, v117\n"
    "v_cvt_pk_bf16_f32 v115, v118, v119\n"
    "buffer_store_dwordx4 v[112:115], v255, s[48:51], s92 offen offset:192 sc0 sc1 nt\n"
    "v_accvgpr_read_b32 v120, a248\n"
    "v_accvgpr_read_b32 v121, a249\n"
    "v_accvgpr_read_b32 v122, a250\n"
    "v_accvgpr_read_b32 v123, a251\n"
    "v_accvgpr_read_b32 v124, a252\n"
    "v_accvgpr_read_b32 v125, a253\n"
    "v_accvgpr_read_b32 v126, a254\n"
    "v_accvgpr_read_b32 v127, a255\n"
    "v_mul_f32 v120, v120, v247\n"
    "v_mul_f32 v121, v121, v247\n"
    "v_mul_f32 v122, v122, v247\n"
    "v_mul_f32 v123, v123, v247\n"
    "v_mul_f32 v124, v124, v247\n"
    "v_mul_f32 v125, v125, v247\n"
    "v_mul_f32 v126, v126, v247\n"
    "v_mul_f32 v127, v127, v247\n"
    "v_cvt_pk_bf16_f32 v120, v120, v121\n"
    "v_cvt_pk_bf16_f32 v121, v122, v123\n"
    "v_cvt_pk_bf16_f32 v122, v124, v125\n"
    "v_cvt_pk_bf16_f32 v123, v126, v127\n"
    "buffer_store_dwordx4 v[120:123], v255, s[48:51], s93 offen offset:192 sc0 sc1 nt\n"
    "s_or_b32 %[flag], s60, s61\n"
    "s_branch L_end_%=\n"
    "L_e3_%=:\n"
    "s_waitcnt lgkmcnt(0)\n"
    ".p2align 6\n"
    "L_loop3_%=:\n"
    "s_waitcnt lgkmcnt(4)\n"
    "v_add_f32 v246, v246, v217\n"
    "v_mfma_f32_16x16x32_bf16 v[192:195], v[128:131], v[0:3], v[248:251]\n"
    "v_mfma_f32_16x16x32_bf16 v[192:195], v[132:135], v[4:7], v[192:195]\n"
    "v_add_f32 v246, v246, v218\n"
    "v_mfma_f32_16x16x32_bf16 v[192:195], v[136:139], v[8:11], v[192:195]\n"
    "v_mfma_f32_16x16x32_bf16 v[192:195], v[140:143], v[12:15], v[192:195]\n"
    "v_add_f32 v246, v246, v219\n"
    "v_mfma_f32_16x16x32_bf16 v[196:199], v[140:143], v[28:31], v[248:251]\n"
    "v_mfma_f32_16x16x32_bf16 v[196:199], v[136:139], v[24:27], v[196:199]\n"
    "v_mfma_f32_16x16x32_bf16 v[196:199], v[132:135], v[20:23], v[196:199]\n"
    "s_nop 3\n"
    "v_exp_f32 v192, v192\n"
    "v_mfma_f32_16x16x32_bf16 v[196:199], v[128:131], v[16:19], v[196:199]\n"
    "v_add_f32 v240, v240, v192\n"
    "v_mfma_f32_16x16x32_bf16 a[192:195], v[160:163], v[232:235], a[192:195]\n"
    "v_exp_f32 v193, v193\n"
    "ds_read_b64_tr_b16 v[160:161], v253 offset:24768\n"
    "v_cvt_pk_bf16_f32 v224, v192, v193\n"
    "v_exp_f32 v194, v194\n"
    "v_mfma_f32_16x16x32_bf16 a[196:199], v[164:167], v[232:235], a[196:199]\n"
    "ds_read_b64_tr_b16 v[162:163], v253 offset:28896\n"
    "v_add_f32 v240, v240, v193\n"
    "v_mfma_f32_16x16x32_bf16 a[200:203], v[168:171], v[232:235], a[200:203]\n"
    "v_exp_f32 v195, v195\n"
    "ds_read_b64_tr_b16 v[164:165], v253 offset:24776\n"
    "v_cvt_pk_bf16_f32 v225, v194, v195\n"
    "v_exp_f32 v196, v196\n"
    "v_mfma_f32_16x16x32_bf16 a[204:207], v[172:175], v[232:235], a[204:207]\n"
    "s_waitcnt lgkmcnt(3)\n"
    "ds_read_b64_tr_b16 v[166:167], v253 offset:28904\n"
    "v_add_f32 v240, v240, v194\n"
    "v_mfma_f32_16x16x32_bf16 v[200:203], v[144:147], v[0:3], v[248:251]\n"
    "v_exp_f32 v197, v197\n"
    "v_mfma_f32_16x16x32_bf16 v[200:203], v[148:151], v[4:7], v[200:203]\n"
    "ds_read_b64_tr_b16 v[168:169], v253 offset:25024\n"
    "v_cvt_pk_bf16_f32 v228, v196, v197\n"
    "v_mfma_f32_16x16x32_bf16 v[200:203], v[152:155], v[8:11], v[200:203]\n"
    "v_exp_f32 v198, v198\n"
    "v_mfma_f32_16x16x32_bf16 v[200:203], v[156:159], v[12:15], v[200:203]\n"
    "ds_read_b64_tr_b16 v[170:171], v253 offset:29152\n"
    "v_add_f32 v240, v240, v195\n"
    "v_mfma_f32_16x16x32_bf16 v[204:207], v[156:159], v[28:31], v[248:251]\n"
    "v_exp_f32 v199, v199\n"
    "v_mfma_f32_16x16x32_bf16 v[204:207], v[152:155], v[24:27], v[204:207]\n"
    "ds_read_b64_tr_b16 v[172:173], v253 offset:25032\n"
    "v_cvt_pk_bf16_f32 v229, v198, v199\n"
    "v_mfma_f32_16x16x32_bf16 v[204:207], v[148:151], v[20:23], v[204:207]\n"
    "v_exp_f32 v200, v200\n"
    "v_mfma_f32_16x16x32_bf16 v[204:207], v[144:147], v[16:19], v[204:207]\n"
    "ds_read_b64_tr_b16 v[174:175], v253 offset:29160\n"
    "v_add_f32 v241, v241, v196\n"
    "v_mfma_f32_16x16x32_bf16 a[208:211], v[176:179], v[232:235], a[208:211]\n"
    "v_exp_f32 v201, v201\n"
    "ds_read_b64_tr_b16 v[176:177], v253 offset:25280\n"
    "v_cvt_pk_bf16_f32 v226, v200, v201\n"
    "v_exp_f32 v202, v202\n"
    "v_mfma_f32_16x16x32_bf16 a[212:215], v[180:183], v[232:235], a[212:215]\n"
    "ds_read_b64_tr_b16 v[178:179], v253 offset:29408\n"
    "v_add_f32 v241, v241, v197\n"
    "v_mfma_f32_16x16x32_bf16 a[216:219], v[184:187], v[232:235], a[216:219]\n"
    "v_exp_f32 v203, v203\n"
    "ds_read_b64_tr_b16 v[180:181], v253 offset:25288\n"
    "v_cvt_pk_bf16_f32 v227, v202, v203\n"
    "v_exp_f32 v204, v204\n"
    "v_mfma_f32_16x16x32_bf16 a[220:223], v[188:191], v[232:235], a[220:223]\n"
    "ds_read_b64_tr_b16 v[182:183], v253 offset:29416\n"
    "v_add_f32 v241, v241, v198\n"
    "v_mfma_f32_16x16x32_bf16 v[208:211], v[128:131], v[32:35], v[248:251]\n"
    "v_exp_f32 v205, v205\n"
    "v_mfma_f32_16x16x32_bf16 v[208:211], v[132:135], v[36:39], v[208:211]\n"
    "ds_read_b64_tr_b16 v[184:185], v253 offset:25536\n"
    "v_cvt_pk_bf16_f32 v230, v204, v205\n"
    "v_mfma_f32_16x16x32_bf16 v[208:211], v[136:139], v[40:43], v[208:211]\n"
    "v_exp_f32 v206, v206\n"
    "v_mfma_f32_16x16x32_bf16 v[208:211], v[140:143], v[44:47], v[208:211]\n"
    "ds_read_b64_tr_b16 v[186:187], v253 offset:29664\n"
    "v_add_f32 v241, v241, v199\n"
    "v_mfma_f32_16x16x32_bf16 v[212:215], v[140:143], v[60:63], v[248:251]\n"
    "v_exp_f32 v207, v207\n"
    "v_mfma_f32_16x16x32_bf16 v[212:215], v[136:139], v[56:59], v[212:215]\n"
    "ds_read_b64_tr_b16 v[188:189], v253 offset:25544\n"
    "v_cvt_pk_bf16_f32 v231, v206, v207\n"
    "v_mfma_f32_16x16x32_bf16 v[212:215], v[132:135], v[52:55], v[212:215]\n"
    "v_exp_f32 v208, v208\n"
    "v_mfma_f32_16x16x32_bf16 v[212:215], v[128:131], v[48:51], v[212:215]\n"
    "s_waitcnt lgkmcnt(13)\n"
    "ds_read_b64_tr_b16 v[190:191], v253 offset:29672\n"
    "v_add_f32 v240, v240, v200\n"
    "v_mfma_f32_16x16x32_bf16 a[0:3], v[160:163], v[224:227], a[0:3]\n"
    "v_exp_f32 v209, v209\n"
    "v_mfma_f32_16x16x32_bf16 a[32:35], v[160:163], v[228:231], a[32:35]\n"
    "s_waitcnt lgkmcnt(12)\n"
    "v_cvt_pk_bf16_f32 v232, v208, v209\n"
    "v_add_f32 v240, v240, v201\n"
    "v_mfma_f32_16x16x32_bf16 a[36:39], v[164:167], v[228:231], a[36:39]\n"
    "v_exp_f32 v210, v210\n"
    "v_mfma_f32_16x16x32_bf16 a[4:7], v[164:167], v[224:227], a[4:7]\n"
    "s_waitcnt lgkmcnt(10)\n"
    "v_add_f32 v240, v240, v202\n"
    "v_add_f32 v241, v241, v204\n"
    "v_mfma_f32_16x16x32_bf16 a[8:11], v[168:171], v[224:227], a[8:11]\n"
    "v_exp_f32 v211, v211\n"
    "v_mfma_f32_16x16x32_bf16 a[40:43], v[168:171], v[228:231], a[40:43]\n"
    "s_waitcnt lgkmcnt(8)\n"
    "v_cvt_pk_bf16_f32 v233, v210, v211\n"
    "v_add_f32 v240, v240, v203\n"
    "v_mfma_f32_16x16x32_bf16 a[44:47], v[172:175], v[228:231], a[44:47]\n"
    "s_add_u32 m0, s54, 8256\n"
    "v_exp_f32 v212, v212\n"
    "v_mfma_f32_16x16x32_bf16 a[12:15], v[172:175], v[224:227], a[12:15]\n"
    "buffer_load_dwordx4 v254, s[40:43], s53 offen nt lds\n"
    "v_mfma_f32_16x16x32_bf16 v[216:219], v[144:147], v[32:35], v[248:251]\n"
    "v_exp_f32 v213, v213\n"
    "v_mfma_f32_16x16x32_bf16 v[216:219], v[148:151], v[36:39], v[216:219]\n"
    "v_cvt_pk_bf16_f32 v236, v212, v213\n"
    "v_add_f32 v241, v241, v205\n"
    "v_mfma_f32_16x16x32_bf16 v[216:219], v[152:155], v[40:43], v[216:219]\n"
    "v_exp_f32 v214, v214\n"
    "v_mfma_f32_16x16x32_bf16 v[216:219], v[156:159], v[44:47], v[216:219]\n"
    "v_add_f32 v241, v241, v206\n"
    "v_add_f32 v242, v242, v208\n"
    "v_mfma_f32_16x16x32_bf16 v[220:223], v[156:159], v[60:63], v[248:251]\n"
    "v_exp_f32 v215, v215\n"
    "v_mfma_f32_16x16x32_bf16 v[220:223], v[152:155], v[56:59], v[220:223]\n"
    "v_cvt_pk_bf16_f32 v237, v214, v215\n"
    "v_add_f32 v241, v241, v207\n"
    "v_mfma_f32_16x16x32_bf16 v[220:223], v[148:151], v[52:55], v[220:223]\n"
    "v_exp_f32 v216, v216\n"
    "v_mfma_f32_16x16x32_bf16 v[220:223], v[144:147], v[48:51], v[220:223]\n"
    "s_waitcnt lgkmcnt(6)\n"
    "v_add_f32 v242, v242, v209\n"
    "v_add_f32 v243, v243, v212\n"
    "v_mfma_f32_16x16x32_bf16 a[16:19], v[176:179], v[224:227], a[16:19]\n"
    "v_exp_f32 v217, v217\n"
    "v_mfma_f32_16x16x32_bf16 a[48:51], v[176:179], v[228:231], a[48:51]\n"
    "s_waitcnt lgkmcnt(4)\n"
    "v_cvt_pk_bf16_f32 v234, v216, v217\n"
    "v_add_f32 v242, v242, v210\n"
    "v_mfma_f32_16x16x32_bf16 a[52:55], v[180:183], v[228:231], a[52:55]\n"
    "v_exp_f32 v218, v218\n"
    "v_mfma_f32_16x16x32_bf16 a[20:23], v[180:183], v[224:227], a[20:23]\n"
    "s_waitcnt lgkmcnt(2)\n"
    "buffer_load_dwordx4 v254, s[40:43], s53 offen offset:1024 nt lds\n"
    "v_mfma_f32_16x16x32_bf16 a[24:27], v[184:187], v[224:227], a[24:27]\n"
    "v_exp_f32 v219, v219\n"
    "v_mfma_f32_16x16x32_bf16 a[56:59], v[184:187], v[228:231], a[56:59]\n"
    "s_waitcnt lgkmcnt(0)\n"
    "v_cvt_pk_bf16_f32 v235, v218, v219\n"
    "v_add_f32 v242, v242, v211\n"
    "v_mfma_f32_16x16x32_bf16 a[60:63], v[188:191], v[228:231], a[60:63]\n"
    "v_exp_f32 v220, v220\n"
    "v_mfma_f32_16x16x32_bf16 a[28:31], v[188:191], v[224:227], a[28:31]\n"
    "v_add_f32 v243, v243, v213\n"
    "v_add_f32 v242, v242, v216\n"
    "v_mfma_f32_16x16x32_bf16 v[192:195], v[128:131], v[64:67], v[248:251]\n"
    "v_exp_f32 v221, v221\n"
    "v_mfma_f32_16x16x32_bf16 v[192:195], v[132:135], v[68:71], v[192:195]\n"
    "v_cvt_pk_bf16_f32 v238, v220, v221\n"
    "v_add_f32 v243, v243, v214\n"
    "v_mfma_f32_16x16x32_bf16 v[192:195], v[136:139], v[72:75], v[192:195]\n"
    "v_exp_f32 v222, v222\n"
    "v_mfma_f32_16x16x32_bf16 v[192:195], v[140:143], v[76:79], v[192:195]\n"
    "v_add_f32 v243, v243, v215\n"
    "v_add_f32 v242, v242, v217\n"
    "v_mfma_f32_16x16x32_bf16 v[196:199], v[140:143], v[92:95], v[248:251]\n"
    "v_exp_f32 v223, v223\n"
    "v_mfma_f32_16x16x32_bf16 v[196:199], v[136:139], v[88:91], v[196:199]\n"
    "v_cvt_pk_bf16_f32 v239, v222, v223\n"
    "v_add_f32 v242, v242, v218\n"
    "v_mfma_f32_16x16x32_bf16 v[196:199], v[132:135], v[84:87], v[196:199]\n"
    "v_exp_f32 v192, v192\n"
    "v_mfma_f32_16x16x32_bf16 v[196:199], v[128:131], v[80:83], v[196:199]\n"
    "v_add_f32 v242, v242, v219\n"
    "v_add_f32 v243, v243, v220\n"
    "v_mfma_f32_16x16x32_bf16 a[64:67], v[160:163], v[232:235], a[64:67]\n"
    "v_exp_f32 v193, v193\n"
    "v_mfma_f32_16x16x32_bf16 a[96:99], v[160:163], v[236:239], a[96:99]\n"
    "v_cvt_pk_bf16_f32 v224, v192, v193\n"
    "v_add_f32 v243, v243, v221\n"
    "v_mfma_f32_16x16x32_bf16 a[100:103], v[164:167], v[236:239], a[100:103]\n"
    "s_add_u32 m0, s55, 66048\n"
    "v_exp_f32 v194, v194\n"
    "v_mfma_f32_16x16x32_bf16 a[68:71], v[164:167], v[232:235], a[68:71]\n"
    "buffer_load_dwordx4 v254, s[44:47], s53 offen nt lds\n"
    "v_mfma_f32_16x16x32_bf16 a[72:75], v[168:171], v[232:235], a[72:75]\n"
    "v_exp_f32 v195, v195\n"
    "v_mfma_f32_16x16x32_bf16 a[104:107], v[168:171], v[236:239], a[104:107]\n"
    "v_cvt_pk_bf16_f32 v225, v194, v195\n"
    "v_add_f32 v243, v243, v222\n"
    "v_mfma_f32_16x16x32_bf16 a[108:111], v[172:175], v[236:239], a[108:111]\n"
    "v_exp_f32 v196, v196\n"
    "v_mfma_f32_16x16x32_bf16 a[76:79], v[172:175], v[232:235], a[76:79]\n"
    "v_add_f32 v243, v243, v223\n"
    "v_add_f32 v244, v244, v192\n"
    "v_mfma_f32_16x16x32_bf16 v[200:203], v[144:147], v[64:67], v[248:251]\n"
    "v_exp_f32 v197, v197\n"
    "v_mfma_f32_16x16x32_bf16 v[200:203], v[148:151], v[68:71], v[200:203]\n"
    "v_cvt_pk_bf16_f32 v228, v196, v197\n"
    "v_add_f32 v244, v244, v193\n"
    "v_mfma_f32_16x16x32_bf16 v[200:203], v[152:155], v[72:75], v[200:203]\n"
    "v_exp_f32 v198, v198\n"
    "v_mfma_f32_16x16x32_bf16 v[200:203], v[156:159], v[76:79], v[200:203]\n"
    "v_add_f32 v244, v244, v194\n"
    "v_add_f32 v245, v245, v196\n"
    "v_mfma_f32_16x16x32_bf16 v[204:207], v[156:159], v[92:95], v[248:251]\n"
    "v_exp_f32 v199, v199\n"
    "v_mfma_f32_16x16x32_bf16 v[204:207], v[152:155], v[88:91], v[204:207]\n"
    "v_cvt_pk_bf16_f32 v229, v198, v199\n"
    "v_add_f32 v244, v244, v195\n"
    "v_mfma_f32_16x16x32_bf16 v[204:207], v[148:151], v[84:87], v[204:207]\n"
    "v_exp_f32 v200, v200\n"
    "v_mfma_f32_16x16x32_bf16 v[204:207], v[144:147], v[80:83], v[204:207]\n"
    "v_add_f32 v245, v245, v197\n"
    "v_add_f32 v244, v244, v200\n"
    "v_mfma_f32_16x16x32_bf16 a[80:83], v[176:179], v[232:235], a[80:83]\n"
    "v_exp_f32 v201, v201\n"
    "v_mfma_f32_16x16x32_bf16 a[112:115], v[176:179], v[236:239], a[112:115]\n"
    "v_cvt_pk_bf16_f32 v226, v200, v201\n"
    "v_add_f32 v245, v245, v198\n"
    "v_mfma_f32_16x16x32_bf16 a[116:119], v[180:183], v[236:239], a[116:119]\n"
    "s_add_u32 m0, s55, 66056\n"
    "v_exp_f32 v202, v202\n"
    "v_mfma_f32_16x16x32_bf16 a[84:87], v[180:183], v[232:235], a[84:87]\n"
    "buffer_load_dwordx4 v254, s[44:47], s53 offen offset:1024 nt lds\n"
    "v_mfma_f32_16x16x32_bf16 a[88:91], v[184:187], v[232:235], a[88:91]\n"
    "s_add_u32 s53, s53, 8192\n"
    "v_exp_f32 v203, v203\n"
    "v_mfma_f32_16x16x32_bf16 a[120:123], v[184:187], v[236:239], a[120:123]\n"
    "v_cvt_pk_bf16_f32 v227, v202, v203\n"
    "v_add_f32 v245, v245, v199\n"
    "v_mfma_f32_16x16x32_bf16 a[124:127], v[188:191], v[236:239], a[124:127]\n"
    "v_exp_f32 v204, v204\n"
    "v_mfma_f32_16x16x32_bf16 a[92:95], v[188:191], v[232:235], a[92:95]\n"
    "v_add_f32 v244, v244, v201\n"
    "v_add_f32 v245, v245, v204\n"
    "v_mfma_f32_16x16x32_bf16 v[208:211], v[128:131], v[96:99], v[248:251]\n"
    "v_exp_f32 v205, v205\n"
    "v_mfma_f32_16x16x32_bf16 v[208:211], v[132:135], v[100:103], v[208:211]\n"
    "v_cvt_pk_bf16_f32 v230, v204, v205\n"
    "v_add_f32 v244, v244, v202\n"
    "v_mfma_f32_16x16x32_bf16 v[208:211], v[136:139], v[104:107], v[208:211]\n"
    "v_exp_f32 v206, v206\n"
    "v_mfma_f32_16x16x32_bf16 v[208:211], v[140:143], v[108:111], v[208:211]\n"
    "v_add_f32 v244, v244, v203\n"
    "v_add_f32 v245, v245, v205\n"
    "v_exp_f32 v207, v207\n"
    "s_nop 0\n"
    "v_cvt_pk_bf16_f32 v231, v206, v207\n"
    "v_add_f32 v245, v245, v206\n"
    "s_nop 1\n"
    "v_exp_f32 v208, v208\n"
    "ds_read_b128 v[140:143], v252 offset:33792\n"
    "v_add_f32 v245, v245, v207\n"
    "v_mfma_f32_16x16x32_bf16 a[128:131], v[160:163], v[224:227], a[128:131]\n"
    "v_exp_f32 v209, v209\n"
    "v_mfma_f32_16x16x32_bf16 a[160:163], v[160:163], v[228:231], a[160:163]\n"
    "ds_read_b128 v[136:139], v252 offset:33536\n"
    "v_cvt_pk_bf16_f32 v232, v208, v209\n"
    "v_mfma_f32_16x16x32_bf16 a[164:167], v[164:167], v[228:231], a[164:167]\n"
    "v_exp_f32 v210, v210\n"
    "v_mfma_f32_16x16x32_bf16 a[132:135], v[164:167], v[224:227], a[132:135]\n"
    "ds_read_b128 v[132:135], v252 offset:33280\n"
    "v_add_f32 v246, v246, v208\n"
    "v_mfma_f32_16x16x32_bf16 a[136:139], v[168:171], v[224:227], a[136:139]\n"
    "v_exp_f32 v211, v211\n"
    "v_mfma_f32_16x16x32_bf16 a[168:171], v[168:171], v[228:231], a[168:171]\n"
    "ds_read_b128 v[128:131], v252 offset:33024\n"
    "v_cvt_pk_bf16_f32 v233, v210, v211\n"
    "v_mfma_f32_16x16x32_bf16 a[172:175], v[172:175], v[228:231], a[172:175]\n"
    "v_mfma_f32_16x16x32_bf16 a[140:143], v[172:175], v[224:227], a[140:143]\n"
    "v_add_f32 v246, v246, v209\n"
    "v_mfma_f32_16x16x32_bf16 v[216:219], v[144:147], v[96:99], v[248:251]\n"
    "v_mfma_f32_16x16x32_bf16 v[216:219], v[148:151], v[100:103], v[216:219]\n"
    "v_add_f32 v246, v246, v210\n"
    "v_mfma_f32_16x16x32_bf16 v[216:219], v[152:155], v[104:107], v[216:219]\n"
    "v_mfma_f32_16x16x32_bf16 v[216:219], v[156:159], v[108:111], v[216:219]\n"
    "v_add_f32 v246, v246, v211\n"
    "s_nop 6\n"
    "v_exp_f32 v216, v216\n"
    "ds_read_b128 v[156:159], v252 offset:37920\n"
    "v_mfma_f32_16x16x32_bf16 a[144:147], v[176:179], v[224:227], a[144:147]\n"
    "v_exp_f32 v217, v217\n"
    "v_mfma_f32_16x16x32_bf16 a[176:179], v[176:179], v[228:231], a[176:179]\n"
    "ds_read_b128 v[152:155], v252 offset:37664\n"
    "v_cvt_pk_bf16_f32 v234, v216, v217\n"
    "v_mfma_f32_16x16x32_bf16 a[180:183], v[180:183], v[228:231], a[180:183]\n"
    "v_exp_f32 v218, v218\n"
    "v_mfma_f32_16x16x32_bf16 a[148:151], v[180:183], v[224:227], a[148:151]\n"
    "ds_read_b128 v[148:151], v252 offset:37408\n"
    "v_add_f32 v246, v246, v216\n"
    "v_mfma_f32_16x16x32_bf16 a[152:155], v[184:187], v[224:227], a[152:155]\n"
    "v_exp_f32 v219, v219\n"
    "v_mfma_f32_16x16x32_bf16 a[184:187], v[184:187], v[228:231], a[184:187]\n"
    "ds_read_b128 v[144:147], v252 offset:37152\n"
    "v_cvt_pk_bf16_f32 v235, v218, v219\n"
    "v_mfma_f32_16x16x32_bf16 a[188:191], v[188:191], v[228:231], a[188:191]\n"
    "s_sub_u32 s58, s58, 1\n"
    "v_mfma_f32_16x16x32_bf16 a[156:159], v[188:191], v[224:227], a[156:159]\n"
    "s_cbranch_scc1 L_x3_%=\n"
    "s_waitcnt lgkmcnt(4)\n"
    "v_add_f32 v246, v246, v217\n"
    "v_mfma_f32_16x16x32_bf16 v[192:195], v[128:131], v[0:3], v[248:251]\n"
    "v_mfma_f32_16x16x32_bf16 v[192:195], v[132:135], v[4:7], v[192:195]\n"
    "v_add_f32 v246, v246, v218\n"
    "v_mfma_f32_16x16x32_bf16 v[192:195], v[136:139], v[8:11], v[192:195]\n"
    "v_mfma_f32_16x16x32_bf16 v[192:195], v[140:143], v[12:15], v[192:195]\n"
    "v_add_f32 v246, v246, v219\n"
    "v_mfma_f32_16x16x32_bf16 v[196:199], v[140:143], v[28:31], v[248:251]\n"
    "v_mfma_f32_16x16x32_bf16 v[196:199], v[136:139], v[24:27], v[196:199]\n"
    "v_mfma_f32_16x16x32_bf16 v[196:199], v[132:135], v[20:23], v[196:199]\n"
    "s_nop 3\n"
    "v_exp_f32 v192, v192\n"
    "v_mfma_f32_16x16x32_bf16 v[196:199], v[128:131], v[16:19], v[196:199]\n"
    "v_add_f32 v240, v240, v192\n"
    "v_mfma_f32_16x16x32_bf16 a[192:195], v[160:163], v[232:235], a[192:195]\n"
    "v_exp_f32 v193, v193\n"
    "ds_read_b64_tr_b16 v[160:161], v253 offset:33024\n"
    "v_cvt_pk_bf16_f32 v224, v192, v193\n"
    "v_exp_f32 v194, v194\n"
    "v_mfma_f32_16x16x32_bf16 a[196:199], v[164:167], v[232:235], a[196:199]\n"
    "ds_read_b64_tr_b16 v[162:163], v253 offset:37152\n"
    "v_add_f32 v240, v240, v193\n"
    "v_mfma_f32_16x16x32_bf16 a[200:203], v[168:171], v[232:235], a[200:203]\n"
    "v_exp_f32 v195, v195\n"
    "ds_read_b64_tr_b16 v[164:165], v253 offset:33032\n"
    "v_cvt_pk_bf16_f32 v225, v194, v195\n"
    "v_exp_f32 v196, v196\n"
    "v_mfma_f32_16x16x32_bf16 a[204:207], v[172:175], v[232:235], a[204:207]\n"
    "s_waitcnt lgkmcnt(3)\n"
    "ds_read_b64_tr_b16 v[166:167], v253 offset:37160\n"
    "v_add_f32 v240, v240, v194\n"
    "v_mfma_f32_16x16x32_bf16 v[200:203], v[144:147], v[0:3], v[248:251]\n"
    "v_exp_f32 v197, v197\n"
    "v_mfma_f32_16x16x32_bf16 v[200:203], v[148:151], v[4:7], v[200:203]\n"
    "ds_read_b64_tr_b16 v[168:169], v253 offset:33280\n"
    "v_cvt_pk_bf16_f32 v228, v196, v197\n"
    "v_mfma_f32_16x16x32_bf16 v[200:203], v[152:155], v[8:11], v[200:203]\n"
    "v_exp_f32 v198, v198\n"
    "v_mfma_f32_16x16x32_bf16 v[200:203], v[156:159], v[12:15], v[200:203]\n"
    "ds_read_b64_tr_b16 v[170:171], v253 offset:37408\n"
    "v_add_f32 v240, v240, v195\n"
    "v_mfma_f32_16x16x32_bf16 v[204:207], v[156:159], v[28:31], v[248:251]\n"
    "v_exp_f32 v199, v199\n"
    "v_mfma_f32_16x16x32_bf16 v[204:207], v[152:155], v[24:27], v[204:207]\n"
    "ds_read_b64_tr_b16 v[172:173], v253 offset:33288\n"
    "v_cvt_pk_bf16_f32 v229, v198, v199\n"
    "v_mfma_f32_16x16x32_bf16 v[204:207], v[148:151], v[20:23], v[204:207]\n"
    "v_exp_f32 v200, v200\n"
    "v_mfma_f32_16x16x32_bf16 v[204:207], v[144:147], v[16:19], v[204:207]\n"
    "ds_read_b64_tr_b16 v[174:175], v253 offset:37416\n"
    "v_add_f32 v241, v241, v196\n"
    "v_mfma_f32_16x16x32_bf16 a[208:211], v[176:179], v[232:235], a[208:211]\n"
    "v_exp_f32 v201, v201\n"
    "ds_read_b64_tr_b16 v[176:177], v253 offset:33536\n"
    "v_cvt_pk_bf16_f32 v226, v200, v201\n"
    "v_exp_f32 v202, v202\n"
    "v_mfma_f32_16x16x32_bf16 a[212:215], v[180:183], v[232:235], a[212:215]\n"
    "ds_read_b64_tr_b16 v[178:179], v253 offset:37664\n"
    "v_add_f32 v241, v241, v197\n"
    "v_mfma_f32_16x16x32_bf16 a[216:219], v[184:187], v[232:235], a[216:219]\n"
    "v_exp_f32 v203, v203\n"
    "ds_read_b64_tr_b16 v[180:181], v253 offset:33544\n"
    "v_cvt_pk_bf16_f32 v227, v202, v203\n"
    "v_exp_f32 v204, v204\n"
    "v_mfma_f32_16x16x32_bf16 a[220:223], v[188:191], v[232:235], a[220:223]\n"
    "ds_read_b64_tr_b16 v[182:183], v253 offset:37672\n"
    "v_add_f32 v241, v241, v198\n"
    "v_mfma_f32_16x16x32_bf16 v[208:211], v[128:131], v[32:35], v[248:251]\n"
    "v_exp_f32 v205, v205\n"
    "v_mfma_f32_16x16x32_bf16 v[208:211], v[132:135], v[36:39], v[208:211]\n"
    "ds_read_b64_tr_b16 v[184:185], v253 offset:33792\n"
    "v_cvt_pk_bf16_f32 v230, v204, v205\n"
    "v_mfma_f32_16x16x32_bf16 v[208:211], v[136:139], v[40:43], v[208:211]\n"
    "v_exp_f32 v206, v206\n"
    "v_mfma_f32_16x16x32_bf16 v[208:211], v[140:143], v[44:47], v[208:211]\n"
    "ds_read_b64_tr_b16 v[186:187], v253 offset:37920\n"
    "v_add_f32 v241, v241, v199\n"
    "v_mfma_f32_16x16x32_bf16 v[212:215], v[140:143], v[60:63], v[248:251]\n"
    "v_exp_f32 v207, v207\n"
    "v_mfma_f32_16x16x32_bf16 v[212:215], v[136:139], v[56:59], v[212:215]\n"
    "ds_read_b64_tr_b16 v[188:189], v253 offset:33800\n"
    "v_cvt_pk_bf16_f32 v231, v206, v207\n"
    "v_mfma_f32_16x16x32_bf16 v[212:215], v[132:135], v[52:55], v[212:215]\n"
    "v_exp_f32 v208, v208\n"
    "v_mfma_f32_16x16x32_bf16 v[212:215], v[128:131], v[48:51], v[212:215]\n"
    "s_waitcnt lgkmcnt(13)\n"
    "ds_read_b64_tr_b16 v[190:191], v253 offset:37928\n"
    "v_add_f32 v240, v240, v200\n"
    "v_mfma_f32_16x16x32_bf16 a[0:3], v[160:163], v[224:227], a[0:3]\n"
    "v_exp_f32 v209, v209\n"
    "v_mfma_f32_16x16x32_bf16 a[32:35], v[160:163], v[228:231], a[32:35]\n"
    "s_waitcnt lgkmcnt(12)\n"
    "v_cvt_pk_bf16_f32 v232, v208, v209\n"
    "v_add_f32 v240, v240, v201\n"
    "v_mfma_f32_16x16x32_bf16 a[36:39], v[164:167], v[228:231], a[36:39]\n"
    "v_exp_f32 v210, v210\n"
    "v_mfma_f32_16x16x32_bf16 a[4:7], v[164:167], v[224:227], a[4:7]\n"
    "s_waitcnt lgkmcnt(10)\n"
    "v_add_f32 v240, v240, v202\n"
    "v_add_f32 v241, v241, v204\n"
    "v_mfma_f32_16x16x32_bf16 a[8:11], v[168:171], v[224:227], a[8:11]\n"
    "v_exp_f32 v211, v211\n"
    "v_mfma_f32_16x16x32_bf16 a[40:43], v[168:171], v[228:231], a[40:43]\n"
    "s_waitcnt lgkmcnt(8)\n"
    "v_cvt_pk_bf16_f32 v233, v210, v211\n"
    "v_add_f32 v240, v240, v203\n"
    "v_mfma_f32_16x16x32_bf16 a[44:47], v[172:175], v[228:231], a[44:47]\n"
    "s_add_u32 m0, s54, 16512\n"
    "v_exp_f32 v212, v212\n"
    "v_mfma_f32_16x16x32_bf16 a[12:15], v[172:175], v[224:227], a[12:15]\n"
    "buffer_load_dwordx4 v254, s[40:43], s53 offen nt lds\n"
    "v_mfma_f32_16x16x32_bf16 v[216:219], v[144:147], v[32:35], v[248:251]\n"
    "v_exp_f32 v213, v213\n"
    "v_mfma_f32_16x16x32_bf16 v[216:219], v[148:151], v[36:39], v[216:219]\n"
    "v_cvt_pk_bf16_f32 v236, v212, v213\n"
    "v_add_f32 v241, v241, v205\n"
    "v_mfma_f32_16x16x32_bf16 v[216:219], v[152:155], v[40:43], v[216:219]\n"
    "v_exp_f32 v214, v214\n"
    "v_mfma_f32_16x16x32_bf16 v[216:219], v[156:159], v[44:47], v[216:219]\n"
    "v_add_f32 v241, v241, v206\n"
    "v_add_f32 v242, v242, v208\n"
    "v_mfma_f32_16x16x32_bf16 v[220:223], v[156:159], v[60:63], v[248:251]\n"
    "v_exp_f32 v215, v215\n"
    "v_mfma_f32_16x16x32_bf16 v[220:223], v[152:155], v[56:59], v[220:223]\n"
    "v_cvt_pk_bf16_f32 v237, v214, v215\n"
    "v_add_f32 v241, v241, v207\n"
    "v_mfma_f32_16x16x32_bf16 v[220:223], v[148:151], v[52:55], v[220:223]\n"
    "v_exp_f32 v216, v216\n"
    "v_mfma_f32_16x16x32_bf16 v[220:223], v[144:147], v[48:51], v[220:223]\n"
    "s_waitcnt lgkmcnt(6)\n"
    "v_add_f32 v242, v242, v209\n"
    "v_add_f32 v243, v243, v212\n"
    "v_mfma_f32_16x16x32_bf16 a[16:19], v[176:179], v[224:227], a[16:19]\n"
    "v_exp_f32 v217, v217\n"
    "v_mfma_f32_16x16x32_bf16 a[48:51], v[176:179], v[228:231], a[48:51]\n"
    "s_waitcnt lgkmcnt(4)\n"
    "v_cvt_pk_bf16_f32 v234, v216, v217\n"
    "v_add_f32 v242, v242, v210\n"
    "v_mfma_f32_16x16x32_bf16 a[52:55], v[180:183], v[228:231], a[52:55]\n"
    "v_exp_f32 v218, v218\n"
    "v_mfma_f32_16x16x32_bf16 a[20:23], v[180:183], v[224:227], a[20:23]\n"
    "s_waitcnt lgkmcnt(2)\n"
    "buffer_load_dwordx4 v254, s[40:43], s53 offen offset:1024 nt lds\n"
    "v_mfma_f32_16x16x32_bf16 a[24:27], v[184:187], v[224:227], a[24:27]\n"
    "v_exp_f32 v219, v219\n"
    "v_mfma_f32_16x16x32_bf16 a[56:59], v[184:187], v[228:231], a[56:59]\n"
    "s_waitcnt lgkmcnt(0)\n"
    "v_cvt_pk_bf16_f32 v235, v218, v219\n"
    "v_add_f32 v242, v242, v211\n"
    "v_mfma_f32_16x16x32_bf16 a[60:63], v[188:191], v[228:231], a[60:63]\n"
    "v_exp_f32 v220, v220\n"
    "v_mfma_f32_16x16x32_bf16 a[28:31], v[188:191], v[224:227], a[28:31]\n"
    "v_add_f32 v243, v243, v213\n"
    "v_add_f32 v242, v242, v216\n"
    "v_mfma_f32_16x16x32_bf16 v[192:195], v[128:131], v[64:67], v[248:251]\n"
    "v_exp_f32 v221, v221\n"
    "v_mfma_f32_16x16x32_bf16 v[192:195], v[132:135], v[68:71], v[192:195]\n"
    "v_cvt_pk_bf16_f32 v238, v220, v221\n"
    "v_add_f32 v243, v243, v214\n"
    "v_mfma_f32_16x16x32_bf16 v[192:195], v[136:139], v[72:75], v[192:195]\n"
    "v_exp_f32 v222, v222\n"
    "v_mfma_f32_16x16x32_bf16 v[192:195], v[140:143], v[76:79], v[192:195]\n"
    "v_add_f32 v243, v243, v215\n"
    "v_add_f32 v242, v242, v217\n"
    "v_mfma_f32_16x16x32_bf16 v[196:199], v[140:143], v[92:95], v[248:251]\n"
    "v_exp_f32 v223, v223\n"
    "v_mfma_f32_16x16x32_bf16 v[196:199], v[136:139], v[88:91], v[196:199]\n"
    "v_cvt_pk_bf16_f32 v239, v222, v223\n"
    "v_add_f32 v242, v242, v218\n"
    "v_mfma_f32_16x16x32_bf16 v[196:199], v[132:135], v[84:87], v[196:199]\n"
    "v_exp_f32 v192, v192\n"
    "v_mfma_f32_16x16x32_bf16 v[196:199], v[128:131], v[80:83], v[196:199]\n"
    "v_add_f32 v242, v242, v219\n"
    "v_add_f32 v243, v243, v220\n"
    "v_mfma_f32_16x16x32_bf16 a[64:67], v[160:163], v[232:235], a[64:67]\n"
    "v_exp_f32 v193, v193\n"
    "v_mfma_f32_16x16x32_bf16 a[96:99], v[160:163], v[236:239], a[96:99]\n"
    "v_cvt_pk_bf16_f32 v224, v192, v193\n"
    "v_add_f32 v243, v243, v221\n"
    "v_mfma_f32_16x16x32_bf16 a[100:103], v[164:167], v[236:239], a[100:103]\n"
    "s_add_u32 m0, s55, 74304\n"
    "v_exp_f32 v194, v194\n"
    "v_mfma_f32_16x16x32_bf16 a[68:71], v[164:167], v[232:235], a[68:71]\n"
    "buffer_load_dwordx4 v254, s[44:47], s53 offen nt lds\n"
    "v_mfma_f32_16x16x32_bf16 a[72:75], v[168:171], v[232:235], a[72:75]\n"
    "v_exp_f32 v195, v195\n"
    "v_mfma_f32_16x16x32_bf16 a[104:107], v[168:171], v[236:239], a[104:107]\n"
    "v_cvt_pk_bf16_f32 v225, v194, v195\n"
    "v_add_f32 v243, v243, v222\n"
    "v_mfma_f32_16x16x32_bf16 a[108:111], v[172:175], v[236:239], a[108:111]\n"
    "v_exp_f32 v196, v196\n"
    "v_mfma_f32_16x16x32_bf16 a[76:79], v[172:175], v[232:235], a[76:79]\n"
    "v_add_f32 v243, v243, v223\n"
    "v_add_f32 v244, v244, v192\n"
    "v_mfma_f32_16x16x32_bf16 v[200:203], v[144:147], v[64:67], v[248:251]\n"
    "v_exp_f32 v197, v197\n"
    "v_mfma_f32_16x16x32_bf16 v[200:203], v[148:151], v[68:71], v[200:203]\n"
    "v_cvt_pk_bf16_f32 v228, v196, v197\n"
    "v_add_f32 v244, v244, v193\n"
    "v_mfma_f32_16x16x32_bf16 v[200:203], v[152:155], v[72:75], v[200:203]\n"
    "v_exp_f32 v198, v198\n"
    "v_mfma_f32_16x16x32_bf16 v[200:203], v[156:159], v[76:79], v[200:203]\n"
    "v_add_f32 v244, v244, v194\n"
    "v_add_f32 v245, v245, v196\n"
    "v_mfma_f32_16x16x32_bf16 v[204:207], v[156:159], v[92:95], v[248:251]\n"
    "v_exp_f32 v199, v199\n"
    "v_mfma_f32_16x16x32_bf16 v[204:207], v[152:155], v[88:91], v[204:207]\n"
    "v_cvt_pk_bf16_f32 v229, v198, v199\n"
    "v_add_f32 v244, v244, v195\n"
    "v_mfma_f32_16x16x32_bf16 v[204:207], v[148:151], v[84:87], v[204:207]\n"
    "v_exp_f32 v200, v200\n"
    "v_mfma_f32_16x16x32_bf16 v[204:207], v[144:147], v[80:83], v[204:207]\n"
    "v_add_f32 v245, v245, v197\n"
    "v_add_f32 v244, v244, v200\n"
    "v_mfma_f32_16x16x32_bf16 a[80:83], v[176:179], v[232:235], a[80:83]\n"
    "v_exp_f32 v201, v201\n"
    "v_mfma_f32_16x16x32_bf16 a[112:115], v[176:179], v[236:239], a[112:115]\n"
    "v_cvt_pk_bf16_f32 v226, v200, v201\n"
    "v_add_f32 v245, v245, v198\n"
    "v_mfma_f32_16x16x32_bf16 a[116:119], v[180:183], v[236:239], a[116:119]\n"
    "s_add_u32 m0, s55, 74312\n"
    "v_exp_f32 v202, v202\n"
    "v_mfma_f32_16x16x32_bf16 a[84:87], v[180:183], v[232:235], a[84:87]\n"
    "buffer_load_dwordx4 v254, s[44:47], s53 offen offset:1024 nt lds\n"
    "v_mfma_f32_16x16x32_bf16 a[88:91], v[184:187], v[232:235], a[88:91]\n"
    "s_add_u32 s53, s53, 8192\n"
    "v_exp_f32 v203, v203\n"
    "v_mfma_f32_16x16x32_bf16 a[120:123], v[184:187], v[236:239], a[120:123]\n"
    "v_cvt_pk_bf16_f32 v227, v202, v203\n"
    "v_add_f32 v245, v245, v199\n"
    "v_mfma_f32_16x16x32_bf16 a[124:127], v[188:191], v[236:239], a[124:127]\n"
    "v_exp_f32 v204, v204\n"
    "v_mfma_f32_16x16x32_bf16 a[92:95], v[188:191], v[232:235], a[92:95]\n"
    "v_add_f32 v244, v244, v201\n"
    "v_add_f32 v245, v245, v204\n"
    "v_mfma_f32_16x16x32_bf16 v[208:211], v[128:131], v[96:99], v[248:251]\n"
    "v_exp_f32 v205, v205\n"
    "v_mfma_f32_16x16x32_bf16 v[208:211], v[132:135], v[100:103], v[208:211]\n"
    "v_cvt_pk_bf16_f32 v230, v204, v205\n"
    "v_add_f32 v244, v244, v202\n"
    "v_mfma_f32_16x16x32_bf16 v[208:211], v[136:139], v[104:107], v[208:211]\n"
    "s_waitcnt vmcnt(8)\n"
    "v_exp_f32 v206, v206\n"
    "v_mfma_f32_16x16x32_bf16 v[208:211], v[140:143], v[108:111], v[208:211]\n"
    "s_barrier\n"
    "v_add_f32 v244, v244, v203\n"
    "v_add_f32 v245, v245, v205\n"
    "v_exp_f32 v207, v207\n"
    "s_nop 0\n"
    "v_cvt_pk_bf16_f32 v231, v206, v207\n"
    "v_add_f32 v245, v245, v206\n"
    "s_nop 0\n"
    "v_exp_f32 v208, v208\n"
    "ds_read_b128 v[140:143], v252 offset:42048\n"
    "v_add_f32 v245, v245, v207\n"
    "v_mfma_f32_16x16x32_bf16 a[128:131], v[160:163], v[224:227], a[128:131]\n"
    "v_exp_f32 v209, v209\n"
    "v_mfma_f32_16x16x32_bf16 a[160:163], v[160:163], v[228:231], a[160:163]\n"
    "ds_read_b128 v[136:139], v252 offset:41792\n"
    "v_cvt_pk_bf16_f32 v232, v208, v209\n"
    "v_mfma_f32_16x16x32_bf16 a[164:167], v[164:167], v[228:231], a[164:167]\n"
    "v_exp_f32 v210, v210\n"
    "v_mfma_f32_16x16x32_bf16 a[132:135], v[164:167], v[224:227], a[132:135]\n"
    "ds_read_b128 v[132:135], v252 offset:41536\n"
    "v_add_f32 v246, v246, v208\n"
    "v_mfma_f32_16x16x32_bf16 a[136:139], v[168:171], v[224:227], a[136:139]\n"
    "v_exp_f32 v211, v211\n"
    "v_mfma_f32_16x16x32_bf16 a[168:171], v[168:171], v[228:231], a[168:171]\n"
    "ds_read_b128 v[128:131], v252 offset:41280\n"
    "v_cvt_pk_bf16_f32 v233, v210, v211\n"
    "v_mfma_f32_16x16x32_bf16 a[172:175], v[172:175], v[228:231], a[172:175]\n"
    "v_mfma_f32_16x16x32_bf16 a[140:143], v[172:175], v[224:227], a[140:143]\n"
    "v_add_f32 v246, v246, v209\n"
    "v_mfma_f32_16x16x32_bf16 v[216:219], v[144:147], v[96:99], v[248:251]\n"
    "v_mfma_f32_16x16x32_bf16 v[216:219], v[148:151], v[100:103], v[216:219]\n"
    "v_add_f32 v246, v246, v210\n"
    "v_mfma_f32_16x16x32_bf16 v[216:219], v[152:155], v[104:107], v[216:219]\n"
    "v_mfma_f32_16x16x32_bf16 v[216:219], v[156:159], v[108:111], v[216:219]\n"
    "v_add_f32 v246, v246, v211\n"
    "s_nop 6\n"
    "v_exp_f32 v216, v216\n"
    "ds_read_b128 v[156:159], v252 offset:46176\n"
    "v_mfma_f32_16x16x32_bf16 a[144:147], v[176:179], v[224:227], a[144:147]\n"
    "v_exp_f32 v217, v217\n"
    "v_mfma_f32_16x16x32_bf16 a[176:179], v[176:179], v[228:231], a[176:179]\n"
    "ds_read_b128 v[152:155], v252 offset:45920\n"
    "v_cvt_pk_bf16_f32 v234, v216, v217\n"
    "v_mfma_f32_16x16x32_bf16 a[180:183], v[180:183], v[228:231], a[180:183]\n"
    "v_exp_f32 v218, v218\n"
    "v_mfma_f32_16x16x32_bf16 a[148:151], v[180:183], v[224:227], a[148:151]\n"
    "ds_read_b128 v[148:151], v252 offset:45664\n"
    "v_add_f32 v246, v246, v216\n"
    "v_mfma_f32_16x16x32_bf16 a[152:155], v[184:187], v[224:227], a[152:155]\n"
    "v_exp_f32 v219, v219\n"
    "v_mfma_f32_16x16x32_bf16 a[184:187], v[184:187], v[228:231], a[184:187]\n"
    "ds_read_b128 v[144:147], v252 offset:45408\n"
    "v_cvt_pk_bf16_f32 v235, v218, v219\n"
    "v_mfma_f32_16x16x32_bf16 a[188:191], v[188:191], v[228:231], a[188:191]\n"
    "v_mfma_f32_16x16x32_bf16 a[156:159], v[188:191], v[224:227], a[156:159]\n"
    "s_waitcnt lgkmcnt(4)\n"
    "v_add_f32 v246, v246, v217\n"
    "v_mfma_f32_16x16x32_bf16 v[192:195], v[128:131], v[0:3], v[248:251]\n"
    "v_mfma_f32_16x16x32_bf16 v[192:195], v[132:135], v[4:7], v[192:195]\n"
    "v_add_f32 v246, v246, v218\n"
    "v_mfma_f32_16x16x32_bf16 v[192:195], v[136:139], v[8:11], v[192:195]\n"
    "v_mfma_f32_16x16x32_bf16 v[192:195], v[140:143], v[12:15], v[192:195]\n"
    "v_add_f32 v246, v246, v219\n"
    "v_mfma_f32_16x16x32_bf16 v[196:199], v[140:143], v[28:31], v[248:251]\n"
    "v_mfma_f32_16x16x32_bf16 v[196:199], v[136:139], v[24:27], v[196:199]\n"
    "v_mfma_f32_16x16x32_bf16 v[196:199], v[132:135], v[20:23], v[196:199]\n"
    "s_nop 3\n"
    "v_exp_f32 v192, v192\n"
    "v_mfma_f32_16x16x32_bf16 v[196:199], v[128:131], v[16:19], v[196:199]\n"
    "v_add_f32 v240, v240, v192\n"
    "v_mfma_f32_16x16x32_bf16 a[192:195], v[160:163], v[232:235], a[192:195]\n"
    "v_exp_f32 v193, v193\n"
    "ds_read_b64_tr_b16 v[160:161], v253 offset:41280\n"
    "v_cvt_pk_bf16_f32 v224, v192, v193\n"
    "v_exp_f32 v194, v194\n"
    "v_mfma_f32_16x16x32_bf16 a[196:199], v[164:167], v[232:235], a[196:199]\n"
    "ds_read_b64_tr_b16 v[162:163], v253 offset:45408\n"
    "v_add_f32 v240, v240, v193\n"
    "v_mfma_f32_16x16x32_bf16 a[200:203], v[168:171], v[232:235], a[200:203]\n"
    "v_exp_f32 v195, v195\n"
    "ds_read_b64_tr_b16 v[164:165], v253 offset:41288\n"
    "v_cvt_pk_bf16_f32 v225, v194, v195\n"
    "v_exp_f32 v196, v196\n"
    "v_mfma_f32_16x16x32_bf16 a[204:207], v[172:175], v[232:235], a[204:207]\n"
    "s_waitcnt lgkmcnt(3)\n"
    "ds_read_b64_tr_b16 v[166:167], v253 offset:45416\n"
    "v_add_f32 v240, v240, v194\n"
    "v_mfma_f32_16x16x32_bf16 v[200:203], v[144:147], v[0:3], v[248:251]\n"
    "v_exp_f32 v197, v197\n"
    "v_mfma_f32_16x16x32_bf16 v[200:203], v[148:151], v[4:7], v[200:203]\n"
    "ds_read_b64_tr_b16 v[168:169], v253 offset:41536\n"
    "v_cvt_pk_bf16_f32 v228, v196, v197\n"
    "v_mfma_f32_16x16x32_bf16 v[200:203], v[152:155], v[8:11], v[200:203]\n"
    "v_exp_f32 v198, v198\n"
    "v_mfma_f32_16x16x32_bf16 v[200:203], v[156:159], v[12:15], v[200:203]\n"
    "ds_read_b64_tr_b16 v[170:171], v253 offset:45664\n"
    "v_add_f32 v240, v240, v195\n"
    "v_mfma_f32_16x16x32_bf16 v[204:207], v[156:159], v[28:31], v[248:251]\n"
    "v_exp_f32 v199, v199\n"
    "v_mfma_f32_16x16x32_bf16 v[204:207], v[152:155], v[24:27], v[204:207]\n"
    "ds_read_b64_tr_b16 v[172:173], v253 offset:41544\n"
    "v_cvt_pk_bf16_f32 v229, v198, v199\n"
    "v_mfma_f32_16x16x32_bf16 v[204:207], v[148:151], v[20:23], v[204:207]\n"
    "v_exp_f32 v200, v200\n"
    "v_mfma_f32_16x16x32_bf16 v[204:207], v[144:147], v[16:19], v[204:207]\n"
    "ds_read_b64_tr_b16 v[174:175], v253 offset:45672\n"
    "v_add_f32 v241, v241, v196\n"
    "v_mfma_f32_16x16x32_bf16 a[208:211], v[176:179], v[232:235], a[208:211]\n"
    "v_exp_f32 v201, v201\n"
    "ds_read_b64_tr_b16 v[176:177], v253 offset:41792\n"
    "v_cvt_pk_bf16_f32 v226, v200, v201\n"
    "v_exp_f32 v202, v202\n"
    "v_mfma_f32_16x16x32_bf16 a[212:215], v[180:183], v[232:235], a[212:215]\n"
    "ds_read_b64_tr_b16 v[178:179], v253 offset:45920\n"
    "v_add_f32 v241, v241, v197\n"
    "v_mfma_f32_16x16x32_bf16 a[216:219], v[184:187], v[232:235], a[216:219]\n"
    "v_exp_f32 v203, v203\n"
    "ds_read_b64_tr_b16 v[180:181], v253 offset:41800\n"
    "v_cvt_pk_bf16_f32 v227, v202, v203\n"
    "v_exp_f32 v204, v204\n"
    "v_mfma_f32_16x16x32_bf16 a[220:223], v[188:191], v[232:235], a[220:223]\n"
    "ds_read_b64_tr_b16 v[182:183], v253 offset:45928\n"
    "v_add_f32 v241, v241, v198\n"
    "v_mfma_f32_16x16x32_bf16 v[208:211], v[128:131], v[32:35], v[248:251]\n"
    "v_exp_f32 v205, v205\n"
    "v_mfma_f32_16x16x32_bf16 v[208:211], v[132:135], v[36:39], v[208:211]\n"
    "ds_read_b64_tr_b16 v[184:185], v253 offset:42048\n"
    "v_cvt_pk_bf16_f32 v230, v204, v205\n"
    "v_mfma_f32_16x16x32_bf16 v[208:211], v[136:139], v[40:43], v[208:211]\n"
    "v_exp_f32 v206, v206\n"
    "v_mfma_f32_16x16x32_bf16 v[208:211], v[140:143], v[44:47], v[208:211]\n"
    "ds_read_b64_tr_b16 v[186:187], v253 offset:46176\n"
    "v_add_f32 v241, v241, v199\n"
    "v_mfma_f32_16x16x32_bf16 v[212:215], v[140:143], v[60:63], v[248:251]\n"
    "v_exp_f32 v207, v207\n"
    "v_mfma_f32_16x16x32_bf16 v[212:215], v[136:139], v[56:59], v[212:215]\n"
    "ds_read_b64_tr_b16 v[188:189], v253 offset:42056\n"
    "v_cvt_pk_bf16_f32 v231, v206, v207\n"
    "v_mfma_f32_16x16x32_bf16 v[212:215], v[132:135], v[52:55], v[212:215]\n"
    "v_exp_f32 v208, v208\n"
    "v_mfma_f32_16x16x32_bf16 v[212:215], v[128:131], v[48:51], v[212:215]\n"
    "s_waitcnt lgkmcnt(13)\n"
    "ds_read_b64_tr_b16 v[190:191], v253 offset:46184\n"
    "v_add_f32 v240, v240, v200\n"
    "v_mfma_f32_16x16x32_bf16 a[0:3], v[160:163], v[224:227], a[0:3]\n"
    "v_exp_f32 v209, v209\n"
    "v_mfma_f32_16x16x32_bf16 a[32:35], v[160:163], v[228:231], a[32:35]\n"
    "s_waitcnt lgkmcnt(12)\n"
    "v_cvt_pk_bf16_f32 v232, v208, v209\n"
    "v_add_f32 v240, v240, v201\n"
    "v_mfma_f32_16x16x32_bf16 a[36:39], v[164:167], v[228:231], a[36:39]\n"
    "v_exp_f32 v210, v210\n"
    "v_mfma_f32_16x16x32_bf16 a[4:7], v[164:167], v[224:227], a[4:7]\n"
    "s_waitcnt lgkmcnt(10)\n"
    "v_add_f32 v240, v240, v202\n"
    "v_add_f32 v241, v241, v204\n"
    "v_mfma_f32_16x16x32_bf16 a[8:11], v[168:171], v[224:227], a[8:11]\n"
    "v_exp_f32 v211, v211\n"
    "v_mfma_f32_16x16x32_bf16 a[40:43], v[168:171], v[228:231], a[40:43]\n"
    "s_waitcnt lgkmcnt(8)\n"
    "v_cvt_pk_bf16_f32 v233, v210, v211\n"
    "v_add_f32 v240, v240, v203\n"
    "v_mfma_f32_16x16x32_bf16 a[44:47], v[172:175], v[228:231], a[44:47]\n"
    "s_add_u32 m0, s54, 24768\n"
    "v_exp_f32 v212, v212\n"
    "v_mfma_f32_16x16x32_bf16 a[12:15], v[172:175], v[224:227], a[12:15]\n"
    "buffer_load_dwordx4 v254, s[40:43], s53 offen nt lds\n"
    "v_mfma_f32_16x16x32_bf16 v[216:219], v[144:147], v[32:35], v[248:251]\n"
    "v_exp_f32 v213, v213\n"
    "v_mfma_f32_16x16x32_bf16 v[216:219], v[148:151], v[36:39], v[216:219]\n"
    "v_cvt_pk_bf16_f32 v236, v212, v213\n"
    "v_add_f32 v241, v241, v205\n"
    "v_mfma_f32_16x16x32_bf16 v[216:219], v[152:155], v[40:43], v[216:219]\n"
    "v_exp_f32 v214, v214\n"
    "v_mfma_f32_16x16x32_bf16 v[216:219], v[156:159], v[44:47], v[216:219]\n"
    "v_add_f32 v241, v241, v206\n"
    "v_add_f32 v242, v242, v208\n"
    "v_mfma_f32_16x16x32_bf16 v[220:223], v[156:159], v[60:63], v[248:251]\n"
    "v_exp_f32 v215, v215\n"
    "v_mfma_f32_16x16x32_bf16 v[220:223], v[152:155], v[56:59], v[220:223]\n"
    "v_cvt_pk_bf16_f32 v237, v214, v215\n"
    "v_add_f32 v241, v241, v207\n"
    "v_mfma_f32_16x16x32_bf16 v[220:223], v[148:151], v[52:55], v[220:223]\n"
    "v_exp_f32 v216, v216\n"
    "v_mfma_f32_16x16x32_bf16 v[220:223], v[144:147], v[48:51], v[220:223]\n"
    "s_waitcnt lgkmcnt(6)\n"
    "v_add_f32 v242, v242, v209\n"
    "v_add_f32 v243, v243, v212\n"
    "v_mfma_f32_16x16x32_bf16 a[16:19], v[176:179], v[224:227], a[16:19]\n"
    "v_exp_f32 v217, v217\n"
    "v_mfma_f32_16x16x32_bf16 a[48:51], v[176:179], v[228:231], a[48:51]\n"
    "s_waitcnt lgkmcnt(4)\n"
    "v_cvt_pk_bf16_f32 v234, v216, v217\n"
    "v_add_f32 v242, v242, v210\n"
    "v_mfma_f32_16x16x32_bf16 a[52:55], v[180:183], v[228:231], a[52:55]\n"
    "v_exp_f32 v218, v218\n"
    "v_mfma_f32_16x16x32_bf16 a[20:23], v[180:183], v[224:227], a[20:23]\n"
    "s_waitcnt lgkmcnt(2)\n"
    "buffer_load_dwordx4 v254, s[40:43], s53 offen offset:1024 nt lds\n"
    "v_mfma_f32_16x16x32_bf16 a[24:27], v[184:187], v[224:227], a[24:27]\n"
    "v_exp_f32 v219, v219\n"
    "v_mfma_f32_16x16x32_bf16 a[56:59], v[184:187], v[228:231], a[56:59]\n"
    "s_waitcnt lgkmcnt(0)\n"
    "v_cvt_pk_bf16_f32 v235, v218, v219\n"
    "v_add_f32 v242, v242, v211\n"
    "v_mfma_f32_16x16x32_bf16 a[60:63], v[188:191], v[228:231], a[60:63]\n"
    "v_exp_f32 v220, v220\n"
    "v_mfma_f32_16x16x32_bf16 a[28:31], v[188:191], v[224:227], a[28:31]\n"
    "v_add_f32 v243, v243, v213\n"
    "v_add_f32 v242, v242, v216\n"
    "v_mfma_f32_16x16x32_bf16 v[192:195], v[128:131], v[64:67], v[248:251]\n"
    "v_exp_f32 v221, v221\n"
    "v_mfma_f32_16x16x32_bf16 v[192:195], v[132:135], v[68:71], v[192:195]\n"
    "v_cvt_pk_bf16_f32 v238, v220, v221\n"
    "v_add_f32 v243, v243, v214\n"
    "v_mfma_f32_16x16x32_bf16 v[192:195], v[136:139], v[72:75], v[192:195]\n"
    "v_exp_f32 v222, v222\n"
    "v_mfma_f32_16x16x32_bf16 v[192:195], v[140:143], v[76:79], v[192:195]\n"
    "v_add_f32 v243, v243, v215\n"
    "v_add_f32 v242, v242, v217\n"
    "v_mfma_f32_16x16x32_bf16 v[196:199], v[140:143], v[92:95], v[248:251]\n"
    "v_exp_f32 v223, v223\n"
    "v_mfma_f32_16x16x32_bf16 v[196:199], v[136:139], v[88:91], v[196:199]\n"
    "v_cvt_pk_bf16_f32 v239, v222, v223\n"
    "v_add_f32 v242, v242, v218\n"
    "v_mfma_f32_16x16x32_bf16 v[196:199], v[132:135], v[84:87], v[196:199]\n"
    "v_exp_f32 v192, v192\n"
    "v_mfma_f32_16x16x32_bf16 v[196:199], v[128:131], v[80:83], v[196:199]\n"
    "v_add_f32 v242, v242, v219\n"
    "v_add_f32 v243, v243, v220\n"
    "v_mfma_f32_16x16x32_bf16 a[64:67], v[160:163], v[232:235], a[64:67]\n"
    "v_exp_f32 v193, v193\n"
    "v_mfma_f32_16x16x32_bf16 a[96:99], v[160:163], v[236:239], a[96:99]\n"
    "v_cvt_pk_bf16_f32 v224, v192, v193\n"
    "v_add_f32 v243, v243, v221\n"
    "v_mfma_f32_16x16x32_bf16 a[100:103], v[164:167], v[236:239], a[100:103]\n"
    "s_add_u32 m0, s55, 82560\n"
    "v_exp_f32 v194, v194\n"
    "v_mfma_f32_16x16x32_bf16 a[68:71], v[164:167], v[232:235], a[68:71]\n"
    "buffer_load_dwordx4 v254, s[44:47], s53 offen nt lds\n"
    "v_mfma_f32_16x16x32_bf16 a[72:75], v[168:171], v[232:235], a[72:75]\n"
    "v_exp_f32 v195, v195\n"
    "v_mfma_f32_16x16x32_bf16 a[104:107], v[168:171], v[236:239], a[104:107]\n"
    "v_cvt_pk_bf16_f32 v225, v194, v195\n"
    "v_add_f32 v243, v243, v222\n"
    "v_mfma_f32_16x16x32_bf16 a[108:111], v[172:175], v[236:239], a[108:111]\n"
    "v_exp_f32 v196, v196\n"
    "v_mfma_f32_16x16x32_bf16 a[76:79], v[172:175], v[232:235], a[76:79]\n"
    "v_add_f32 v243, v243, v223\n"
    "v_add_f32 v244, v244, v192\n"
    "v_mfma_f32_16x16x32_bf16 v[200:203], v[144:147], v[64:67], v[248:251]\n"
    "v_exp_f32 v197, v197\n"
    "v_mfma_f32_16x16x32_bf16 v[200:203], v[148:151], v[68:71], v[200:203]\n"
    "v_cvt_pk_bf16_f32 v228, v196, v197\n"
    "v_add_f32 v244, v244, v193\n"
    "v_mfma_f32_16x16x32_bf16 v[200:203], v[152:155], v[72:75], v[200:203]\n"
    "v_exp_f32 v198, v198\n"
    "v_mfma_f32_16x16x32_bf16 v[200:203], v[156:159], v[76:79], v[200:203]\n"
    "v_add_f32 v244, v244, v194\n"
    "v_add_f32 v245, v245, v196\n"
    "v_mfma_f32_16x16x32_bf16 v[204:207], v[156:159], v[92:95], v[248:251]\n"
    "v_exp_f32 v199, v199\n"
    "v_mfma_f32_16x16x32_bf16 v[204:207], v[152:155], v[88:91], v[204:207]\n"
    "v_cvt_pk_bf16_f32 v229, v198, v199\n"
    "v_add_f32 v244, v244, v195\n"
    "v_mfma_f32_16x16x32_bf16 v[204:207], v[148:151], v[84:87], v[204:207]\n"
    "v_exp_f32 v200, v200\n"
    "v_mfma_f32_16x16x32_bf16 v[204:207], v[144:147], v[80:83], v[204:207]\n"
    "v_add_f32 v245, v245, v197\n"
    "v_add_f32 v244, v244, v200\n"
    "v_mfma_f32_16x16x32_bf16 a[80:83], v[176:179], v[232:235], a[80:83]\n"
    "v_exp_f32 v201, v201\n"
    "v_mfma_f32_16x16x32_bf16 a[112:115], v[176:179], v[236:239], a[112:115]\n"
    "v_cvt_pk_bf16_f32 v226, v200, v201\n"
    "v_add_f32 v245, v245, v198\n"
    "v_mfma_f32_16x16x32_bf16 a[116:119], v[180:183], v[236:239], a[116:119]\n"
    "s_add_u32 m0, s55, 82568\n"
    "v_exp_f32 v202, v202\n"
    "v_mfma_f32_16x16x32_bf16 a[84:87], v[180:183], v[232:235], a[84:87]\n"
    "buffer_load_dwordx4 v254, s[44:47], s53 offen offset:1024 nt lds\n"
    "v_mfma_f32_16x16x32_bf16 a[88:91], v[184:187], v[232:235], a[88:91]\n"
    "s_add_u32 s53, s53, 8192\n"
    "v_exp_f32 v203, v203\n"
    "v_mfma_f32_16x16x32_bf16 a[120:123], v[184:187], v[236:239], a[120:123]\n"
    "v_cvt_pk_bf16_f32 v227, v202, v203\n"
    "v_add_f32 v245, v245, v199\n"
    "v_mfma_f32_16x16x32_bf16 a[124:127], v[188:191], v[236:239], a[124:127]\n"
    "v_exp_f32 v204, v204\n"
    "v_mfma_f32_16x16x32_bf16 a[92:95], v[188:191], v[232:235], a[92:95]\n"
    "v_add_f32 v244, v244, v201\n"
    "v_add_f32 v245, v245, v204\n"
    "v_mfma_f32_16x16x32_bf16 v[208:211], v[128:131], v[96:99], v[248:251]\n"
    "v_exp_f32 v205, v205\n"
    "v_mfma_f32_16x16x32_bf16 v[208:211], v[132:135], v[100:103], v[208:211]\n"
    "v_cvt_pk_bf16_f32 v230, v204, v205\n"
    "v_add_f32 v244, v244, v202\n"
    "v_mfma_f32_16x16x32_bf16 v[208:211], v[136:139], v[104:107], v[208:211]\n"
    "v_exp_f32 v206, v206\n"
    "v_mfma_f32_16x16x32_bf16 v[208:211], v[140:143], v[108:111], v[208:211]\n"
    "v_add_f32 v244, v244, v203\n"
    "v_add_f32 v245, v245, v205\n"
    "v_exp_f32 v207, v207\n"
    "s_nop 0\n"
    "v_cvt_pk_bf16_f32 v231, v206, v207\n"
    "v_add_f32 v245, v245, v206\n"
    "s_nop 1\n"
    "v_exp_f32 v208, v208\n"
    "ds_read_b128 v[140:143], v252 offset:768\n"
    "v_add_f32 v245, v245, v207\n"
    "v_mfma_f32_16x16x32_bf16 a[128:131], v[160:163], v[224:227], a[128:131]\n"
    "v_exp_f32 v209, v209\n"
    "v_mfma_f32_16x16x32_bf16 a[160:163], v[160:163], v[228:231], a[160:163]\n"
    "ds_read_b128 v[136:139], v252 offset:512\n"
    "v_cvt_pk_bf16_f32 v232, v208, v209\n"
    "v_mfma_f32_16x16x32_bf16 a[164:167], v[164:167], v[228:231], a[164:167]\n"
    "v_exp_f32 v210, v210\n"
    "v_mfma_f32_16x16x32_bf16 a[132:135], v[164:167], v[224:227], a[132:135]\n"
    "ds_read_b128 v[132:135], v252 offset:256\n"
    "v_add_f32 v246, v246, v208\n"
    "v_mfma_f32_16x16x32_bf16 a[136:139], v[168:171], v[224:227], a[136:139]\n"
    "v_exp_f32 v211, v211\n"
    "v_mfma_f32_16x16x32_bf16 a[168:171], v[168:171], v[228:231], a[168:171]\n"
    "ds_read_b128 v[128:131], v252 offset:0\n"
    "v_cvt_pk_bf16_f32 v233, v210, v211\n"
    "v_mfma_f32_16x16x32_bf16 a[172:175], v[172:175], v[228:231], a[172:175]\n"
    "v_mfma_f32_16x16x32_bf16 a[140:143], v[172:175], v[224:227], a[140:143]\n"
    "v_add_f32 v246, v246, v209\n"
    "v_mfma_f32_16x16x32_bf16 v[216:219], v[144:147], v[96:99], v[248:251]\n"
    "v_mfma_f32_16x16x32_bf16 v[216:219], v[148:151], v[100:103], v[216:219]\n"
    "v_add_f32 v246, v246, v210\n"
    "v_mfma_f32_16x16x32_bf16 v[216:219], v[152:155], v[104:107], v[216:219]\n"
    "v_mfma_f32_16x16x32_bf16 v[216:219], v[156:159], v[108:111], v[216:219]\n"
    "v_add_f32 v246, v246, v211\n"
    "s_nop 6\n"
    "v_exp_f32 v216, v216\n"
    "ds_read_b128 v[156:159], v252 offset:4896\n"
    "v_mfma_f32_16x16x32_bf16 a[144:147], v[176:179], v[224:227], a[144:147]\n"
    "v_exp_f32 v217, v217\n"
    "v_mfma_f32_16x16x32_bf16 a[176:179], v[176:179], v[228:231], a[176:179]\n"
    "ds_read_b128 v[152:155], v252 offset:4640\n"
    "v_cvt_pk_bf16_f32 v234, v216, v217\n"
    "v_mfma_f32_16x16x32_bf16 a[180:183], v[180:183], v[228:231], a[180:183]\n"
    "v_exp_f32 v218, v218\n"
    "v_mfma_f32_16x16x32_bf16 a[148:151], v[180:183], v[224:227], a[148:151]\n"
    "ds_read_b128 v[148:151], v252 offset:4384\n"
    "v_add_f32 v246, v246, v216\n"
    "v_mfma_f32_16x16x32_bf16 a[152:155], v[184:187], v[224:227], a[152:155]\n"
    "v_exp_f32 v219, v219\n"
    "v_mfma_f32_16x16x32_bf16 a[184:187], v[184:187], v[228:231], a[184:187]\n"
    "ds_read_b128 v[144:147], v252 offset:4128\n"
    "v_cvt_pk_bf16_f32 v235, v218, v219\n"
    "v_mfma_f32_16x16x32_bf16 a[188:191], v[188:191], v[228:231], a[188:191]\n"
    "v_mfma_f32_16x16x32_bf16 a[156:159], v[188:191], v[224:227], a[156:159]\n"
    "s_waitcnt lgkmcnt(4)\n"
    "v_add_f32 v246, v246, v217\n"
    "v_mfma_f32_16x16x32_bf16 v[192:195], v[128:131], v[0:3], v[248:251]\n"
    "v_mfma_f32_16x16x32_bf16 v[192:195], v[132:135], v[4:7], v[192:195]\n"
    "v_add_f32 v246, v246, v218\n"
    "v_mfma_f32_16x16x32_bf16 v[192:195], v[136:139], v[8:11], v[192:195]\n"
    "v_mfma_f32_16x16x32_bf16 v[192:195], v[140:143], v[12:15], v[192:195]\n"
    "v_add_f32 v246, v246, v219\n"
    "v_mfma_f32_16x16x32_bf16 v[196:199], v[140:143], v[28:31], v[248:251]\n"
    "v_mfma_f32_16x16x32_bf16 v[196:199], v[136:139], v[24:27], v[196:199]\n"
    "v_mfma_f32_16x16x32_bf16 v[196:199], v[132:135], v[20:23], v[196:199]\n"
    "s_nop 3\n"
    "v_exp_f32 v192, v192\n"
    "v_mfma_f32_16x16x32_bf16 v[196:199], v[128:131], v[16:19], v[196:199]\n"
    "v_add_f32 v240, v240, v192\n"
    "v_mfma_f32_16x16x32_bf16 a[192:195], v[160:163], v[232:235], a[192:195]\n"
    "v_exp_f32 v193, v193\n"
    "ds_read_b64_tr_b16 v[160:161], v253 offset:0\n"
    "v_cvt_pk_bf16_f32 v224, v192, v193\n"
    "v_exp_f32 v194, v194\n"
    "v_mfma_f32_16x16x32_bf16 a[196:199], v[164:167], v[232:235], a[196:199]\n"
    "ds_read_b64_tr_b16 v[162:163], v253 offset:4128\n"
    "v_add_f32 v240, v240, v193\n"
    "v_mfma_f32_16x16x32_bf16 a[200:203], v[168:171], v[232:235], a[200:203]\n"
    "v_exp_f32 v195, v195\n"
    "ds_read_b64_tr_b16 v[164:165], v253 offset:8\n"
    "v_cvt_pk_bf16_f32 v225, v194, v195\n"
    "v_exp_f32 v196, v196\n"
    "v_mfma_f32_16x16x32_bf16 a[204:207], v[172:175], v[232:235], a[204:207]\n"
    "s_waitcnt lgkmcnt(3)\n"
    "ds_read_b64_tr_b16 v[166:167], v253 offset:4136\n"
    "v_add_f32 v240, v240, v194\n"
    "v_mfma_f32_16x16x32_bf16 v[200:203], v[144:147], v[0:3], v[248:251]\n"
    "v_exp_f32 v197, v197\n"
    "v_mfma_f32_16x16x32_bf16 v[200:203], v[148:151], v[4:7], v[200:203]\n"
    "ds_read_b64_tr_b16 v[168:169], v253 offset:256\n"
    "v_cvt_pk_bf16_f32 v228, v196, v197\n"
    "v_mfma_f32_16x16x32_bf16 v[200:203], v[152:155], v[8:11], v[200:203]\n"
    "v_exp_f32 v198, v198\n"
    "v_mfma_f32_16x16x32_bf16 v[200:203], v[156:159], v[12:15], v[200:203]\n"
    "ds_read_b64_tr_b16 v[170:171], v253 offset:4384\n"
    "v_add_f32 v240, v240, v195\n"
    "v_mfma_f32_16x16x32_bf16 v[204:207], v[156:159], v[28:31], v[248:251]\n"
    "v_exp_f32 v199, v199\n"
    "v_mfma_f32_16x16x32_bf16 v[204:207], v[152:155], v[24:27], v[204:207]\n"
    "ds_read_b64_tr_b16 v[172:173], v253 offset:264\n"
    "v_cvt_pk_bf16_f32 v229, v198, v199\n"
    "v_mfma_f32_16x16x32_bf16 v[204:207], v[148:151], v[20:23], v[204:207]\n"
    "v_exp_f32 v200, v200\n"
    "v_mfma_f32_16x16x32_bf16 v[204:207], v[144:147], v[16:19], v[204:207]\n"
    "ds_read_b64_tr_b16 v[174:175], v253 offset:4392\n"
    "v_add_f32 v241, v241, v196\n"
    "v_mfma_f32_16x16x32_bf16 a[208:211], v[176:179], v[232:235], a[208:211]\n"
    "v_exp_f32 v201, v201\n"
    "ds_read_b64_tr_b16 v[176:177], v253 offset:512\n"
    "v_cvt_pk_bf16_f32 v226, v200, v201\n"
    "v_exp_f32 v202, v202\n"
    "v_mfma_f32_16x16x32_bf16 a[212:215], v[180:183], v[232:235], a[212:215]\n"
    "ds_read_b64_tr_b16 v[178:179], v253 offset:4640\n"
    "v_add_f32 v241, v241, v197\n"
    "v_mfma_f32_16x16x32_bf16 a[216:219], v[184:187], v[232:235], a[216:219]\n"
    "v_exp_f32 v203, v203\n"
    "ds_read_b64_tr_b16 v[180:181], v253 offset:520\n"
    "v_cvt_pk_bf16_f32 v227, v202, v203\n"
    "v_exp_f32 v204, v204\n"
    "v_mfma_f32_16x16x32_bf16 a[220:223], v[188:191], v[232:235], a[220:223]\n"
    "ds_read_b64_tr_b16 v[182:183], v253 offset:4648\n"
    "v_add_f32 v241, v241, v198\n"
    "v_mfma_f32_16x16x32_bf16 v[208:211], v[128:131], v[32:35], v[248:251]\n"
    "v_exp_f32 v205, v205\n"
    "v_mfma_f32_16x16x32_bf16 v[208:211], v[132:135], v[36:39], v[208:211]\n"
    "ds_read_b64_tr_b16 v[184:185], v253 offset:768\n"
    "v_cvt_pk_bf16_f32 v230, v204, v205\n"
    "v_mfma_f32_16x16x32_bf16 v[208:211], v[136:139], v[40:43], v[208:211]\n"
    "v_exp_f32 v206, v206\n"
    "v_mfma_f32_16x16x32_bf16 v[208:211], v[140:143], v[44:47], v[208:211]\n"
    "ds_read_b64_tr_b16 v[186:187], v253 offset:4896\n"
    "v_add_f32 v241, v241, v199\n"
    "v_mfma_f32_16x16x32_bf16 v[212:215], v[140:143], v[60:63], v[248:251]\n"
    "v_exp_f32 v207, v207\n"
    "v_mfma_f32_16x16x32_bf16 v[212:215], v[136:139], v[56:59], v[212:215]\n"
    "ds_read_b64_tr_b16 v[188:189], v253 offset:776\n"
    "v_cvt_pk_bf16_f32 v231, v206, v207\n"
    "v_mfma_f32_16x16x32_bf16 v[212:215], v[132:135], v[52:55], v[212:215]\n"
    "v_exp_f32 v208, v208\n"
    "v_mfma_f32_16x16x32_bf16 v[212:215], v[128:131], v[48:51], v[212:215]\n"
    "s_waitcnt lgkmcnt(13)\n"
    "ds_read_b64_tr_b16 v[190:191], v253 offset:4904\n"
    "v_add_f32 v240, v240, v200\n"
    "v_mfma_f32_16x16x32_bf16 a[0:3], v[160:163], v[224:227], a[0:3]\n"
    "v_exp_f32 v209, v209\n"
    "v_mfma_f32_16x16x32_bf16 a[32:35], v[160:163], v[228:231], a[32:35]\n"
    "s_waitcnt lgkmcnt(12)\n"
    "v_cvt_pk_bf16_f32 v232, v208, v209\n"
    "v_add_f32 v240, v240, v201\n"
    "v_mfma_f32_16x16x32_bf16 a[36:39], v[164:167], v[228:231], a[36:39]\n"
    "v_exp_f32 v210, v210\n"
    "v_mfma_f32_16x16x32_bf16 a[4:7], v[164:167], v[224:227], a[4:7]\n"
    "s_waitcnt lgkmcnt(10)\n"
    "v_add_f32 v240, v240, v202\n"
    "v_add_f32 v241, v241, v204\n"
    "v_mfma_f32_16x16x32_bf16 a[8:11], v[168:171], v[224:227], a[8:11]\n"
    "v_exp_f32 v211, v211\n"
    "v_mfma_f32_16x16x32_bf16 a[40:43], v[168:171], v[228:231], a[40:43]\n"
    "s_waitcnt lgkmcnt(8)\n"
    "v_cvt_pk_bf16_f32 v233, v210, v211\n"
    "v_add_f32 v240, v240, v203\n"
    "v_mfma_f32_16x16x32_bf16 a[44:47], v[172:175], v[228:231], a[44:47]\n"
    "s_add_u32 m0, s54, 33024\n"
    "v_exp_f32 v212, v212\n"
    "v_mfma_f32_16x16x32_bf16 a[12:15], v[172:175], v[224:227], a[12:15]\n"
    "buffer_load_dwordx4 v254, s[40:43], s53 offen nt lds\n"
    "v_mfma_f32_16x16x32_bf16 v[216:219], v[144:147], v[32:35], v[248:251]\n"
    "v_exp_f32 v213, v213\n"
    "v_mfma_f32_16x16x32_bf16 v[216:219], v[148:151], v[36:39], v[216:219]\n"
    "v_cvt_pk_bf16_f32 v236, v212, v213\n"
    "v_add_f32 v241, v241, v205\n"
    "v_mfma_f32_16x16x32_bf16 v[216:219], v[152:155], v[40:43], v[216:219]\n"
    "v_exp_f32 v214, v214\n"
    "v_mfma_f32_16x16x32_bf16 v[216:219], v[156:159], v[44:47], v[216:219]\n"
    "v_add_f32 v241, v241, v206\n"
    "v_add_f32 v242, v242, v208\n"
    "v_mfma_f32_16x16x32_bf16 v[220:223], v[156:159], v[60:63], v[248:251]\n"
    "v_exp_f32 v215, v215\n"
    "v_mfma_f32_16x16x32_bf16 v[220:223], v[152:155], v[56:59], v[220:223]\n"
    "v_cvt_pk_bf16_f32 v237, v214, v215\n"
    "v_add_f32 v241, v241, v207\n"
    "v_mfma_f32_16x16x32_bf16 v[220:223], v[148:151], v[52:55], v[220:223]\n"
    "v_exp_f32 v216, v216\n"
    "v_mfma_f32_16x16x32_bf16 v[220:223], v[144:147], v[48:51], v[220:223]\n"
    "s_waitcnt lgkmcnt(6)\n"
    "v_add_f32 v242, v242, v209\n"
    "v_add_f32 v243, v243, v212\n"
    "v_mfma_f32_16x16x32_bf16 a[16:19], v[176:179], v[224:227], a[16:19]\n"
    "v_exp_f32 v217, v217\n"
    "v_mfma_f32_16x16x32_bf16 a[48:51], v[176:179], v[228:231], a[48:51]\n"
    "s_waitcnt lgkmcnt(4)\n"
    "v_cvt_pk_bf16_f32 v234, v216, v217\n"
    "v_add_f32 v242, v242, v210\n"
    "v_mfma_f32_16x16x32_bf16 a[52:55], v[180:183], v[228:231], a[52:55]\n"
    "v_exp_f32 v218, v218\n"
    "v_mfma_f32_16x16x32_bf16 a[20:23], v[180:183], v[224:227], a[20:23]\n"
    "s_waitcnt lgkmcnt(2)\n"
    "buffer_load_dwordx4 v254, s[40:43], s53 offen offset:1024 nt lds\n"
    "v_mfma_f32_16x16x32_bf16 a[24:27], v[184:187], v[224:227], a[24:27]\n"
    "v_exp_f32 v219, v219\n"
    "v_mfma_f32_16x16x32_bf16 a[56:59], v[184:187], v[228:231], a[56:59]\n"
    "s_waitcnt lgkmcnt(0)\n"
    "v_cvt_pk_bf16_f32 v235, v218, v219\n"
    "v_add_f32 v242, v242, v211\n"
    "v_mfma_f32_16x16x32_bf16 a[60:63], v[188:191], v[228:231], a[60:63]\n"
    "v_exp_f32 v220, v220\n"
    "v_mfma_f32_16x16x32_bf16 a[28:31], v[188:191], v[224:227], a[28:31]\n"
    "v_add_f32 v243, v243, v213\n"
    "v_add_f32 v242, v242, v216\n"
    "v_mfma_f32_16x16x32_bf16 v[192:195], v[128:131], v[64:67], v[248:251]\n"
    "v_exp_f32 v221, v221\n"
    "v_mfma_f32_16x16x32_bf16 v[192:195], v[132:135], v[68:71], v[192:195]\n"
    "v_cvt_pk_bf16_f32 v238, v220, v221\n"
    "v_add_f32 v243, v243, v214\n"
    "v_mfma_f32_16x16x32_bf16 v[192:195], v[136:139], v[72:75], v[192:195]\n"
    "v_exp_f32 v222, v222\n"
    "v_mfma_f32_16x16x32_bf16 v[192:195], v[140:143], v[76:79], v[192:195]\n"
    "v_add_f32 v243, v243, v215\n"
    "v_add_f32 v242, v242, v217\n"
    "v_mfma_f32_16x16x32_bf16 v[196:199], v[140:143], v[92:95], v[248:251]\n"
    "v_exp_f32 v223, v223\n"
    "v_mfma_f32_16x16x32_bf16 v[196:199], v[136:139], v[88:91], v[196:199]\n"
    "v_cvt_pk_bf16_f32 v239, v222, v223\n"
    "v_add_f32 v242, v242, v218\n"
    "v_mfma_f32_16x16x32_bf16 v[196:199], v[132:135], v[84:87], v[196:199]\n"
    "v_exp_f32 v192, v192\n"
    "v_mfma_f32_16x16x32_bf16 v[196:199], v[128:131], v[80:83], v[196:199]\n"
    "v_add_f32 v242, v242, v219\n"
    "v_add_f32 v243, v243, v220\n"
    "v_mfma_f32_16x16x32_bf16 a[64:67], v[160:163], v[232:235], a[64:67]\n"
    "v_exp_f32 v193, v193\n"
    "v_mfma_f32_16x16x32_bf16 a[96:99], v[160:163], v[236:239], a[96:99]\n"
    "v_cvt_pk_bf16_f32 v224, v192, v193\n"
    "v_add_f32 v243, v243, v221\n"
    "v_mfma_f32_16x16x32_bf16 a[100:103], v[164:167], v[236:239], a[100:103]\n"
    "s_add_u32 m0, s55, 90816\n"
    "v_exp_f32 v194, v194\n"
    "v_mfma_f32_16x16x32_bf16 a[68:71], v[164:167], v[232:235], a[68:71]\n"
    "buffer_load_dwordx4 v254, s[44:47], s53 offen nt lds\n"
    "v_mfma_f32_16x16x32_bf16 a[72:75], v[168:171], v[232:235], a[72:75]\n"
    "v_exp_f32 v195, v195\n"
    "v_mfma_f32_16x16x32_bf16 a[104:107], v[168:171], v[236:239], a[104:107]\n"
    "v_cvt_pk_bf16_f32 v225, v194, v195\n"
    "v_add_f32 v243, v243, v222\n"
    "v_mfma_f32_16x16x32_bf16 a[108:111], v[172:175], v[236:239], a[108:111]\n"
    "v_exp_f32 v196, v196\n"
    "v_mfma_f32_16x16x32_bf16 a[76:79], v[172:175], v[232:235], a[76:79]\n"
    "v_add_f32 v243, v243, v223\n"
    "v_add_f32 v244, v244, v192\n"
    "v_mfma_f32_16x16x32_bf16 v[200:203], v[144:147], v[64:67], v[248:251]\n"
    "v_exp_f32 v197, v197\n"
    "v_mfma_f32_16x16x32_bf16 v[200:203], v[148:151], v[68:71], v[200:203]\n"
    "v_cvt_pk_bf16_f32 v228, v196, v197\n"
    "v_add_f32 v244, v244, v193\n"
    "v_mfma_f32_16x16x32_bf16 v[200:203], v[152:155], v[72:75], v[200:203]\n"
    "v_exp_f32 v198, v198\n"
    "v_mfma_f32_16x16x32_bf16 v[200:203], v[156:159], v[76:79], v[200:203]\n"
    "v_add_f32 v244, v244, v194\n"
    "v_add_f32 v245, v245, v196\n"
    "v_mfma_f32_16x16x32_bf16 v[204:207], v[156:159], v[92:95], v[248:251]\n"
    "v_exp_f32 v199, v199\n"
    "v_mfma_f32_16x16x32_bf16 v[204:207], v[152:155], v[88:91], v[204:207]\n"
    "v_cvt_pk_bf16_f32 v229, v198, v199\n"
    "v_add_f32 v244, v244, v195\n"
    "v_mfma_f32_16x16x32_bf16 v[204:207], v[148:151], v[84:87], v[204:207]\n"
    "v_exp_f32 v200, v200\n"
    "v_mfma_f32_16x16x32_bf16 v[204:207], v[144:147], v[80:83], v[204:207]\n"
    "v_add_f32 v245, v245, v197\n"
    "v_add_f32 v244, v244, v200\n"
    "v_mfma_f32_16x16x32_bf16 a[80:83], v[176:179], v[232:235], a[80:83]\n"
    "v_exp_f32 v201, v201\n"
    "v_mfma_f32_16x16x32_bf16 a[112:115], v[176:179], v[236:239], a[112:115]\n"
    "v_cvt_pk_bf16_f32 v226, v200, v201\n"
    "v_add_f32 v245, v245, v198\n"
    "v_mfma_f32_16x16x32_bf16 a[116:119], v[180:183], v[236:239], a[116:119]\n"
    "s_add_u32 m0, s55, 90824\n"
    "v_exp_f32 v202, v202\n"
    "v_mfma_f32_16x16x32_bf16 a[84:87], v[180:183], v[232:235], a[84:87]\n"
    "buffer_load_dwordx4 v254, s[44:47], s53 offen offset:1024 nt lds\n"
    "v_mfma_f32_16x16x32_bf16 a[88:91], v[184:187], v[232:235], a[88:91]\n"
    "s_add_u32 s53, s53, 8192\n"
    "v_exp_f32 v203, v203\n"
    "v_mfma_f32_16x16x32_bf16 a[120:123], v[184:187], v[236:239], a[120:123]\n"
    "v_cvt_pk_bf16_f32 v227, v202, v203\n"
    "v_add_f32 v245, v245, v199\n"
    "v_mfma_f32_16x16x32_bf16 a[124:127], v[188:191], v[236:239], a[124:127]\n"
    "v_exp_f32 v204, v204\n"
    "v_mfma_f32_16x16x32_bf16 a[92:95], v[188:191], v[232:235], a[92:95]\n"
    "v_add_f32 v244, v244, v201\n"
    "v_add_f32 v245, v245, v204\n"
    "v_mfma_f32_16x16x32_bf16 v[208:211], v[128:131], v[96:99], v[248:251]\n"
    "v_exp_f32 v205, v205\n"
    "v_mfma_f32_16x16x32_bf16 v[208:211], v[132:135], v[100:103], v[208:211]\n"
    "v_cvt_pk_bf16_f32 v230, v204, v205\n"
    "v_add_f32 v244, v244, v202\n"
    "v_mfma_f32_16x16x32_bf16 v[208:211], v[136:139], v[104:107], v[208:211]\n"
    "s_waitcnt vmcnt(8)\n"
    "v_exp_f32 v206, v206\n"
    "v_mfma_f32_16x16x32_bf16 v[208:211], v[140:143], v[108:111], v[208:211]\n"
    "s_barrier\n"
    "v_add_f32 v244, v244, v203\n"
    "v_add_f32 v245, v245, v205\n"
    "v_exp_f32 v207, v207\n"
    "s_nop 0\n"
    "v_cvt_pk_bf16_f32 v231, v206, v207\n"
    "v_add_f32 v245, v245, v206\n"
    "s_nop 0\n"
    "v_exp_f32 v208, v208\n"
    "ds_read_b128 v[140:143], v252 offset:9024\n"
    "v_add_f32 v245, v245, v207\n"
    "v_mfma_f32_16x16x32_bf16 a[128:131], v[160:163], v[224:227], a[128:131]\n"
    "v_exp_f32 v209, v209\n"
    "v_mfma_f32_16x16x32_bf16 a[160:163], v[160:163], v[228:231], a[160:163]\n"
    "ds_read_b128 v[136:139], v252 offset:8768\n"
    "v_cvt_pk_bf16_f32 v232, v208, v209\n"
    "v_mfma_f32_16x16x32_bf16 a[164:167], v[164:167], v[228:231], a[164:167]\n"
    "v_exp_f32 v210, v210\n"
    "v_mfma_f32_16x16x32_bf16 a[132:135], v[164:167], v[224:227], a[132:135]\n"
    "ds_read_b128 v[132:135], v252 offset:8512\n"
    "v_add_f32 v246, v246, v208\n"
    "v_mfma_f32_16x16x32_bf16 a[136:139], v[168:171], v[224:227], a[136:139]\n"
    "v_exp_f32 v211, v211\n"
    "v_mfma_f32_16x16x32_bf16 a[168:171], v[168:171], v[228:231], a[168:171]\n"
    "ds_read_b128 v[128:131], v252 offset:8256\n"
    "v_cvt_pk_bf16_f32 v233, v210, v211\n"
    "v_mfma_f32_16x16x32_bf16 a[172:175], v[172:175], v[228:231], a[172:175]\n"
    "v_mfma_f32_16x16x32_bf16 a[140:143], v[172:175], v[224:227], a[140:143]\n"
    "v_add_f32 v246, v246, v209\n"
    "v_mfma_f32_16x16x32_bf16 v[216:219], v[144:147], v[96:99], v[248:251]\n"
    "v_mfma_f32_16x16x32_bf16 v[216:219], v[148:151], v[100:103], v[216:219]\n"
    "v_add_f32 v246, v246, v210\n"
    "v_mfma_f32_16x16x32_bf16 v[216:219], v[152:155], v[104:107], v[216:219]\n"
    "v_mfma_f32_16x16x32_bf16 v[216:219], v[156:159], v[108:111], v[216:219]\n"
    "v_add_f32 v246, v246, v211\n"
    "s_nop 6\n"
    "v_exp_f32 v216, v216\n"
    "ds_read_b128 v[156:159], v252 offset:13152\n"
    "v_mfma_f32_16x16x32_bf16 a[144:147], v[176:179], v[224:227], a[144:147]\n"
    "v_exp_f32 v217, v217\n"
    "v_mfma_f32_16x16x32_bf16 a[176:179], v[176:179], v[228:231], a[176:179]\n"
    "ds_read_b128 v[152:155], v252 offset:12896\n"
    "v_cvt_pk_bf16_f32 v234, v216, v217\n"
    "v_mfma_f32_16x16x32_bf16 a[180:183], v[180:183], v[228:231], a[180:183]\n"
    "v_exp_f32 v218, v218\n"
    "v_mfma_f32_16x16x32_bf16 a[148:151], v[180:183], v[224:227], a[148:151]\n"
    "ds_read_b128 v[148:151], v252 offset:12640\n"
    "v_add_f32 v246, v246, v216\n"
    "v_mfma_f32_16x16x32_bf16 a[152:155], v[184:187], v[224:227], a[152:155]\n"
    "v_exp_f32 v219, v219\n"
    "v_mfma_f32_16x16x32_bf16 a[184:187], v[184:187], v[228:231], a[184:187]\n"
    "ds_read_b128 v[144:147], v252 offset:12384\n"
    "v_cvt_pk_bf16_f32 v235, v218, v219\n"
    "v_mfma_f32_16x16x32_bf16 a[188:191], v[188:191], v[228:231], a[188:191]\n"
    "v_mfma_f32_16x16x32_bf16 a[156:159], v[188:191], v[224:227], a[156:159]\n"
    "s_waitcnt lgkmcnt(4)\n"
    "v_add_f32 v246, v246, v217\n"
    "v_mfma_f32_16x16x32_bf16 v[192:195], v[128:131], v[0:3], v[248:251]\n"
    "v_mfma_f32_16x16x32_bf16 v[192:195], v[132:135], v[4:7], v[192:195]\n"
    "v_add_f32 v246, v246, v218\n"
    "v_mfma_f32_16x16x32_bf16 v[192:195], v[136:139], v[8:11], v[192:195]\n"
    "v_mfma_f32_16x16x32_bf16 v[192:195], v[140:143], v[12:15], v[192:195]\n"
    "v_add_f32 v246, v246, v219\n"
    "v_mfma_f32_16x16x32_bf16 v[196:199], v[140:143], v[28:31], v[248:251]\n"
    "v_mfma_f32_16x16x32_bf16 v[196:199], v[136:139], v[24:27], v[196:199]\n"
    "v_mfma_f32_16x16x32_bf16 v[196:199], v[132:135], v[20:23], v[196:199]\n"
    "s_nop 3\n"
    "v_exp_f32 v192, v192\n"
    "v_mfma_f32_16x16x32_bf16 v[196:199], v[128:131], v[16:19], v[196:199]\n"
    "v_add_f32 v240, v240, v192\n"
    "v_mfma_f32_16x16x32_bf16 a[192:195], v[160:163], v[232:235], a[192:195]\n"
    "v_exp_f32 v193, v193\n"
    "ds_read_b64_tr_b16 v[160:161], v253 offset:8256\n"
    "v_cvt_pk_bf16_f32 v224, v192, v193\n"
    "v_exp_f32 v194, v194\n"
    "v_mfma_f32_16x16x32_bf16 a[196:199], v[164:167], v[232:235], a[196:199]\n"
    "ds_read_b64_tr_b16 v[162:163], v253 offset:12384\n"
    "v_add_f32 v240, v240, v193\n"
    "v_mfma_f32_16x16x32_bf16 a[200:203], v[168:171], v[232:235], a[200:203]\n"
    "v_exp_f32 v195, v195\n"
    "ds_read_b64_tr_b16 v[164:165], v253 offset:8264\n"
    "v_cvt_pk_bf16_f32 v225, v194, v195\n"
    "v_exp_f32 v196, v196\n"
    "v_mfma_f32_16x16x32_bf16 a[204:207], v[172:175], v[232:235], a[204:207]\n"
    "s_waitcnt lgkmcnt(3)\n"
    "ds_read_b64_tr_b16 v[166:167], v253 offset:12392\n"
    "v_add_f32 v240, v240, v194\n"
    "v_mfma_f32_16x16x32_bf16 v[200:203], v[144:147], v[0:3], v[248:251]\n"
    "v_exp_f32 v197, v197\n"
    "v_mfma_f32_16x16x32_bf16 v[200:203], v[148:151], v[4:7], v[200:203]\n"
    "ds_read_b64_tr_b16 v[168:169], v253 offset:8512\n"
    "v_cvt_pk_bf16_f32 v228, v196, v197\n"
    "v_mfma_f32_16x16x32_bf16 v[200:203], v[152:155], v[8:11], v[200:203]\n"
    "v_exp_f32 v198, v198\n"
    "v_mfma_f32_16x16x32_bf16 v[200:203], v[156:159], v[12:15], v[200:203]\n"
    "ds_read_b64_tr_b16 v[170:171], v253 offset:12640\n"
    "v_add_f32 v240, v240, v195\n"
    "v_mfma_f32_16x16x32_bf16 v[204:207], v[156:159], v[28:31], v[248:251]\n"
    "v_exp_f32 v199, v199\n"
    "v_mfma_f32_16x16x32_bf16 v[204:207], v[152:155], v[24:27], v[204:207]\n"
    "ds_read_b64_tr_b16 v[172:173], v253 offset:8520\n"
    "v_cvt_pk_bf16_f32 v229, v198, v199\n"
    "v_mfma_f32_16x16x32_bf16 v[204:207], v[148:151], v[20:23], v[204:207]\n"
    "v_exp_f32 v200, v200\n"
    "v_mfma_f32_16x16x32_bf16 v[204:207], v[144:147], v[16:19], v[204:207]\n"
    "ds_read_b64_tr_b16 v[174:175], v253 offset:12648\n"
    "v_add_f32 v241, v241, v196\n"
    "v_mfma_f32_16x16x32_bf16 a[208:211], v[176:179], v[232:235], a[208:211]\n"
    "v_exp_f32 v201, v201\n"
    "ds_read_b64_tr_b16 v[176:177], v253 offset:8768\n"
    "v_cvt_pk_bf16_f32 v226, v200, v201\n"
    "v_exp_f32 v202, v202\n"
    "v_mfma_f32_16x16x32_bf16 a[212:215], v[180:183], v[232:235], a[212:215]\n"
    "ds_read_b64_tr_b16 v[178:179], v253 offset:12896\n"
    "v_add_f32 v241, v241, v197\n"
    "v_mfma_f32_16x16x32_bf16 a[216:219], v[184:187], v[232:235], a[216:219]\n"
    "v_exp_f32 v203, v203\n"
    "ds_read_b64_tr_b16 v[180:181], v253 offset:8776\n"
    "v_cvt_pk_bf16_f32 v227, v202, v203\n"
    "v_exp_f32 v204, v204\n"
    "v_mfma_f32_16x16x32_bf16 a[220:223], v[188:191], v[232:235], a[220:223]\n"
    "ds_read_b64_tr_b16 v[182:183], v253 offset:12904\n"
    "v_add_f32 v241, v241, v198\n"
    "v_mfma_f32_16x16x32_bf16 v[208:211], v[128:131], v[32:35], v[248:251]\n"
    "v_exp_f32 v205, v205\n"
    "v_mfma_f32_16x16x32_bf16 v[208:211], v[132:135], v[36:39], v[208:211]\n"
    "ds_read_b64_tr_b16 v[184:185], v253 offset:9024\n"
    "v_cvt_pk_bf16_f32 v230, v204, v205\n"
    "v_mfma_f32_16x16x32_bf16 v[208:211], v[136:139], v[40:43], v[208:211]\n"
    "v_exp_f32 v206, v206\n"
    "v_mfma_f32_16x16x32_bf16 v[208:211], v[140:143], v[44:47], v[208:211]\n"
    "ds_read_b64_tr_b16 v[186:187], v253 offset:13152\n"
    "v_add_f32 v241, v241, v199\n"
    "v_mfma_f32_16x16x32_bf16 v[212:215], v[140:143], v[60:63], v[248:251]\n"
    "v_exp_f32 v207, v207\n"
    "v_mfma_f32_16x16x32_bf16 v[212:215], v[136:139], v[56:59], v[212:215]\n"
    "ds_read_b64_tr_b16 v[188:189], v253 offset:9032\n"
    "v_cvt_pk_bf16_f32 v231, v206, v207\n"
    "v_mfma_f32_16x16x32_bf16 v[212:215], v[132:135], v[52:55], v[212:215]\n"
    "v_exp_f32 v208, v208\n"
    "v_mfma_f32_16x16x32_bf16 v[212:215], v[128:131], v[48:51], v[212:215]\n"
    "s_waitcnt lgkmcnt(13)\n"
    "ds_read_b64_tr_b16 v[190:191], v253 offset:13160\n"
    "v_add_f32 v240, v240, v200\n"
    "v_mfma_f32_16x16x32_bf16 a[0:3], v[160:163], v[224:227], a[0:3]\n"
    "v_exp_f32 v209, v209\n"
    "v_mfma_f32_16x16x32_bf16 a[32:35], v[160:163], v[228:231], a[32:35]\n"
    "s_waitcnt lgkmcnt(12)\n"
    "v_cvt_pk_bf16_f32 v232, v208, v209\n"
    "v_add_f32 v240, v240, v201\n"
    "v_mfma_f32_16x16x32_bf16 a[36:39], v[164:167], v[228:231], a[36:39]\n"
    "v_exp_f32 v210, v210\n"
    "v_mfma_f32_16x16x32_bf16 a[4:7], v[164:167], v[224:227], a[4:7]\n"
    "s_waitcnt lgkmcnt(10)\n"
    "v_add_f32 v240, v240, v202\n"
    "v_add_f32 v241, v241, v204\n"
    "v_mfma_f32_16x16x32_bf16 a[8:11], v[168:171], v[224:227], a[8:11]\n"
    "v_exp_f32 v211, v211\n"
    "v_mfma_f32_16x16x32_bf16 a[40:43], v[168:171], v[228:231], a[40:43]\n"
    "s_waitcnt lgkmcnt(8)\n"
    "v_cvt_pk_bf16_f32 v233, v210, v211\n"
    "v_add_f32 v240, v240, v203\n"
    "v_mfma_f32_16x16x32_bf16 a[44:47], v[172:175], v[228:231], a[44:47]\n"
    "s_add_u32 m0, s54, 41280\n"
    "v_exp_f32 v212, v212\n"
    "v_mfma_f32_16x16x32_bf16 a[12:15], v[172:175], v[224:227], a[12:15]\n"
    "buffer_load_dwordx4 v254, s[40:43], s53 offen nt lds\n"
    "v_mfma_f32_16x16x32_bf16 v[216:219], v[144:147], v[32:35], v[248:251]\n"
    "v_exp_f32 v213, v213\n"
    "v_mfma_f32_16x16x32_bf16 v[216:219], v[148:151], v[36:39], v[216:219]\n"
    "v_cvt_pk_bf16_f32 v236, v212, v213\n"
    "v_add_f32 v241, v241, v205\n"
    "v_mfma_f32_16x16x32_bf16 v[216:219], v[152:155], v[40:43], v[216:219]\n"
    "v_exp_f32 v214, v214\n"
    "v_mfma_f32_16x16x32_bf16 v[216:219], v[156:159], v[44:47], v[216:219]\n"
    "v_add_f32 v241, v241, v206\n"
    "v_add_f32 v242, v242, v208\n"
    "v_mfma_f32_16x16x32_bf16 v[220:223], v[156:159], v[60:63], v[248:251]\n"
    "v_exp_f32 v215, v215\n"
    "v_mfma_f32_16x16x32_bf16 v[220:223], v[152:155], v[56:59], v[220:223]\n"
    "v_cvt_pk_bf16_f32 v237, v214, v215\n"
    "v_add_f32 v241, v241, v207\n"
    "v_mfma_f32_16x16x32_bf16 v[220:223], v[148:151], v[52:55], v[220:223]\n"
    "v_exp_f32 v216, v216\n"
    "v_mfma_f32_16x16x32_bf16 v[220:223], v[144:147], v[48:51], v[220:223]\n"
    "s_waitcnt lgkmcnt(6)\n"
    "v_add_f32 v242, v242, v209\n"
    "v_add_f32 v243, v243, v212\n"
    "v_mfma_f32_16x16x32_bf16 a[16:19], v[176:179], v[224:227], a[16:19]\n"
    "v_exp_f32 v217, v217\n"
    "v_mfma_f32_16x16x32_bf16 a[48:51], v[176:179], v[228:231], a[48:51]\n"
    "s_waitcnt lgkmcnt(4)\n"
    "v_cvt_pk_bf16_f32 v234, v216, v217\n"
    "v_add_f32 v242, v242, v210\n"
    "v_mfma_f32_16x16x32_bf16 a[52:55], v[180:183], v[228:231], a[52:55]\n"
    "v_exp_f32 v218, v218\n"
    "v_mfma_f32_16x16x32_bf16 a[20:23], v[180:183], v[224:227], a[20:23]\n"
    "s_waitcnt lgkmcnt(2)\n"
    "buffer_load_dwordx4 v254, s[40:43], s53 offen offset:1024 nt lds\n"
    "v_mfma_f32_16x16x32_bf16 a[24:27], v[184:187], v[224:227], a[24:27]\n"
    "v_exp_f32 v219, v219\n"
    "v_mfma_f32_16x16x32_bf16 a[56:59], v[184:187], v[228:231], a[56:59]\n"
    "s_waitcnt lgkmcnt(0)\n"
    "v_cvt_pk_bf16_f32 v235, v218, v219\n"
    "v_add_f32 v242, v242, v211\n"
    "v_mfma_f32_16x16x32_bf16 a[60:63], v[188:191], v[228:231], a[60:63]\n"
    "v_exp_f32 v220, v220\n"
    "v_mfma_f32_16x16x32_bf16 a[28:31], v[188:191], v[224:227], a[28:31]\n"
    "v_add_f32 v243, v243, v213\n"
    "v_add_f32 v242, v242, v216\n"
    "v_mfma_f32_16x16x32_bf16 v[192:195], v[128:131], v[64:67], v[248:251]\n"
    "v_exp_f32 v221, v221\n"
    "v_mfma_f32_16x16x32_bf16 v[192:195], v[132:135], v[68:71], v[192:195]\n"
    "v_cvt_pk_bf16_f32 v238, v220, v221\n"
    "v_add_f32 v243, v243, v214\n"
    "v_mfma_f32_16x16x32_bf16 v[192:195], v[136:139], v[72:75], v[192:195]\n"
    "v_exp_f32 v222, v222\n"
    "v_mfma_f32_16x16x32_bf16 v[192:195], v[140:143], v[76:79], v[192:195]\n"
    "v_add_f32 v243, v243, v215\n"
    "v_add_f32 v242, v242, v217\n"
    "v_mfma_f32_16x16x32_bf16 v[196:199], v[140:143], v[92:95], v[248:251]\n"
    "v_exp_f32 v223, v223\n"
    "v_mfma_f32_16x16x32_bf16 v[196:199], v[136:139], v[88:91], v[196:199]\n"
    "v_cvt_pk_bf16_f32 v239, v222, v223\n"
    "v_add_f32 v242, v242, v218\n"
    "v_mfma_f32_16x16x32_bf16 v[196:199], v[132:135], v[84:87], v[196:199]\n"
    "v_exp_f32 v192, v192\n"
    "v_mfma_f32_16x16x32_bf16 v[196:199], v[128:131], v[80:83], v[196:199]\n"
    "v_add_f32 v242, v242, v219\n"
    "v_add_f32 v243, v243, v220\n"
    "v_mfma_f32_16x16x32_bf16 a[64:67], v[160:163], v[232:235], a[64:67]\n"
    "v_exp_f32 v193, v193\n"
    "v_mfma_f32_16x16x32_bf16 a[96:99], v[160:163], v[236:239], a[96:99]\n"
    "v_cvt_pk_bf16_f32 v224, v192, v193\n"
    "v_add_f32 v243, v243, v221\n"
    "v_mfma_f32_16x16x32_bf16 a[100:103], v[164:167], v[236:239], a[100:103]\n"
    "s_add_u32 m0, s55, 99072\n"
    "v_exp_f32 v194, v194\n"
    "v_mfma_f32_16x16x32_bf16 a[68:71], v[164:167], v[232:235], a[68:71]\n"
    "buffer_load_dwordx4 v254, s[44:47], s53 offen nt lds\n"
    "v_mfma_f32_16x16x32_bf16 a[72:75], v[168:171], v[232:235], a[72:75]\n"
    "v_exp_f32 v195, v195\n"
    "v_mfma_f32_16x16x32_bf16 a[104:107], v[168:171], v[236:239], a[104:107]\n"
    "v_cvt_pk_bf16_f32 v225, v194, v195\n"
    "v_add_f32 v243, v243, v222\n"
    "v_mfma_f32_16x16x32_bf16 a[108:111], v[172:175], v[236:239], a[108:111]\n"
    "v_exp_f32 v196, v196\n"
    "v_mfma_f32_16x16x32_bf16 a[76:79], v[172:175], v[232:235], a[76:79]\n"
    "v_add_f32 v243, v243, v223\n"
    "v_add_f32 v244, v244, v192\n"
    "v_mfma_f32_16x16x32_bf16 v[200:203], v[144:147], v[64:67], v[248:251]\n"
    "v_exp_f32 v197, v197\n"
    "v_mfma_f32_16x16x32_bf16 v[200:203], v[148:151], v[68:71], v[200:203]\n"
    "v_cvt_pk_bf16_f32 v228, v196, v197\n"
    "v_add_f32 v244, v244, v193\n"
    "v_mfma_f32_16x16x32_bf16 v[200:203], v[152:155], v[72:75], v[200:203]\n"
    "v_exp_f32 v198, v198\n"
    "v_mfma_f32_16x16x32_bf16 v[200:203], v[156:159], v[76:79], v[200:203]\n"
    "v_add_f32 v244, v244, v194\n"
    "v_add_f32 v245, v245, v196\n"
    "v_mfma_f32_16x16x32_bf16 v[204:207], v[156:159], v[92:95], v[248:251]\n"
    "v_exp_f32 v199, v199\n"
    "v_mfma_f32_16x16x32_bf16 v[204:207], v[152:155], v[88:91], v[204:207]\n"
    "v_cvt_pk_bf16_f32 v229, v198, v199\n"
    "v_add_f32 v244, v244, v195\n"
    "v_mfma_f32_16x16x32_bf16 v[204:207], v[148:151], v[84:87], v[204:207]\n"
    "v_exp_f32 v200, v200\n"
    "v_mfma_f32_16x16x32_bf16 v[204:207], v[144:147], v[80:83], v[204:207]\n"
    "v_add_f32 v245, v245, v197\n"
    "v_add_f32 v244, v244, v200\n"
    "v_mfma_f32_16x16x32_bf16 a[80:83], v[176:179], v[232:235], a[80:83]\n"
    "v_exp_f32 v201, v201\n"
    "v_mfma_f32_16x16x32_bf16 a[112:115], v[176:179], v[236:239], a[112:115]\n"
    "v_cvt_pk_bf16_f32 v226, v200, v201\n"
    "v_add_f32 v245, v245, v198\n"
    "v_mfma_f32_16x16x32_bf16 a[116:119], v[180:183], v[236:239], a[116:119]\n"
    "s_add_u32 m0, s55, 99080\n"
    "v_exp_f32 v202, v202\n"
    "v_mfma_f32_16x16x32_bf16 a[84:87], v[180:183], v[232:235], a[84:87]\n"
    "buffer_load_dwordx4 v254, s[44:47], s53 offen offset:1024 nt lds\n"
    "v_mfma_f32_16x16x32_bf16 a[88:91], v[184:187], v[232:235], a[88:91]\n"
    "s_add_u32 s53, s53, 8192\n"
    "v_exp_f32 v203, v203\n"
    "v_mfma_f32_16x16x32_bf16 a[120:123], v[184:187], v[236:239], a[120:123]\n"
    "v_cvt_pk_bf16_f32 v227, v202, v203\n"
    "v_add_f32 v245, v245, v199\n"
    "v_mfma_f32_16x16x32_bf16 a[124:127], v[188:191], v[236:239], a[124:127]\n"
    "v_exp_f32 v204, v204\n"
    "v_mfma_f32_16x16x32_bf16 a[92:95], v[188:191], v[232:235], a[92:95]\n"
    "v_add_f32 v244, v244, v201\n"
    "v_add_f32 v245, v245, v204\n"
    "v_mfma_f32_16x16x32_bf16 v[208:211], v[128:131], v[96:99], v[248:251]\n"
    "v_exp_f32 v205, v205\n"
    "v_mfma_f32_16x16x32_bf16 v[208:211], v[132:135], v[100:103], v[208:211]\n"
    "v_cvt_pk_bf16_f32 v230, v204, v205\n"
    "v_add_f32 v244, v244, v202\n"
    "v_mfma_f32_16x16x32_bf16 v[208:211], v[136:139], v[104:107], v[208:211]\n"
    "v_exp_f32 v206, v206\n"
    "v_mfma_f32_16x16x32_bf16 v[208:211], v[140:143], v[108:111], v[208:211]\n"
    "v_add_f32 v244, v244, v203\n"
    "v_add_f32 v245, v245, v205\n"
    "v_exp_f32 v207, v207\n"
    "s_nop 0\n"
    "v_cvt_pk_bf16_f32 v231, v206, v207\n"
    "v_add_f32 v245, v245, v206\n"
    "s_nop 1\n"
    "v_exp_f32 v208, v208\n"
    "ds_read_b128 v[140:143], v252 offset:17280\n"
    "v_add_f32 v245, v245, v207\n"
    "v_mfma_f32_16x16x32_bf16 a[128:131], v[160:163], v[224:227], a[128:131]\n"
    "v_exp_f32 v209, v209\n"
    "v_mfma_f32_16x16x32_bf16 a[160:163], v[160:163], v[228:231], a[160:163]\n"
    "ds_read_b128 v[136:139], v252 offset:17024\n"
    "v_cvt_pk_bf16_f32 v232, v208, v209\n"
    "v_mfma_f32_16x16x32_bf16 a[164:167], v[164:167], v[228:231], a[164:167]\n"
    "v_exp_f32 v210, v210\n"
    "v_mfma_f32_16x16x32_bf16 a[132:135], v[164:167], v[224:227], a[132:135]\n"
    "ds_read_b128 v[132:135], v252 offset:16768\n"
    "v_add_f32 v246, v246, v208\n"
    "v_mfma_f32_16x16x32_bf16 a[136:139], v[168:171], v[224:227], a[136:139]\n"
    "v_exp_f32 v211, v211\n"
    "v_mfma_f32_16x16x32_bf16 a[168:171], v[168:171], v[228:231], a[168:171]\n"
    "ds_read_b128 v[128:131], v252 offset:16512\n"
    "v_cvt_pk_bf16_f32 v233, v210, v211\n"
    "v_mfma_f32_16x16x32_bf16 a[172:175], v[172:175], v[228:231], a[172:175]\n"
    "v_mfma_f32_16x16x32_bf16 a[140:143], v[172:175], v[224:227], a[140:143]\n"
    "v_add_f32 v246, v246, v209\n"
    "v_mfma_f32_16x16x32_bf16 v[216:219], v[144:147], v[96:99], v[248:251]\n"
    "v_mfma_f32_16x16x32_bf16 v[216:219], v[148:151], v[100:103], v[216:219]\n"
    "v_add_f32 v246, v246, v210\n"
    "v_mfma_f32_16x16x32_bf16 v[216:219], v[152:155], v[104:107], v[216:219]\n"
    "v_mfma_f32_16x16x32_bf16 v[216:219], v[156:159], v[108:111], v[216:219]\n"
    "v_add_f32 v246, v246, v211\n"
    "s_nop 6\n"
    "v_exp_f32 v216, v216\n"
    "ds_read_b128 v[156:159], v252 offset:21408\n"
    "v_mfma_f32_16x16x32_bf16 a[144:147], v[176:179], v[224:227], a[144:147]\n"
    "v_exp_f32 v217, v217\n"
    "v_mfma_f32_16x16x32_bf16 a[176:179], v[176:179], v[228:231], a[176:179]\n"
    "ds_read_b128 v[152:155], v252 offset:21152\n"
    "v_cvt_pk_bf16_f32 v234, v216, v217\n"
    "v_mfma_f32_16x16x32_bf16 a[180:183], v[180:183], v[228:231], a[180:183]\n"
    "v_exp_f32 v218, v218\n"
    "v_mfma_f32_16x16x32_bf16 a[148:151], v[180:183], v[224:227], a[148:151]\n"
    "ds_read_b128 v[148:151], v252 offset:20896\n"
    "v_add_f32 v246, v246, v216\n"
    "v_mfma_f32_16x16x32_bf16 a[152:155], v[184:187], v[224:227], a[152:155]\n"
    "v_exp_f32 v219, v219\n"
    "v_mfma_f32_16x16x32_bf16 a[184:187], v[184:187], v[228:231], a[184:187]\n"
    "ds_read_b128 v[144:147], v252 offset:20640\n"
    "v_cvt_pk_bf16_f32 v235, v218, v219\n"
    "v_mfma_f32_16x16x32_bf16 a[188:191], v[188:191], v[228:231], a[188:191]\n"
    "v_mfma_f32_16x16x32_bf16 a[156:159], v[188:191], v[224:227], a[156:159]\n"
    "s_waitcnt lgkmcnt(4)\n"
    "v_add_f32 v246, v246, v217\n"
    "v_mfma_f32_16x16x32_bf16 v[192:195], v[128:131], v[0:3], v[248:251]\n"
    "v_mfma_f32_16x16x32_bf16 v[192:195], v[132:135], v[4:7], v[192:195]\n"
    "v_add_f32 v246, v246, v218\n"
    "v_mfma_f32_16x16x32_bf16 v[192:195], v[136:139], v[8:11], v[192:195]\n"
    "v_mfma_f32_16x16x32_bf16 v[192:195], v[140:143], v[12:15], v[192:195]\n"
    "v_add_f32 v246, v246, v219\n"
    "v_mfma_f32_16x16x32_bf16 v[196:199], v[140:143], v[28:31], v[248:251]\n"
    "v_mfma_f32_16x16x32_bf16 v[196:199], v[136:139], v[24:27], v[196:199]\n"
    "v_mfma_f32_16x16x32_bf16 v[196:199], v[132:135], v[20:23], v[196:199]\n"
    "s_nop 3\n"
    "v_exp_f32 v192, v192\n"
    "v_mfma_f32_16x16x32_bf16 v[196:199], v[128:131], v[16:19], v[196:199]\n"
    "v_add_f32 v240, v240, v192\n"
    "v_mfma_f32_16x16x32_bf16 a[192:195], v[160:163], v[232:235], a[192:195]\n"
    "v_exp_f32 v193, v193\n"
    "ds_read_b64_tr_b16 v[160:161], v253 offset:16512\n"
    "v_cvt_pk_bf16_f32 v224, v192, v193\n"
    "v_exp_f32 v194, v194\n"
    "v_mfma_f32_16x16x32_bf16 a[196:199], v[164:167], v[232:235], a[196:199]\n"
    "ds_read_b64_tr_b16 v[162:163], v253 offset:20640\n"
    "v_add_f32 v240, v240, v193\n"
    "v_mfma_f32_16x16x32_bf16 a[200:203], v[168:171], v[232:235], a[200:203]\n"
    "v_exp_f32 v195, v195\n"
    "ds_read_b64_tr_b16 v[164:165], v253 offset:16520\n"
    "v_cvt_pk_bf16_f32 v225, v194, v195\n"
    "v_exp_f32 v196, v196\n"
    "v_mfma_f32_16x16x32_bf16 a[204:207], v[172:175], v[232:235], a[204:207]\n"
    "s_waitcnt lgkmcnt(3)\n"
    "ds_read_b64_tr_b16 v[166:167], v253 offset:20648\n"
    "v_add_f32 v240, v240, v194\n"
    "v_mfma_f32_16x16x32_bf16 v[200:203], v[144:147], v[0:3], v[248:251]\n"
    "v_exp_f32 v197, v197\n"
    "v_mfma_f32_16x16x32_bf16 v[200:203], v[148:151], v[4:7], v[200:203]\n"
    "ds_read_b64_tr_b16 v[168:169], v253 offset:16768\n"
    "v_cvt_pk_bf16_f32 v228, v196, v197\n"
    "v_mfma_f32_16x16x32_bf16 v[200:203], v[152:155], v[8:11], v[200:203]\n"
    "v_exp_f32 v198, v198\n"
    "v_mfma_f32_16x16x32_bf16 v[200:203], v[156:159], v[12:15], v[200:203]\n"
    "ds_read_b64_tr_b16 v[170:171], v253 offset:20896\n"
    "v_add_f32 v240, v240, v195\n"
    "v_mfma_f32_16x16x32_bf16 v[204:207], v[156:159], v[28:31], v[248:251]\n"
    "v_exp_f32 v199, v199\n"
    "v_mfma_f32_16x16x32_bf16 v[204:207], v[152:155], v[24:27], v[204:207]\n"
    "ds_read_b64_tr_b16 v[172:173], v253 offset:16776\n"
    "v_cvt_pk_bf16_f32 v229, v198, v199\n"
    "v_mfma_f32_16x16x32_bf16 v[204:207], v[148:151], v[20:23], v[204:207]\n"
    "v_exp_f32 v200, v200\n"
    "v_mfma_f32_16x16x32_bf16 v[204:207], v[144:147], v[16:19], v[204:207]\n"
    "ds_read_b64_tr_b16 v[174:175], v253 offset:20904\n"
    "v_add_f32 v241, v241, v196\n"
    "v_mfma_f32_16x16x32_bf16 a[208:211], v[176:179], v[232:235], a[208:211]\n"
    "v_exp_f32 v201, v201\n"
    "ds_read_b64_tr_b16 v[176:177], v253 offset:17024\n"
    "v_cvt_pk_bf16_f32 v226, v200, v201\n"
    "v_exp_f32 v202, v202\n"
    "v_mfma_f32_16x16x32_bf16 a[212:215], v[180:183], v[232:235], a[212:215]\n"
    "ds_read_b64_tr_b16 v[178:179], v253 offset:21152\n"
    "v_add_f32 v241, v241, v197\n"
    "v_mfma_f32_16x16x32_bf16 a[216:219], v[184:187], v[232:235], a[216:219]\n"
    "v_exp_f32 v203, v203\n"
    "ds_read_b64_tr_b16 v[180:181], v253 offset:17032\n"
    "v_cvt_pk_bf16_f32 v227, v202, v203\n"
    "v_exp_f32 v204, v204\n"
    "v_mfma_f32_16x16x32_bf16 a[220:223], v[188:191], v[232:235], a[220:223]\n"
    "ds_read_b64_tr_b16 v[182:183], v253 offset:21160\n"
    "v_add_f32 v241, v241, v198\n"
    "v_mfma_f32_16x16x32_bf16 v[208:211], v[128:131], v[32:35], v[248:251]\n"
    "v_exp_f32 v205, v205\n"
    "v_mfma_f32_16x16x32_bf16 v[208:211], v[132:135], v[36:39], v[208:211]\n"
    "ds_read_b64_tr_b16 v[184:185], v253 offset:17280\n"
    "v_cvt_pk_bf16_f32 v230, v204, v205\n"
    "v_mfma_f32_16x16x32_bf16 v[208:211], v[136:139], v[40:43], v[208:211]\n"
    "v_exp_f32 v206, v206\n"
    "v_mfma_f32_16x16x32_bf16 v[208:211], v[140:143], v[44:47], v[208:211]\n"
    "ds_read_b64_tr_b16 v[186:187], v253 offset:21408\n"
    "v_add_f32 v241, v241, v199\n"
    "v_mfma_f32_16x16x32_bf16 v[212:215], v[140:143], v[60:63], v[248:251]\n"
    "v_exp_f32 v207, v207\n"
    "v_mfma_f32_16x16x32_bf16 v[212:215], v[136:139], v[56:59], v[212:215]\n"
    "ds_read_b64_tr_b16 v[188:189], v253 offset:17288\n"
    "v_cvt_pk_bf16_f32 v231, v206, v207\n"
    "v_mfma_f32_16x16x32_bf16 v[212:215], v[132:135], v[52:55], v[212:215]\n"
    "v_exp_f32 v208, v208\n"
    "v_mfma_f32_16x16x32_bf16 v[212:215], v[128:131], v[48:51], v[212:215]\n"
    "s_waitcnt lgkmcnt(13)\n"
    "ds_read_b64_tr_b16 v[190:191], v253 offset:21416\n"
    "v_add_f32 v240, v240, v200\n"
    "v_mfma_f32_16x16x32_bf16 a[0:3], v[160:163], v[224:227], a[0:3]\n"
    "v_exp_f32 v209, v209\n"
    "v_mfma_f32_16x16x32_bf16 a[32:35], v[160:163], v[228:231], a[32:35]\n"
    "s_waitcnt lgkmcnt(12)\n"
    "v_cvt_pk_bf16_f32 v232, v208, v209\n"
    "v_add_f32 v240, v240, v201\n"
    "v_mfma_f32_16x16x32_bf16 a[36:39], v[164:167], v[228:231], a[36:39]\n"
    "v_exp_f32 v210, v210\n"
    "v_mfma_f32_16x16x32_bf16 a[4:7], v[164:167], v[224:227], a[4:7]\n"
    "s_waitcnt lgkmcnt(10)\n"
    "v_add_f32 v240, v240, v202\n"
    "v_add_f32 v241, v241, v204\n"
    "v_mfma_f32_16x16x32_bf16 a[8:11], v[168:171], v[224:227], a[8:11]\n"
    "v_exp_f32 v211, v211\n"
    "v_mfma_f32_16x16x32_bf16 a[40:43], v[168:171], v[228:231], a[40:43]\n"
    "s_waitcnt lgkmcnt(8)\n"
    "v_cvt_pk_bf16_f32 v233, v210, v211\n"
    "v_add_f32 v240, v240, v203\n"
    "v_mfma_f32_16x16x32_bf16 a[44:47], v[172:175], v[228:231], a[44:47]\n"
    "s_add_u32 m0, s54, 0\n"
    "v_exp_f32 v212, v212\n"
    "v_mfma_f32_16x16x32_bf16 a[12:15], v[172:175], v[224:227], a[12:15]\n"
    "buffer_load_dwordx4 v254, s[40:43], s53 offen nt lds\n"
    "v_mfma_f32_16x16x32_bf16 v[216:219], v[144:147], v[32:35], v[248:251]\n"
    "v_exp_f32 v213, v213\n"
    "v_mfma_f32_16x16x32_bf16 v[216:219], v[148:151], v[36:39], v[216:219]\n"
    "v_cvt_pk_bf16_f32 v236, v212, v213\n"
    "v_add_f32 v241, v241, v205\n"
    "v_mfma_f32_16x16x32_bf16 v[216:219], v[152:155], v[40:43], v[216:219]\n"
    "v_exp_f32 v214, v214\n"
    "v_mfma_f32_16x16x32_bf16 v[216:219], v[156:159], v[44:47], v[216:219]\n"
    "v_add_f32 v241, v241, v206\n"
    "v_add_f32 v242, v242, v208\n"
    "v_mfma_f32_16x16x32_bf16 v[220:223], v[156:159], v[60:63], v[248:251]\n"
    "v_exp_f32 v215, v215\n"
    "v_mfma_f32_16x16x32_bf16 v[220:223], v[152:155], v[56:59], v[220:223]\n"
    "v_cvt_pk_bf16_f32 v237, v214, v215\n"
    "v_add_f32 v241, v241, v207\n"
    "v_mfma_f32_16x16x32_bf16 v[220:223], v[148:151], v[52:55], v[220:223]\n"
    "v_exp_f32 v216, v216\n"
    "v_mfma_f32_16x16x32_bf16 v[220:223], v[144:147], v[48:51], v[220:223]\n"
    "s_waitcnt lgkmcnt(6)\n"
    "v_add_f32 v242, v242, v209\n"
    "v_add_f32 v243, v243, v212\n"
    "v_mfma_f32_16x16x32_bf16 a[16:19], v[176:179], v[224:227], a[16:19]\n"
    "v_exp_f32 v217, v217\n"
    "v_mfma_f32_16x16x32_bf16 a[48:51], v[176:179], v[228:231], a[48:51]\n"
    "s_waitcnt lgkmcnt(4)\n"
    "v_cvt_pk_bf16_f32 v234, v216, v217\n"
    "v_add_f32 v242, v242, v210\n"
    "v_mfma_f32_16x16x32_bf16 a[52:55], v[180:183], v[228:231], a[52:55]\n"
    "v_exp_f32 v218, v218\n"
    "v_mfma_f32_16x16x32_bf16 a[20:23], v[180:183], v[224:227], a[20:23]\n"
    "s_waitcnt lgkmcnt(2)\n"
    "buffer_load_dwordx4 v254, s[40:43], s53 offen offset:1024 nt lds\n"
    "v_mfma_f32_16x16x32_bf16 a[24:27], v[184:187], v[224:227], a[24:27]\n"
    "v_exp_f32 v219, v219\n"
    "v_mfma_f32_16x16x32_bf16 a[56:59], v[184:187], v[228:231], a[56:59]\n"
    "s_waitcnt lgkmcnt(0)\n"
    "v_cvt_pk_bf16_f32 v235, v218, v219\n"
    "v_add_f32 v242, v242, v211\n"
    "v_mfma_f32_16x16x32_bf16 a[60:63], v[188:191], v[228:231], a[60:63]\n"
    "v_exp_f32 v220, v220\n"
    "v_mfma_f32_16x16x32_bf16 a[28:31], v[188:191], v[224:227], a[28:31]\n"
    "v_add_f32 v243, v243, v213\n"
    "v_add_f32 v242, v242, v216\n"
    "v_mfma_f32_16x16x32_bf16 v[192:195], v[128:131], v[64:67], v[248:251]\n"
    "v_exp_f32 v221, v221\n"
    "v_mfma_f32_16x16x32_bf16 v[192:195], v[132:135], v[68:71], v[192:195]\n"
    "v_cvt_pk_bf16_f32 v238, v220, v221\n"
    "v_add_f32 v243, v243, v214\n"
    "v_mfma_f32_16x16x32_bf16 v[192:195], v[136:139], v[72:75], v[192:195]\n"
    "v_exp_f32 v222, v222\n"
    "v_mfma_f32_16x16x32_bf16 v[192:195], v[140:143], v[76:79], v[192:195]\n"
    "v_add_f32 v243, v243, v215\n"
    "v_add_f32 v242, v242, v217\n"
    "v_mfma_f32_16x16x32_bf16 v[196:199], v[140:143], v[92:95], v[248:251]\n"
    "v_exp_f32 v223, v223\n"
    "v_mfma_f32_16x16x32_bf16 v[196:199], v[136:139], v[88:91], v[196:199]\n"
    "v_cvt_pk_bf16_f32 v239, v222, v223\n"
    "v_add_f32 v242, v242, v218\n"
    "v_mfma_f32_16x16x32_bf16 v[196:199], v[132:135], v[84:87], v[196:199]\n"
    "v_exp_f32 v192, v192\n"
    "v_mfma_f32_16x16x32_bf16 v[196:199], v[128:131], v[80:83], v[196:199]\n"
    "v_add_f32 v242, v242, v219\n"
    "v_add_f32 v243, v243, v220\n"
    "v_mfma_f32_16x16x32_bf16 a[64:67], v[160:163], v[232:235], a[64:67]\n"
    "v_exp_f32 v193, v193\n"
    "v_mfma_f32_16x16x32_bf16 a[96:99], v[160:163], v[236:239], a[96:99]\n"
    "v_cvt_pk_bf16_f32 v224, v192, v193\n"
    "v_add_f32 v243, v243, v221\n"
    "v_mfma_f32_16x16x32_bf16 a[100:103], v[164:167], v[236:239], a[100:103]\n"
    "s_add_u32 m0, s55, 57792\n"
    "v_exp_f32 v194, v194\n"
    "v_mfma_f32_16x16x32_bf16 a[68:71], v[164:167], v[232:235], a[68:71]\n"
    "buffer_load_dwordx4 v254, s[44:47], s53 offen nt lds\n"
    "v_mfma_f32_16x16x32_bf16 a[72:75], v[168:171], v[232:235], a[72:75]\n"
    "v_exp_f32 v195, v195\n"
    "v_mfma_f32_16x16x32_bf16 a[104:107], v[168:171], v[236:239], a[104:107]\n"
    "v_cvt_pk_bf16_f32 v225, v194, v195\n"
    "v_add_f32 v243, v243, v222\n"
    "v_mfma_f32_16x16x32_bf16 a[108:111], v[172:175], v[236:239], a[108:111]\n"
    "v_exp_f32 v196, v196\n"
    "v_mfma_f32_16x16x32_bf16 a[76:79], v[172:175], v[232:235], a[76:79]\n"
    "v_add_f32 v243, v243, v223\n"
    "v_add_f32 v244, v244, v192\n"
    "v_mfma_f32_16x16x32_bf16 v[200:203], v[144:147], v[64:67], v[248:251]\n"
    "v_exp_f32 v197, v197\n"
    "v_mfma_f32_16x16x32_bf16 v[200:203], v[148:151], v[68:71], v[200:203]\n"
    "v_cvt_pk_bf16_f32 v228, v196, v197\n"
    "v_add_f32 v244, v244, v193\n"
    "v_mfma_f32_16x16x32_bf16 v[200:203], v[152:155], v[72:75], v[200:203]\n"
    "v_exp_f32 v198, v198\n"
    "v_mfma_f32_16x16x32_bf16 v[200:203], v[156:159], v[76:79], v[200:203]\n"
    "v_add_f32 v244, v244, v194\n"
    "v_add_f32 v245, v245, v196\n"
    "v_mfma_f32_16x16x32_bf16 v[204:207], v[156:159], v[92:95], v[248:251]\n"
    "v_exp_f32 v199, v199\n"
    "v_mfma_f32_16x16x32_bf16 v[204:207], v[152:155], v[88:91], v[204:207]\n"
    "v_cvt_pk_bf16_f32 v229, v198, v199\n"
    "v_add_f32 v244, v244, v195\n"
    "v_mfma_f32_16x16x32_bf16 v[204:207], v[148:151], v[84:87], v[204:207]\n"
    "v_exp_f32 v200, v200\n"
    "v_mfma_f32_16x16x32_bf16 v[204:207], v[144:147], v[80:83], v[204:207]\n"
    "v_add_f32 v245, v245, v197\n"
    "v_add_f32 v244, v244, v200\n"
    "v_mfma_f32_16x16x32_bf16 a[80:83], v[176:179], v[232:235], a[80:83]\n"
    "v_exp_f32 v201, v201\n"
    "v_mfma_f32_16x16x32_bf16 a[112:115], v[176:179], v[236:239], a[112:115]\n"
    "v_cvt_pk_bf16_f32 v226, v200, v201\n"
    "v_add_f32 v245, v245, v198\n"
    "v_mfma_f32_16x16x32_bf16 a[116:119], v[180:183], v[236:239], a[116:119]\n"
    "s_add_u32 m0, s55, 57800\n"
    "v_exp_f32 v202, v202\n"
    "v_mfma_f32_16x16x32_bf16 a[84:87], v[180:183], v[232:235], a[84:87]\n"
    "buffer_load_dwordx4 v254, s[44:47], s53 offen offset:1024 nt lds\n"
    "v_mfma_f32_16x16x32_bf16 a[88:91], v[184:187], v[232:235], a[88:91]\n"
    "s_add_u32 s53, s53, 8192\n"
    "v_exp_f32 v203, v203\n"
    "v_mfma_f32_16x16x32_bf16 a[120:123], v[184:187], v[236:239], a[120:123]\n"
    "v_cvt_pk_bf16_f32 v227, v202, v203\n"
    "v_add_f32 v245, v245, v199\n"
    "v_mfma_f32_16x16x32_bf16 a[124:127], v[188:191], v[236:239], a[124:127]\n"
    "v_exp_f32 v204, v204\n"
    "v_mfma_f32_16x16x32_bf16 a[92:95], v[188:191], v[232:235], a[92:95]\n"
    "v_add_f32 v244, v244, v201\n"
    "v_add_f32 v245, v245, v204\n"
    "v_mfma_f32_16x16x32_bf16 v[208:211], v[128:131], v[96:99], v[248:251]\n"
    "v_exp_f32 v205, v205\n"
    "v_mfma_f32_16x16x32_bf16 v[208:211], v[132:135], v[100:103], v[208:211]\n"
    "v_cvt_pk_bf16_f32 v230, v204, v205\n"
    "v_add_f32 v244, v244, v202\n"
    "v_mfma_f32_16x16x32_bf16 v[208:211], v[136:139], v[104:107], v[208:211]\n"
    "s_waitcnt vmcnt(8)\n"
    "v_exp_f32 v206, v206\n"
    "v_mfma_f32_16x16x32_bf16 v[208:211], v[140:143], v[108:111], v[208:211]\n"
    "s_barrier\n"
    "v_add_f32 v244, v244, v203\n"
    "v_add_f32 v245, v245, v205\n"
    "v_exp_f32 v207, v207\n"
    "s_nop 0\n"
    "v_cvt_pk_bf16_f32 v231, v206, v207\n"
    "v_add_f32 v245, v245, v206\n"
    "s_nop 0\n"
    "v_exp_f32 v208, v208\n"
    "ds_read_b128 v[140:143], v252 offset:25536\n"
    "v_add_f32 v245, v245, v207\n"
    "v_mfma_f32_16x16x32_bf16 a[128:131], v[160:163], v[224:227], a[128:131]\n"
    "v_exp_f32 v209, v209\n"
    "v_mfma_f32_16x16x32_bf16 a[160:163], v[160:163], v[228:231], a[160:163]\n"
    "ds_read_b128 v[136:139], v252 offset:25280\n"
    "v_cvt_pk_bf16_f32 v232, v208, v209\n"
    "v_mfma_f32_16x16x32_bf16 a[164:167], v[164:167], v[228:231], a[164:167]\n"
    "v_exp_f32 v210, v210\n"
    "v_mfma_f32_16x16x32_bf16 a[132:135], v[164:167], v[224:227], a[132:135]\n"
    "ds_read_b128 v[132:135], v252 offset:25024\n"
    "v_add_f32 v246, v246, v208\n"
    "v_mfma_f32_16x16x32_bf16 a[136:139], v[168:171], v[224:227], a[136:139]\n"
    "v_exp_f32 v211, v211\n"
    "v_mfma_f32_16x16x32_bf16 a[168:171], v[168:171], v[228:231], a[168:171]\n"
    "ds_read_b128 v[128:131], v252 offset:24768\n"
    "v_cvt_pk_bf16_f32 v233, v210, v211\n"
    "v_mfma_f32_16x16x32_bf16 a[172:175], v[172:175], v[228:231], a[172:175]\n"
    "v_mfma_f32_16x16x32_bf16 a[140:143], v[172:175], v[224:227], a[140:143]\n"
    "v_add_f32 v246, v246, v209\n"
    "v_mfma_f32_16x16x32_bf16 v[216:219], v[144:147], v[96:99], v[248:251]\n"
    "v_mfma_f32_16x16x32_bf16 v[216:219], v[148:151], v[100:103], v[216:219]\n"
    "v_add_f32 v246, v246, v210\n"
    "v_mfma_f32_16x16x32_bf16 v[216:219], v[152:155], v[104:107], v[216:219]\n"
    "v_mfma_f32_16x16x32_bf16 v[216:219], v[156:159], v[108:111], v[216:219]\n"
    "v_add_f32 v246, v246, v211\n"
    "s_nop 6\n"
    "v_exp_f32 v216, v216\n"
    "ds_read_b128 v[156:159], v252 offset:29664\n"
    "v_mfma_f32_16x16x32_bf16 a[144:147], v[176:179], v[224:227], a[144:147]\n"
    "v_exp_f32 v217, v217\n"
    "v_mfma_f32_16x16x32_bf16 a[176:179], v[176:179], v[228:231], a[176:179]\n"
    "ds_read_b128 v[152:155], v252 offset:29408\n"
    "v_cvt_pk_bf16_f32 v234, v216, v217\n"
    "v_mfma_f32_16x16x32_bf16 a[180:183], v[180:183], v[228:231], a[180:183]\n"
    "v_exp_f32 v218, v218\n"
    "v_mfma_f32_16x16x32_bf16 a[148:151], v[180:183], v[224:227], a[148:151]\n"
    "ds_read_b128 v[148:151], v252 offset:29152\n"
    "v_add_f32 v246, v246, v216\n"
    "v_mfma_f32_16x16x32_bf16 a[152:155], v[184:187], v[224:227], a[152:155]\n"
    "v_exp_f32 v219, v219\n"
    "v_mfma_f32_16x16x32_bf16 a[184:187], v[184:187], v[228:231], a[184:187]\n"
    "ds_read_b128 v[144:147], v252 offset:28896\n"
    "v_cvt_pk_bf16_f32 v235, v218, v219\n"
    "v_mfma_f32_16x16x32_bf16 a[188:191], v[188:191], v[228:231], a[188:191]\n"
    "v_mfma_f32_16x16x32_bf16 a[156:159], v[188:191], v[224:227], a[156:159]\n"
    "s_branch L_loop3_%=\n"
    "L_x3_%=:\n"
    "s_waitcnt lgkmcnt(0)\n"
    "s_nop 7\n"
    "s_branch L_exit_%=\n"
    "L_end_%=:\n"
    : [flag] "=&s"(flag)
    : [ka] "s"(__builtin_amdgcn_kernarg_segment_ptr()), [w] "s"(w), [h] "s"(blockIdx.x)
    : "v0","v1","v2","v3","v4","v5","v6","v7","v8","v9","v10","v11","v12","v13","v14","v15","v16","v17","v18","v19","v20","v21","v22","v23","v24","v25","v26","v27","v28","v29","v30","v31","v32","v33","v34","v35","v36","v37","v38","v39","v40","v41","v42","v43","v44","v45","v46","v47","v48","v49","v50","v51","v52","v53","v54","v55","v56","v57","v58","v59","v60","v61","v62","v63","v64","v65","v66","v67","v68","v69","v70","v71","v72","v73","v74","v75","v76","v77","v78","v79","v80","v81","v82","v83","v84","v85","v86","v87","v88","v89","v90","v91","v92","v93","v94","v95","v96","v97","v98","v99","v100","v101","v102","v103","v104","v105","v106","v107","v108","v109","v110","v111","v112","v113","v114","v115","v116","v117","v118","v119","v120","v121","v122","v123","v124","v125","v126","v127","v128","v129","v130","v131","v132","v133","v134","v135","v136","v137","v138","v139","v140","v141","v142","v143","v144","v145","v146","v147","v148","v149","v150","v151","v152","v153","v154","v155","v156","v157","v158","v159","v160","v161","v162","v163","v164","v165","v166","v167","v168","v169","v170","v171","v172","v173","v174","v175","v176","v177","v178","v179","v180","v181","v182","v183","v184","v185","v186","v187","v188","v189","v190","v191","v192","v193","v194","v195","v196","v197","v198","v199","v200","v201","v202","v203","v204","v205","v206","v207","v208","v209","v210","v211","v212","v213","v214","v215","v216","v217","v218","v219","v220","v221","v222","v223","v224","v225","v226","v227","v228","v229","v230","v231","v232","v233","v234","v235","v236","v237","v238","v239","v240","v241","v242","v243","v244","v245","v246","v247","v248","v249","v250","v251","v252","v253","v254","v255","a0","a1","a2","a3","a4","a5","a6","a7","a8","a9","a10","a11","a12","a13","a14","a15","a16","a17","a18","a19","a20","a21","a22","a23","a24","a25","a26","a27","a28","a29","a30","a31","a32","a33","a34","a35","a36","a37","a38","a39","a40","a41","a42","a43","a44","a45","a46","a47","a48","a49","a50","a51","a52","a53","a54","a55","a56","a57","a58","a59","a60","a61","a62","a63","a64","a65","a66","a67","a68","a69","a70","a71","a72","a73","a74","a75","a76","a77","a78","a79","a80","a81","a82","a83","a84","a85","a86","a87","a88","a89","a90","a91","a92","a93","a94","a95","a96","a97","a98","a99","a100","a101","a102","a103","a104","a105","a106","a107","a108","a109","a110","a111","a112","a113","a114","a115","a116","a117","a118","a119","a120","a121","a122","a123","a124","a125","a126","a127","a128","a129","a130","a131","a132","a133","a134","a135","a136","a137","a138","a139","a140","a141","a142","a143","a144","a145","a146","a147","a148","a149","a150","a151","a152","a153","a154","a155","a156","a157","a158","a159","a160","a161","a162","a163","a164","a165","a166","a167","a168","a169","a170","a171","a172","a173","a174","a175","a176","a177","a178","a179","a180","a181","a182","a183","a184","a185","a186","a187","a188","a189","a190","a191","a192","a193","a194","a195","a196","a197","a198","a199","a200","a201","a202","a203","a204","a205","a206","a207","a208","a209","a210","a211","a212","a213","a214","a215","a216","a217","a218","a219","a220","a221","a222","a223","a224","a225","a226","a227","a228","a229","a230","a231","a232","a233","a234","a235","a236","a237","a238","a239","a240","a241","a242","a243","a244","a245","a246","a247","a248","a249","a250","a251","a252","a253","a254","a255","s36","s37","s38","s39","s40","s41","s42","s43","s44","s45","s46","s47","s48","s49","s50","s51","s52","s53","s54","s55","s56","s57","s58","s59","s60","s61","s62","s63","s64","s65","s66","s67","s68","s69","s70","s71","s72","s73","s74","s75","s76","s77","s78","s79","s80","s81","s82","s83","s84","s85","s86","s87","s88","s89","s90","s91","s92","s93","s94","s95","vcc","scc","memory");
  if (__builtin_expect(flag != 0, 0)) {
    asm volatile("s_waitcnt vmcnt(0)" ::: "memory");
    slow_rows(q, k, v, o, w);
  }
}

extern "C" void kernel_launch(void* const* d_in, const int* in_sizes, int n_in, void* d_out, int out_size, void* d_ws, size_t ws_size,
                              hipStream_t stream) {
  (void)hipFuncSetAttribute((const void*)mha_softmax_kernel, hipFuncAttributeMaxDynamicSharedMemorySize, LDS_BYTES);
  hipLaunchKernelGGL(mha_softmax_kernel, dim3(256), dim3(256), LDS_BYTES, stream, (const unsigned short*)d_in[0], (const unsigned short*)d_in[1],
                     (const unsigned short*)d_in[2], (unsigned short*)d_out);
}
```
